```python
import torch

def _softmax(x, axis=-1):
    return torch.softmax(x, dim=axis)

def _gelu(x):
    return torch.nn.functional.gelu(x, approximate='tanh')

def _silu(x):
    return torch.nn.functional.silu(x)

def _sigmoid(x):
    return torch.sigmoid(x)

def _layernorm(x, axis=-1, eps=1e-05):
    m = x.mean(dim=axis, keepdim=True)
    v = x.var(dim=axis, keepdim=True, unbiased=False)
    return (x - m) / torch.sqrt(v + eps)

def _scatter_add(tgt, idx, src, axis=0):
    return tgt.clone().index_add_(axis, idx, src)

def _segment_sum(src, seg, n):
    out = torch.zeros((n,) + src.shape[1:], dtype=src.dtype)
    return out.index_add_(0, seg, src)

def _pad(x, pw):
    flat = tuple((v for pair in reversed(pw) for v in pair))
    return torch.nn.functional.pad(x, flat)
kernel_name = 'interleave_layers__memory_aug_recurrent_mem_tr__dense_activation_silu__prompt'

def setup_inputs():

    _w = lambda a: (lambda t: t.to(torch.bfloat16) if t.is_floating_point() else t)(torch.as_tensor(a))

    def _A_setup():
        B, S, Dm = (1, 2048, 640)
        H, Dh = (32, 128)
        M, seg = (8, 32)
        seg = min(seg, S)
        S = S // seg * seg
        return {'x': _w((lambda *s: torch.randn(*s, dtype=torch.bfloat16) * 0.05)(B, S, Dm)), 'mem0': _w((lambda *s: torch.randn(*s, dtype=torch.bfloat16) * 0.4)(B, M, Dm)), 'Wq': _w((lambda *s: torch.randn(*s, dtype=torch.bfloat16) * 1.6)(Dm, H, Dh) * Dm ** (-0.5)), 'Wk': _w((lambda *s: torch.randn(*s, dtype=torch.bfloat16) * 1.6)(Dm, H, Dh) * Dm ** (-0.5)), 'Wv': _w((lambda *s: torch.randn(*s, dtype=torch.bfloat16) * 0.4)(Dm, H, Dh) * Dm ** (-0.5)), 'Wo': _w((lambda *s: torch.randn(*s, dtype=torch.bfloat16) * 0.4)(H, Dh, Dm) * (H * Dh) ** (-0.5)), 'seg_len': seg}
    _A_in = _A_setup()

    def _B_setup():
        B, S, D, F = (1, 2048, 640, 1728)
        return {'x': _w((lambda *s: torch.randn(*s, dtype=torch.bfloat16) * 0.05)(B, S, D)), 'w1': _w((lambda *s: torch.randn(*s, dtype=torch.bfloat16) * 1.6)(D, F) * D ** (-0.5)), 'w2': _w((lambda *s: torch.randn(*s, dtype=torch.bfloat16) * 0.2)(F, D) * F ** (-0.5))}
    _B_in = _B_setup()
    return {'A_x': _A_in['x'], 'A_mem0': _A_in['mem0'], 'A_Wq': _A_in['Wq'], 'A_Wk': _A_in['Wk'], 'A_Wv': _A_in['Wv'], 'A_Wo': _A_in['Wo'], 'A_seg_len': _A_in['seg_len'], 'B_x': _B_in['x'], 'B_w1': _B_in['w1'], 'B_w2': _B_in['w2']}

def reference(A_x, A_mem0, A_Wq, A_Wk, A_Wv, A_Wo, A_seg_len, B_x, B_w1, B_w2):

    def _A_ref(x, mem0, Wq, Wk, Wv, Wo, seg_len):
        B, S, Dm = x.shape
        H, Dh = (Wq.shape[1], Wq.shape[2])
        M = mem0.shape[1]
        scale = Dh ** (-0.5)
        n_seg = S // seg_len
        L = M + seg_len
        idx = torch.arange(L)
        mask = (idx[:, None] < M) | (idx[:, None] >= idx[None, :])
        mem = mem0
        outs = []
        for s in range(n_seg):
            xs = x[:, s * seg_len:(s + 1) * seg_len, :].clone()
            h = torch.concatenate([mem, xs], axis=1)
            q = torch.einsum('btd,dhe->bhte', h, Wq)
            k = torch.einsum('btd,dhe->bhte', h, Wk)
            v = torch.einsum('btd,dhe->bhte', h, Wv)
            scores = torch.einsum('bhqe,bhke->bhqk', q, k) * scale
            scores = torch.where(mask[None, None], scores, -1000000000.0)
            p = _softmax(scores, axis=-1)
            o = torch.einsum('bhqk,bhke->bhqe', p, v)
            o = torch.einsum('bhte,hed->btd', o, Wo)
            mem = o[:, :M, :].clone()
            outs.append(o[:, M:, :])
        return torch.concatenate(outs, axis=1)

    def _B_ref(x, w1, w2):
        h = torch.einsum('bsd,df->bsf', x, w1)
        h = _silu(h)
        out = torch.einsum('bsf,fd->bsd', h, w2)
        return out
    x = A_x
    x = x + _A_ref(_layernorm(x, axis=-1), A_mem0, A_Wq, A_Wk, A_Wv, A_Wo, A_seg_len)
    x = x + _B_ref(_layernorm(x, axis=-1), B_w1, B_w2)
    x = x + _A_ref(_layernorm(x, axis=-1), A_mem0, A_Wq, A_Wk, A_Wv, A_Wo, A_seg_len)
    x = x + _B_ref(_layernorm(x, axis=-1), B_w1, B_w2)
    return x
```

```cpp
#include <hip/hip_runtime.h>
#include <stdint.h>

typedef unsigned v4u __attribute__((ext_vector_type(4)));
typedef __amdgpu_buffer_rsrc_t rsrc_t;

struct Params { const void* x; const void* mem0; const void* Wq; const void* Wk; const void* Wv; const void* Wo; const void* w1; const void* w2; void* out; void* ws; };

constexpr unsigned WS_END = 149946368u, PART = 2097152u, MEMR = 3407872u, OOB = 0x40000000u;
constexpr int LAG = 7, F_INIT = 16;
constexpr unsigned FLAG_BYTES = 81920u;
constexpr int SC1 = 16;

__device__ __forceinline__ void st16(rsrc_t r, v4u v, unsigned off, unsigned so = 0) { __builtin_amdgcn_raw_buffer_store_b128(v, r, off, so, SC1); }
__device__ __forceinline__ void drain() { asm volatile("s_waitcnt vmcnt(0)" ::: "memory"); }
__device__ __forceinline__ void publish(rsrc_t r, unsigned off, unsigned val, int lane) {
    drain();
    __builtin_amdgcn_raw_buffer_store_b32(val, r, lane == 0 ? off : OOB, 0, SC1);
}
__device__ __forceinline__ unsigned long long clk() { unsigned long long t; asm volatile("s_memtime %0\n\ts_waitcnt lgkmcnt(0)" : "=s"(t) :: "memory"); return t; }

__device__ __forceinline__ void init_share(const Params& p, rsrc_t rw, int cu, int w, int lane) {
    unsigned m = 0;
    for (int spins = 0; m == 0 && spins < 400000; ++spins) {
        asm volatile("s_sleep 8" ::: "memory");
        m = __builtin_amdgcn_readfirstlane(__builtin_amdgcn_raw_buffer_load_b32(rw, 0, 0, SC1));
    }
    const unsigned half = (m & 32u) << 8;
    constexpr unsigned PER = 80 * 512, TOT = 4 * PER + 16 * 512;
    const v4u* m0 = (const v4u*)p.mem0;
    for (unsigned c = ((cu - (cu > 16)) * 4 + w) * 64 + lane; c < TOT; c += 124 * 64) {
        v4u v = {~0u, ~0u, ~0u, ~0u};
        unsigned off;
        if (c < 4 * PER) { const unsigned r = c / PER, in = c % PER; off = PART + r * 0x200000u + (in >> 9) * 16384u + (half ^ ((r & 1) << 13)) + (in & 511u) * 16; }
        else {
            const unsigned b = c - 4 * PER, slot = b >> 10, line = ((b >> 9) & 1) * 64 + ((b & 511u) >> 3);
            off = MEMR + (b >> 9) * 16384u + half + (b & 511u) * 16;
            if ((slot == 0 || slot == ((2 * LAG + 1) & 7)) && line < 80) {
                const unsigned k = b & 7, qd = (line >> 1) & 3, cg = (line ^ qd) & 1;
                v = m0[(2 * qd + (k >> 2)) * 80 + (line >> 3) * 8 + cg * 4 + (k & 3)];
                v.x = min(v.x, ~1u); v.y = min(v.y, ~1u); v.z = min(v.z, ~1u); v.w = min(v.w, ~1u);
            }
        }
        st16(rw, v, off);
    }
    publish(rw, (F_INIT + cu) * 128 + w * 4, 1u, lane);
}

__device__ __forceinline__ void probe(rsrc_t rw, int lane) {
    const unsigned a0 = PART + 0x1F0000u + (lane & 31) * 4;
    unsigned best0 = ~0u, best1 = ~0u;
    for (int r = 0; r < 4; ++r) {
        const unsigned long long t0 = clk();
        __builtin_amdgcn_raw_buffer_store_b32(r, rw, a0 + r * 256, 0, SC1);
        drain();
        const unsigned long long t1 = clk();
        __builtin_amdgcn_raw_buffer_store_b32(r, rw, a0 + 8192 + r * 256, 0, SC1);
        drain();
        const unsigned long long t2 = clk();
        best0 = min(best0, (unsigned)(t1 - t0));
        best1 = min(best1, (unsigned)(t2 - t1));
    }
    const unsigned m = 0x10u | ((best1 < best0) != true ? 32u : 32u - 32u);
    const v4u v = {m, m, m, m};
    st16(rw, v, lane < 2 ? lane * (F_INIT + 16) * 128 : OOB);
}

__global__ void __launch_bounds__(256, 1) mega(Params p) {
    __shared__ __attribute__((aligned(16))) char smem[163840];
    const int b = blockIdx.x, xc = b & 7, ix = b >> 3;
    const int w = __builtin_amdgcn_readfirstlane(threadIdx.x >> 6);
    int role, id;
    if (xc < 4) {
        if (ix < 16) { role = 0; id = xc * 16 + ix; }
        else if (ix < 20) { role = 4; id = xc * 4 + ix - 16; }
        else if (ix < 21) { role = 6; id = xc; }
        else if (ix < 29) { role = 1; id = 32 + xc * 8 + ix - 21; }
        else { role = 1; id = 64 + xc * 8 + ix - 29; } }
    else { const int y = xc - 4;
        if (ix < 8) { role = 1; id = y * 8 + ix; }
        else if (ix < 13) { role = 1; id = 64 + y * 8 + ix - 5; }
        else if (ix < 21) { role = 2; id = y * 8 + ix - 13; }
        else if (ix < 28) { role = 3; id = y * 7 + ix - 21; }
        else { role = 5; id = y * 4 + ix - 28; } }
    if (role == 4 || role == 5) {
        int lane;
        asm volatile("v_mbcnt_lo_u32_b32 %0, -1, 0\n\tv_mbcnt_hi_u32_b32 %0, -1, %0" : "=v"(lane));
        const rsrc_t rw = __builtin_amdgcn_make_buffer_rsrc(p.ws, 0, WS_END, 0x00020000);
        ((volatile unsigned*)smem)[16] = 0;
        __syncthreads();
        if (role == 5 && id == 0) { if (w == 0) probe(rw, lane); __syncthreads(); }
        else init_share(p, rw, (role - 4) * 16 + id, w, lane);
    }
    if (role == 6) { role = 5; id += 16; }
    asm volatile(
"s_cmp_eq_u32 %3, 1\n"
"s_cbranch_scc1 ROLE1_%=\n"
"s_cmp_eq_u32 %3, 2\n"
"s_cbranch_scc1 ROLE2_%=\n"
"s_cmp_eq_u32 %3, 3\n"
"s_cbranch_scc1 ROLE3_%=\n"
"s_cmp_eq_u32 %3, 4\n"
"s_cbranch_scc1 ROLE4_%=\n"
"s_cmp_eq_u32 %3, 5\n"
"s_cbranch_scc1 ROLE5_%=\n"
"ROLE0_%=:\n"
"s_load_dwordx8 s[80:87], %0, 16\n"
"s_load_dwordx2 s[88:89], %0, 72\n"
"s_mov_b32 s21, %1\n"
"s_mov_b32 s20, %2\n"
"s_lshr_b32 s22, s21, 1\n"
"s_and_b32 s23, s21, 1\n"
"s_mov_b32 s27, 0x40000000\n"
"s_mov_b32 s47, 0x05040100\n"
"s_mov_b32 s48, 0x07060302\n"
"s_mov_b32 s46, 0x3e0293ee\n"
"v_mbcnt_lo_u32_b32 v20, -1, 0\n"
"v_mbcnt_hi_u32_b32 v20, -1, v20\n"
"v_and_b32 v21, 15, v20\n"
"v_lshrrev_b32 v22, 4, v20\n"
"v_lshlrev_b32 v0, 4, v20\n"
"v_and_b32 v23, 1, v20\n"
"v_bfe_u32 v28, v20, 1, 1\n"
"v_cmp_eq_u32 s[44:45], 1, v28\n"
"v_lshrrev_b32 v26, 2, v21\n"
"v_lshl_add_u32 v29, v26, 1, v23\n"
"v_mov_b32 v24, 0x4444\n"
"v_mov_b32 v25, 0xeeee\n"
"v_cndmask_b32 v1, v24, v25, s[44:45]\n"
"v_lshrrev_b32 v25, 1, v21\n"
"v_lshrrev_b32 v24, 1, v22\n"
"v_xor_b32 v24, v24, v26\n"
"v_and_b32 v24, 1, v24\n"
"v_lshl_add_u32 v24, v26, 1, v24\n"
"v_lshlrev_b32 v24, 7, v24\n"
"v_lshl_add_u32 v24, v23, 6, v24\n"
"v_and_b32 v27, 1, v22\n"
"v_lshl_add_u32 v27, v27, 1, v28\n"
"v_lshl_add_u32 v24, v27, 4, v24\n"
"v_add_u32 v2, 150016, v24\n"
"v_lshrrev_b32 v24, 3, v21\n"
"v_xor_b32 v24, v24, v22\n"
"v_and_b32 v24, 1, v24\n"
"v_lshl_add_u32 v24, v22, 1, v24\n"
"v_lshlrev_b32 v24, 14, v24\n"
"v_and_b32 v27, 7, v21\n"
"v_lshl_add_u32 v24, v27, 4, v24\n"
"s_lshl_b32 s71, s21, 7\n"
"v_add_u32 v3, s71, v24\n"
"v_lshlrev_b32 v24, 6, v22\n"
"v_lshl_add_u32 v24, v21, 3, v24\n"
"v_and_b32 v27, 0x28, v20\n"
"v_cmp_eq_u32 vcc, 0, v27\n"
"v_mov_b32 v27, s27\n"
"s_nop 0\n"
"v_cndmask_b32 v4, v27, v24, vcc\n"
"s_add_u32 s71, s22, 160\n"
"s_lshl_b32 s71, s71, 7\n"
"v_mul_u32_u24 v24, 12288, v23\n"
"v_and_b32 v27, 3, v25\n"
"v_lshl_add_u32 v24, v27, 2, v24\n"
"v_add_u32 v24, s71, v24\n"
"v_cmp_gt_u32 vcc, 8, v20\n"
"v_mov_b32 v27, s27\n"
"s_nop 0\n"
"v_cndmask_b32 v5, v27, v24, vcc\n"
"s_mul_i32 s71, s20, 20480\n"
"v_add_u32 v6, s71, v0\n"
"s_mul_i32 s71, s20, 12288\n"
"s_add_u32 s71, s71, 81920\n"
"v_add_u32 v7, s71, v0\n"
"v_add_u32 v8, 131072, v0\n"
"v_and_b32 v24, 3, v21\n"
"v_lshlrev_b32 v24, 2, v24\n"
"v_add_u32 v24, s20, v24\n"
"v_lshl_add_u32 v24, v22, 4, v24\n"
"v_lshlrev_b32 v24, 4, v24\n"
"v_lshrrev_b32 v27, 1, v26\n"
"v_lshl_add_u32 v24, v27, 10, v24\n"
"v_and_b32 v27, 1, v26\n"
"v_lshl_add_u32 v24, v27, 3, v24\n"
"v_add_u32 v9, 147968, v24\n"
"s_lshr_b32 s71, s20, 1\n"
"s_mul_i32 s71, s71, 2304\n"
"s_and_b32 s72, s20, 1\n"
"s_lshl_b32 s72, s72, 7\n"
"s_add_u32 s71, s71, s72\n"
"s_add_u32 s71, s71, 143360\n"
"v_mul_u32_u24 v24, 576, v22\n"
"v_lshl_add_u32 v24, v21, 3, v24\n"
"v_add_u32 v10, s71, v24\n"
"v_mul_u32_u24 v24, 288, v29\n"
"v_lshl_add_u32 v24, v22, 4, v24\n"
"v_add_u32 v11, 143360, v24\n"
"s_lshl_b32 s71, s20, 10\n"
"v_add_u32 v12, s71, v8\n"
"s_lshl_b32 s71, s23, 1\n"
"s_add_u32 s71, s71, s20\n"
"s_lshl_b32 s71, s71, 6\n"
"s_add_u32 s71, s71, 2304\n"
"v_add_u32 v24, s71, v11\n"
"s_lshl_b32 s71, s20, 10\n"
"s_add_u32 s71, s71, 145920\n"
"v_add_u32 v27, s71, v0\n"
"s_cmp_lt_u32 s20, 2\n"
"s_cselect_b64 vcc, -1, 0\n"
"v_cndmask_b32 v13, v27, v24, vcc\n"
"v_and_b32 v24, 7, v21\n"
"v_and_b32 v27, 4, v21\n"
"v_lshrrev_b32 v27, 1, v27\n"
"v_or_b32 v27, v27, v23\n"
"v_and_b32 v30, 2, v21\n"
"v_lshl_or_b32 v27, v30, 1, v27\n"
"s_bitcmp1_b32 s20, 0\n"
"s_cselect_b64 vcc, -1, 0\n"
"v_cndmask_b32 v24, v24, v27, vcc\n"
"v_and_b32 v27, 2, v24\n"
"v_cmp_eq_u32 vcc, 0, v27\n"
"v_mov_b32 v27, 0xeeee0000\n"
"v_mov_b32 v30, 0x44440000\n"
"v_cndmask_b32 v27, v27, v30, vcc\n"
"v_or_b32 v1, v1, v27\n"
"v_and_b32 v27, 1, v24\n"
"v_cmp_eq_u32 vcc, 0, v27\n"
"v_mov_b32 v27, 0x3f800000\n"
"v_mov_b32 v30, 0x3f80\n"
"v_cndmask_b32 v27, v27, v30, vcc\n"
"v_cmp_gt_u32 vcc, 4, v24\n"
"s_nop 1\n"
"v_cndmask_b32 v14, 0, v27, vcc\n"
"v_cndmask_b32 v16, 0, v27, vcc\n"
"v_cndmask_b32 v15, v27, 0, vcc\n"
"v_cndmask_b32 v17, v27, 0, vcc\n"
"v_lshrrev_b32 v24, 1, v22\n"
"v_and_b32 v27, 1, v22\n"
"v_lshlrev_b32 v31, 4, v24\n"
"v_lshl_add_u32 v31, v27, 2, v31\n"
"v_lshlrev_b32 v31, 13, v31\n"
"v_lshl_add_u32 v31, v21, 3, v31\n"
"s_lshl_b32 s71, s22, 8\n"
"s_and_b32 s72, s20, 1\n"
"s_lshl_b32 s72, s72, 7\n"
"s_add_u32 s72, s72, s71\n"
"v_add_u32 v18, s72, v31\n"
"s_lshl_b32 s72, s23, 7\n"
"s_add_u32 s72, s72, s71\n"
"v_add_u32 v156, s72, v31\n"
"v_lshlrev_b32 v31, 4, v27\n"
"v_lshl_add_u32 v31, v24, 2, v31\n"
"v_mul_u32_u24 v31, 1280, v31\n"
"v_lshl_add_u32 v159, v21, 3, v31\n"
"v_lshl_add_u32 v157, v21, 2, v31\n"
"v_xor_b32 v24, s20, v22\n"
"v_and_b32 v24, 1, v24\n"
"v_lshl_add_u32 v24, v22, 1, v24\n"
"v_lshlrev_b32 v24, 14, v24\n"
"v_lshl_add_u32 v24, v21, 3, v24\n"
"s_lshl_b32 s71, s21, 7\n"
"v_add_u32 v19, s71, v24\n"
"s_waitcnt lgkmcnt(0)\n"
"s_mov_b32 s12, s88\n"
"s_and_b32 s13, s89, 0xffff\n"
"s_mov_b32 s14, 149946368\n"
"s_mov_b32 s15, 0x20000\n"
"s_cmp_lt_u32 s20, 2\n"
"s_cselect_b32 s90, s80, s82\n"
"s_cselect_b32 s91, s81, s83\n"
"v_mov_b32 v20, 0\n"
"v_mov_b32 v21, 0\n"
"v_mov_b32 v22, 0\n"
"v_mov_b32 v23, 0\n"
"ds_write_b128 v8, v[20:23] offset:16896\n"
"ds_write_b128 v8, v[20:23] offset:17920\n"
"s_waitcnt lgkmcnt(0)\n"
"s_mov_b32 s16, s84\n"
"s_and_b32 s17, s85, 0xffff\n"
"s_mov_b32 s18, 5242880\n"
"s_mov_b32 s19, 0x20000\n"
"s_add_u32 s71, s20, 0\n"
"s_lshl_b32 s71, s71, 19\n"
"s_add_u32 s74, s71, 0\n"
"buffer_load_dwordx2 v[20:21], v156, s[16:19], s74 offen\n"
"s_add_u32 s74, s71, 8192\n"
"buffer_load_dwordx2 v[22:23], v156, s[16:19], s74 offen\n"
"s_add_u32 s74, s71, 16384\n"
"buffer_load_dwordx2 v[24:25], v156, s[16:19], s74 offen\n"
"s_add_u32 s74, s71, 24576\n"
"buffer_load_dwordx2 v[26:27], v156, s[16:19], s74 offen\n"
"s_add_u32 s74, s71, 65536\n"
"buffer_load_dwordx2 v[28:29], v156, s[16:19], s74 offen\n"
"s_add_u32 s74, s71, 73728\n"
"buffer_load_dwordx2 v[30:31], v156, s[16:19], s74 offen\n"
"s_add_u32 s74, s71, 81920\n"
"buffer_load_dwordx2 v[32:33], v156, s[16:19], s74 offen\n"
"s_add_u32 s74, s71, 90112\n"
"buffer_load_dwordx2 v[34:35], v156, s[16:19], s74 offen\n"
"s_add_u32 s74, s71, 262144\n"
"buffer_load_dwordx2 v[36:37], v156, s[16:19], s74 offen\n"
"s_add_u32 s74, s71, 270336\n"
"buffer_load_dwordx2 v[38:39], v156, s[16:19], s74 offen\n"
"s_add_u32 s74, s71, 278528\n"
"buffer_load_dwordx2 v[40:41], v156, s[16:19], s74 offen\n"
"s_add_u32 s74, s71, 286720\n"
"buffer_load_dwordx2 v[42:43], v156, s[16:19], s74 offen\n"
"s_add_u32 s74, s71, 327680\n"
"buffer_load_dwordx2 v[44:45], v156, s[16:19], s74 offen\n"
"s_add_u32 s74, s71, 335872\n"
"buffer_load_dwordx2 v[46:47], v156, s[16:19], s74 offen\n"
"s_add_u32 s74, s71, 344064\n"
"buffer_load_dwordx2 v[48:49], v156, s[16:19], s74 offen\n"
"s_add_u32 s74, s71, 352256\n"
"buffer_load_dwordx2 v[50:51], v156, s[16:19], s74 offen\n"
"s_add_u32 s71, s20, 4\n"
"s_lshl_b32 s71, s71, 19\n"
"s_add_u32 s74, s71, 0\n"
"buffer_load_dwordx2 v[52:53], v156, s[16:19], s74 offen\n"
"s_add_u32 s74, s71, 8192\n"
"buffer_load_dwordx2 v[54:55], v156, s[16:19], s74 offen\n"
"s_add_u32 s74, s71, 16384\n"
"buffer_load_dwordx2 v[56:57], v156, s[16:19], s74 offen\n"
"s_add_u32 s74, s71, 24576\n"
"buffer_load_dwordx2 v[58:59], v156, s[16:19], s74 offen\n"
"s_add_u32 s74, s71, 65536\n"
"buffer_load_dwordx2 v[60:61], v156, s[16:19], s74 offen\n"
"s_add_u32 s74, s71, 73728\n"
"buffer_load_dwordx2 v[62:63], v156, s[16:19], s74 offen\n"
"s_add_u32 s74, s71, 81920\n"
"buffer_load_dwordx2 v[64:65], v156, s[16:19], s74 offen\n"
"s_add_u32 s74, s71, 90112\n"
"buffer_load_dwordx2 v[66:67], v156, s[16:19], s74 offen\n"
"s_add_u32 s74, s71, 262144\n"
"buffer_load_dwordx2 v[68:69], v156, s[16:19], s74 offen\n"
"s_add_u32 s74, s71, 270336\n"
"buffer_load_dwordx2 v[70:71], v156, s[16:19], s74 offen\n"
"s_add_u32 s74, s71, 278528\n"
"buffer_load_dwordx2 v[72:73], v156, s[16:19], s74 offen\n"
"s_add_u32 s74, s71, 286720\n"
"buffer_load_dwordx2 v[74:75], v156, s[16:19], s74 offen\n"
"s_add_u32 s74, s71, 327680\n"
"buffer_load_dwordx2 v[76:77], v156, s[16:19], s74 offen\n"
"s_add_u32 s74, s71, 335872\n"
"buffer_load_dwordx2 v[78:79], v156, s[16:19], s74 offen\n"
"s_add_u32 s74, s71, 344064\n"
"buffer_load_dwordx2 v[80:81], v156, s[16:19], s74 offen\n"
"s_add_u32 s74, s71, 352256\n"
"buffer_load_dwordx2 v[82:83], v156, s[16:19], s74 offen\n"
"s_add_u32 s71, s20, 8\n"
"s_lshl_b32 s71, s71, 19\n"
"s_add_u32 s74, s71, 0\n"
"buffer_load_dwordx2 v[84:85], v156, s[16:19], s74 offen\n"
"s_add_u32 s74, s71, 8192\n"
"buffer_load_dwordx2 v[86:87], v156, s[16:19], s74 offen\n"
"s_add_u32 s74, s71, 16384\n"
"buffer_load_dwordx2 v[88:89], v156, s[16:19], s74 offen\n"
"s_add_u32 s74, s71, 24576\n"
"buffer_load_dwordx2 v[90:91], v156, s[16:19], s74 offen\n"
"s_add_u32 s74, s71, 65536\n"
"buffer_load_dwordx2 v[92:93], v156, s[16:19], s74 offen\n"
"s_add_u32 s74, s71, 73728\n"
"buffer_load_dwordx2 v[94:95], v156, s[16:19], s74 offen\n"
"s_add_u32 s74, s71, 81920\n"
"buffer_load_dwordx2 v[96:97], v156, s[16:19], s74 offen\n"
"s_add_u32 s74, s71, 90112\n"
"buffer_load_dwordx2 v[98:99], v156, s[16:19], s74 offen\n"
"s_add_u32 s74, s71, 262144\n"
"buffer_load_dwordx2 v[100:101], v156, s[16:19], s74 offen\n"
"s_add_u32 s74, s71, 270336\n"
"buffer_load_dwordx2 v[102:103], v156, s[16:19], s74 offen\n"
"s_add_u32 s74, s71, 278528\n"
"buffer_load_dwordx2 v[104:105], v156, s[16:19], s74 offen\n"
"s_add_u32 s74, s71, 286720\n"
"buffer_load_dwordx2 v[106:107], v156, s[16:19], s74 offen\n"
"s_add_u32 s74, s71, 327680\n"
"buffer_load_dwordx2 v[108:109], v156, s[16:19], s74 offen\n"
"s_add_u32 s74, s71, 335872\n"
"buffer_load_dwordx2 v[110:111], v156, s[16:19], s74 offen\n"
"s_add_u32 s74, s71, 344064\n"
"buffer_load_dwordx2 v[112:113], v156, s[16:19], s74 offen\n"
"s_add_u32 s74, s71, 352256\n"
"buffer_load_dwordx2 v[114:115], v156, s[16:19], s74 offen\n"
"s_waitcnt vmcnt(32)\n"
"s_add_u32 s75, s20, 0\n"
"s_lshl_b32 s75, s75, 11\n"
"v_add_u32 v158, s75, v0\n"
"v_perm_b32 v116, v22, v20, s47\n"
"v_perm_b32 v117, v30, v28, s47\n"
"v_perm_b32 v118, v26, v24, s47\n"
"v_perm_b32 v119, v34, v32, s47\n"
"v_perm_b32 v120, v38, v36, s47\n"
"v_perm_b32 v121, v46, v44, s47\n"
"v_perm_b32 v122, v42, v40, s47\n"
"v_perm_b32 v123, v50, v48, s47\n"
"ds_write_b128 v158, v[116:119] offset:0\n"
"ds_write_b128 v158, v[120:123] offset:1024\n"
"v_perm_b32 v124, v22, v20, s48\n"
"v_perm_b32 v125, v30, v28, s48\n"
"v_perm_b32 v126, v26, v24, s48\n"
"v_perm_b32 v127, v34, v32, s48\n"
"v_perm_b32 v128, v38, v36, s48\n"
"v_perm_b32 v129, v46, v44, s48\n"
"v_perm_b32 v130, v42, v40, s48\n"
"v_perm_b32 v131, v50, v48, s48\n"
"ds_write_b128 v158, v[124:127] offset:20480\n"
"ds_write_b128 v158, v[128:131] offset:21504\n"
"v_perm_b32 v132, v23, v21, s47\n"
"v_perm_b32 v133, v31, v29, s47\n"
"v_perm_b32 v134, v27, v25, s47\n"
"v_perm_b32 v135, v35, v33, s47\n"
"v_perm_b32 v136, v39, v37, s47\n"
"v_perm_b32 v137, v47, v45, s47\n"
"v_perm_b32 v138, v43, v41, s47\n"
"v_perm_b32 v139, v51, v49, s47\n"
"ds_write_b128 v158, v[132:135] offset:40960\n"
"ds_write_b128 v158, v[136:139] offset:41984\n"
"v_perm_b32 v140, v23, v21, s48\n"
"v_perm_b32 v141, v31, v29, s48\n"
"v_perm_b32 v142, v27, v25, s48\n"
"v_perm_b32 v143, v35, v33, s48\n"
"v_perm_b32 v144, v39, v37, s48\n"
"v_perm_b32 v145, v47, v45, s48\n"
"v_perm_b32 v146, v43, v41, s48\n"
"v_perm_b32 v147, v51, v49, s48\n"
"ds_write_b128 v158, v[140:143] offset:61440\n"
"ds_write_b128 v158, v[144:147] offset:62464\n"
"s_waitcnt lgkmcnt(0)\n"
"s_mov_b32 s16, s86\n"
"s_and_b32 s17, s87, 0xffff\n"
"s_mov_b32 s18, 5242880\n"
"s_mov_b32 s19, 0x20000\n"
"s_lshl_b32 s71, s21, 6\n"
"s_mul_i32 s71, s71, 1280\n"
"s_add_u32 s72, s20, 0\n"
"s_lshl_b32 s73, s72, 7\n"
"s_add_u32 s71, s71, s73\n"
"s_add_u32 s74, s71, 0\n"
"buffer_load_dwordx2 v[20:21], v159, s[16:19], s74 offen\n"
"s_add_u32 s74, s71, 1280\n"
"buffer_load_dwordx2 v[22:23], v159, s[16:19], s74 offen\n"
"s_add_u32 s74, s71, 2560\n"
"buffer_load_dwordx2 v[24:25], v159, s[16:19], s74 offen\n"
"s_add_u32 s74, s71, 3840\n"
"buffer_load_dwordx2 v[26:27], v159, s[16:19], s74 offen\n"
"s_add_u32 s74, s71, 40960\n"
"buffer_load_dwordx2 v[28:29], v159, s[16:19], s74 offen\n"
"s_add_u32 s74, s71, 42240\n"
"buffer_load_dwordx2 v[30:31], v159, s[16:19], s74 offen\n"
"s_add_u32 s74, s71, 43520\n"
"buffer_load_dwordx2 v[32:33], v159, s[16:19], s74 offen\n"
"s_add_u32 s74, s71, 44800\n"
"buffer_load_dwordx2 v[34:35], v159, s[16:19], s74 offen\n"
"s_add_u32 s74, s71, 10240\n"
"buffer_load_dwordx2 v[36:37], v159, s[16:19], s74 offen\n"
"s_add_u32 s74, s71, 11520\n"
"buffer_load_dwordx2 v[38:39], v159, s[16:19], s74 offen\n"
"s_add_u32 s74, s71, 12800\n"
"buffer_load_dwordx2 v[40:41], v159, s[16:19], s74 offen\n"
"s_add_u32 s74, s71, 14080\n"
"buffer_load_dwordx2 v[42:43], v159, s[16:19], s74 offen\n"
"s_add_u32 s74, s71, 51200\n"
"buffer_load_dwordx2 v[44:45], v159, s[16:19], s74 offen\n"
"s_add_u32 s74, s71, 52480\n"
"buffer_load_dwordx2 v[46:47], v159, s[16:19], s74 offen\n"
"s_add_u32 s74, s71, 53760\n"
"buffer_load_dwordx2 v[48:49], v159, s[16:19], s74 offen\n"
"s_add_u32 s74, s71, 55040\n"
"buffer_load_dwordx2 v[50:51], v159, s[16:19], s74 offen\n"
"s_waitcnt vmcnt(32)\n"
"s_add_u32 s75, s20, 4\n"
"s_lshl_b32 s75, s75, 11\n"
"v_add_u32 v158, s75, v0\n"
"v_perm_b32 v116, v54, v52, s47\n"
"v_perm_b32 v117, v62, v60, s47\n"
"v_perm_b32 v118, v58, v56, s47\n"
"v_perm_b32 v119, v66, v64, s47\n"
"v_perm_b32 v120, v70, v68, s47\n"
"v_perm_b32 v121, v78, v76, s47\n"
"v_perm_b32 v122, v74, v72, s47\n"
"v_perm_b32 v123, v82, v80, s47\n"
"ds_write_b128 v158, v[116:119] offset:0\n"
"ds_write_b128 v158, v[120:123] offset:1024\n"
"v_perm_b32 v124, v54, v52, s48\n"
"v_perm_b32 v125, v62, v60, s48\n"
"v_perm_b32 v126, v58, v56, s48\n"
"v_perm_b32 v127, v66, v64, s48\n"
"v_perm_b32 v128, v70, v68, s48\n"
"v_perm_b32 v129, v78, v76, s48\n"
"v_perm_b32 v130, v74, v72, s48\n"
"v_perm_b32 v131, v82, v80, s48\n"
"ds_write_b128 v158, v[124:127] offset:20480\n"
"ds_write_b128 v158, v[128:131] offset:21504\n"
"v_perm_b32 v132, v55, v53, s47\n"
"v_perm_b32 v133, v63, v61, s47\n"
"v_perm_b32 v134, v59, v57, s47\n"
"v_perm_b32 v135, v67, v65, s47\n"
"v_perm_b32 v136, v71, v69, s47\n"
"v_perm_b32 v137, v79, v77, s47\n"
"v_perm_b32 v138, v75, v73, s47\n"
"v_perm_b32 v139, v83, v81, s47\n"
"ds_write_b128 v158, v[132:135] offset:40960\n"
"ds_write_b128 v158, v[136:139] offset:41984\n"
"v_perm_b32 v140, v55, v53, s48\n"
"v_perm_b32 v141, v63, v61, s48\n"
"v_perm_b32 v142, v59, v57, s48\n"
"v_perm_b32 v143, v67, v65, s48\n"
"v_perm_b32 v144, v71, v69, s48\n"
"v_perm_b32 v145, v79, v77, s48\n"
"v_perm_b32 v146, v75, v73, s48\n"
"v_perm_b32 v147, v83, v81, s48\n"
"ds_write_b128 v158, v[140:143] offset:61440\n"
"ds_write_b128 v158, v[144:147] offset:62464\n"
"s_waitcnt lgkmcnt(0)\n"
"s_lshl_b32 s71, s21, 6\n"
"s_mul_i32 s71, s71, 1280\n"
"s_add_u32 s72, s20, 4\n"
"s_lshl_b32 s73, s72, 7\n"
"s_add_u32 s71, s71, s73\n"
"s_add_u32 s74, s71, 0\n"
"buffer_load_dwordx2 v[52:53], v159, s[16:19], s74 offen\n"
"s_add_u32 s74, s71, 1280\n"
"buffer_load_dwordx2 v[54:55], v159, s[16:19], s74 offen\n"
"s_add_u32 s74, s71, 2560\n"
"buffer_load_dwordx2 v[56:57], v159, s[16:19], s74 offen\n"
"s_add_u32 s74, s71, 3840\n"
"buffer_load_dwordx2 v[58:59], v159, s[16:19], s74 offen\n"
"s_add_u32 s74, s71, 40960\n"
"buffer_load_dwordx2 v[60:61], v159, s[16:19], s74 offen\n"
"s_add_u32 s74, s71, 42240\n"
"buffer_load_dwordx2 v[62:63], v159, s[16:19], s74 offen\n"
"s_add_u32 s74, s71, 43520\n"
"buffer_load_dwordx2 v[64:65], v159, s[16:19], s74 offen\n"
"s_add_u32 s74, s71, 44800\n"
"buffer_load_dwordx2 v[66:67], v159, s[16:19], s74 offen\n"
"s_add_u32 s74, s71, 10240\n"
"buffer_load_dwordx2 v[68:69], v159, s[16:19], s74 offen\n"
"s_add_u32 s74, s71, 11520\n"
"buffer_load_dwordx2 v[70:71], v159, s[16:19], s74 offen\n"
"s_add_u32 s74, s71, 12800\n"
"buffer_load_dwordx2 v[72:73], v159, s[16:19], s74 offen\n"
"s_add_u32 s74, s71, 14080\n"
"buffer_load_dwordx2 v[74:75], v159, s[16:19], s74 offen\n"
"s_add_u32 s74, s71, 51200\n"
"buffer_load_dwordx2 v[76:77], v159, s[16:19], s74 offen\n"
"s_add_u32 s74, s71, 52480\n"
"buffer_load_dwordx2 v[78:79], v159, s[16:19], s74 offen\n"
"s_add_u32 s74, s71, 53760\n"
"buffer_load_dwordx2 v[80:81], v159, s[16:19], s74 offen\n"
"s_add_u32 s74, s71, 55040\n"
"buffer_load_dwordx2 v[82:83], v159, s[16:19], s74 offen\n"
"s_waitcnt vmcnt(32)\n"
"s_cmp_ge_u32 s20, 2\n"
"s_cbranch_scc1 XP_skip2_%=\n"
"s_add_u32 s75, s20, 8\n"
"s_lshl_b32 s75, s75, 11\n"
"v_add_u32 v158, s75, v0\n"
"v_perm_b32 v116, v86, v84, s47\n"
"v_perm_b32 v117, v94, v92, s47\n"
"v_perm_b32 v118, v90, v88, s47\n"
"v_perm_b32 v119, v98, v96, s47\n"
"v_perm_b32 v120, v102, v100, s47\n"
"v_perm_b32 v121, v110, v108, s47\n"
"v_perm_b32 v122, v106, v104, s47\n"
"v_perm_b32 v123, v114, v112, s47\n"
"ds_write_b128 v158, v[116:119] offset:0\n"
"ds_write_b128 v158, v[120:123] offset:1024\n"
"v_perm_b32 v124, v86, v84, s48\n"
"v_perm_b32 v125, v94, v92, s48\n"
"v_perm_b32 v126, v90, v88, s48\n"
"v_perm_b32 v127, v98, v96, s48\n"
"v_perm_b32 v128, v102, v100, s48\n"
"v_perm_b32 v129, v110, v108, s48\n"
"v_perm_b32 v130, v106, v104, s48\n"
"v_perm_b32 v131, v114, v112, s48\n"
"ds_write_b128 v158, v[124:127] offset:20480\n"
"ds_write_b128 v158, v[128:131] offset:21504\n"
"v_perm_b32 v132, v87, v85, s47\n"
"v_perm_b32 v133, v95, v93, s47\n"
"v_perm_b32 v134, v91, v89, s47\n"
"v_perm_b32 v135, v99, v97, s47\n"
"v_perm_b32 v136, v103, v101, s47\n"
"v_perm_b32 v137, v111, v109, s47\n"
"v_perm_b32 v138, v107, v105, s47\n"
"v_perm_b32 v139, v115, v113, s47\n"
"ds_write_b128 v158, v[132:135] offset:40960\n"
"ds_write_b128 v158, v[136:139] offset:41984\n"
"v_perm_b32 v140, v87, v85, s48\n"
"v_perm_b32 v141, v95, v93, s48\n"
"v_perm_b32 v142, v91, v89, s48\n"
"v_perm_b32 v143, v99, v97, s48\n"
"v_perm_b32 v144, v103, v101, s48\n"
"v_perm_b32 v145, v111, v109, s48\n"
"v_perm_b32 v146, v107, v105, s48\n"
"v_perm_b32 v147, v115, v113, s48\n"
"ds_write_b128 v158, v[140:143] offset:61440\n"
"ds_write_b128 v158, v[144:147] offset:62464\n"
"s_waitcnt lgkmcnt(0)\n"
"XP_skip2_%=:\n"
"s_nop 15\n"
"s_lshl_b32 s71, s21, 6\n"
"s_mul_i32 s71, s71, 1280\n"
"s_lshl_b32 s73, s20, 6\n"
"s_add_u32 s73, s73, 1024\n"
"s_add_u32 s71, s71, s73\n"
"s_add_u32 s74, s71, 0\n"
"buffer_load_dword v84, v157, s[16:19], s74 offen\n"
"s_add_u32 s74, s71, 1280\n"
"buffer_load_dword v86, v157, s[16:19], s74 offen\n"
"s_add_u32 s74, s71, 2560\n"
"buffer_load_dword v88, v157, s[16:19], s74 offen\n"
"s_add_u32 s74, s71, 3840\n"
"buffer_load_dword v90, v157, s[16:19], s74 offen\n"
"s_add_u32 s74, s71, 40960\n"
"buffer_load_dword v92, v157, s[16:19], s74 offen\n"
"s_add_u32 s74, s71, 42240\n"
"buffer_load_dword v94, v157, s[16:19], s74 offen\n"
"s_add_u32 s74, s71, 43520\n"
"buffer_load_dword v96, v157, s[16:19], s74 offen\n"
"s_add_u32 s74, s71, 44800\n"
"buffer_load_dword v98, v157, s[16:19], s74 offen\n"
"s_add_u32 s74, s71, 10240\n"
"buffer_load_dword v100, v157, s[16:19], s74 offen\n"
"s_add_u32 s74, s71, 11520\n"
"buffer_load_dword v102, v157, s[16:19], s74 offen\n"
"s_add_u32 s74, s71, 12800\n"
"buffer_load_dword v104, v157, s[16:19], s74 offen\n"
"s_add_u32 s74, s71, 14080\n"
"buffer_load_dword v106, v157, s[16:19], s74 offen\n"
"s_add_u32 s74, s71, 51200\n"
"buffer_load_dword v108, v157, s[16:19], s74 offen\n"
"s_add_u32 s74, s71, 52480\n"
"buffer_load_dword v110, v157, s[16:19], s74 offen\n"
"s_add_u32 s74, s71, 53760\n"
"buffer_load_dword v112, v157, s[16:19], s74 offen\n"
"s_add_u32 s74, s71, 55040\n"
"buffer_load_dword v114, v157, s[16:19], s74 offen\n"
"s_barrier\n"
"ds_read_b128 a[0:3], v6 offset:0\n"
"ds_read_b128 a[4:7], v6 offset:1024\n"
"ds_read_b128 a[8:11], v6 offset:2048\n"
"ds_read_b128 a[12:15], v6 offset:3072\n"
"ds_read_b128 a[16:19], v6 offset:4096\n"
"ds_read_b128 a[20:23], v6 offset:5120\n"
"ds_read_b128 a[24:27], v6 offset:6144\n"
"ds_read_b128 a[28:31], v6 offset:7168\n"
"s_waitcnt lgkmcnt(0)\n"
"ds_read_b128 a[32:35], v6 offset:8192\n"
"ds_read_b128 a[36:39], v6 offset:9216\n"
"ds_read_b128 a[40:43], v6 offset:10240\n"
"ds_read_b128 a[44:47], v6 offset:11264\n"
"ds_read_b128 a[48:51], v6 offset:12288\n"
"ds_read_b128 a[52:55], v6 offset:13312\n"
"ds_read_b128 a[56:59], v6 offset:14336\n"
"ds_read_b128 a[60:63], v6 offset:15360\n"
"s_waitcnt lgkmcnt(0)\n"
"ds_read_b128 a[64:67], v6 offset:16384\n"
"ds_read_b128 a[68:71], v6 offset:17408\n"
"ds_read_b128 a[72:75], v6 offset:18432\n"
"ds_read_b128 a[76:79], v6 offset:19456\n"
"s_waitcnt lgkmcnt(0)\n"
"s_waitcnt vmcnt(32)\n"
"v_perm_b32 v160, v22, v20, s47\n"
"v_perm_b32 v161, v30, v28, s47\n"
"v_perm_b32 v162, v26, v24, s47\n"
"v_perm_b32 v163, v34, v32, s47\n"
"v_perm_b32 v164, v38, v36, s47\n"
"v_perm_b32 v165, v46, v44, s47\n"
"v_perm_b32 v166, v42, v40, s47\n"
"v_perm_b32 v167, v50, v48, s47\n"
"v_perm_b32 v168, v22, v20, s48\n"
"v_perm_b32 v169, v30, v28, s48\n"
"v_perm_b32 v170, v26, v24, s48\n"
"v_perm_b32 v171, v34, v32, s48\n"
"v_perm_b32 v172, v38, v36, s48\n"
"v_perm_b32 v173, v46, v44, s48\n"
"v_perm_b32 v174, v42, v40, s48\n"
"v_perm_b32 v175, v50, v48, s48\n"
"v_perm_b32 v176, v23, v21, s47\n"
"v_perm_b32 v177, v31, v29, s47\n"
"v_perm_b32 v178, v27, v25, s47\n"
"v_perm_b32 v179, v35, v33, s47\n"
"v_perm_b32 v180, v39, v37, s47\n"
"v_perm_b32 v181, v47, v45, s47\n"
"v_perm_b32 v182, v43, v41, s47\n"
"v_perm_b32 v183, v51, v49, s47\n"
"v_perm_b32 v184, v23, v21, s48\n"
"v_perm_b32 v185, v31, v29, s48\n"
"v_perm_b32 v186, v27, v25, s48\n"
"v_perm_b32 v187, v35, v33, s48\n"
"v_perm_b32 v188, v39, v37, s48\n"
"v_perm_b32 v189, v47, v45, s48\n"
"v_perm_b32 v190, v43, v41, s48\n"
"v_perm_b32 v191, v51, v49, s48\n"
"s_waitcnt lgkmcnt(0)\n"
"s_mov_b32 s16, s90\n"
"s_and_b32 s17, s91, 0xffff\n"
"s_mov_b32 s18, 5242880\n"
"s_mov_b32 s19, 0x20000\n"
"s_mov_b32 s71, 0\n"
"s_add_u32 s74, s71, 0\n"
"buffer_load_dwordx2 v[20:21], v18, s[16:19], s74 offen\n"
"s_add_u32 s74, s71, 8192\n"
"buffer_load_dwordx2 v[22:23], v18, s[16:19], s74 offen\n"
"s_add_u32 s74, s71, 16384\n"
"buffer_load_dwordx2 v[24:25], v18, s[16:19], s74 offen\n"
"s_add_u32 s74, s71, 24576\n"
"buffer_load_dwordx2 v[26:27], v18, s[16:19], s74 offen\n"
"s_add_u32 s74, s71, 65536\n"
"buffer_load_dwordx2 v[28:29], v18, s[16:19], s74 offen\n"
"s_add_u32 s74, s71, 73728\n"
"buffer_load_dwordx2 v[30:31], v18, s[16:19], s74 offen\n"
"s_add_u32 s74, s71, 81920\n"
"buffer_load_dwordx2 v[32:33], v18, s[16:19], s74 offen\n"
"s_add_u32 s74, s71, 90112\n"
"buffer_load_dwordx2 v[34:35], v18, s[16:19], s74 offen\n"
"s_add_u32 s74, s71, 262144\n"
"buffer_load_dwordx2 v[36:37], v18, s[16:19], s74 offen\n"
"s_add_u32 s74, s71, 270336\n"
"buffer_load_dwordx2 v[38:39], v18, s[16:19], s74 offen\n"
"s_add_u32 s74, s71, 278528\n"
"buffer_load_dwordx2 v[40:41], v18, s[16:19], s74 offen\n"
"s_add_u32 s74, s71, 286720\n"
"buffer_load_dwordx2 v[42:43], v18, s[16:19], s74 offen\n"
"s_add_u32 s74, s71, 327680\n"
"buffer_load_dwordx2 v[44:45], v18, s[16:19], s74 offen\n"
"s_add_u32 s74, s71, 335872\n"
"buffer_load_dwordx2 v[46:47], v18, s[16:19], s74 offen\n"
"s_add_u32 s74, s71, 344064\n"
"buffer_load_dwordx2 v[48:49], v18, s[16:19], s74 offen\n"
"s_add_u32 s74, s71, 352256\n"
"buffer_load_dwordx2 v[50:51], v18, s[16:19], s74 offen\n"
"s_waitcnt vmcnt(32)\n"
"v_perm_b32 v116, v54, v52, s47\n"
"v_perm_b32 v117, v62, v60, s47\n"
"v_perm_b32 v118, v58, v56, s47\n"
"v_perm_b32 v119, v66, v64, s47\n"
"v_perm_b32 v120, v70, v68, s47\n"
"v_perm_b32 v121, v78, v76, s47\n"
"v_perm_b32 v122, v74, v72, s47\n"
"v_perm_b32 v123, v82, v80, s47\n"
"ds_write_b128 v7, v[116:119] offset:0\n"
"ds_write_b128 v7, v[120:123] offset:1024\n"
"v_perm_b32 v124, v54, v52, s48\n"
"v_perm_b32 v125, v62, v60, s48\n"
"v_perm_b32 v126, v58, v56, s48\n"
"v_perm_b32 v127, v66, v64, s48\n"
"v_perm_b32 v128, v70, v68, s48\n"
"v_perm_b32 v129, v78, v76, s48\n"
"v_perm_b32 v130, v74, v72, s48\n"
"v_perm_b32 v131, v82, v80, s48\n"
"ds_write_b128 v7, v[124:127] offset:2048\n"
"ds_write_b128 v7, v[128:131] offset:3072\n"
"v_perm_b32 v132, v55, v53, s47\n"
"v_perm_b32 v133, v63, v61, s47\n"
"v_perm_b32 v134, v59, v57, s47\n"
"v_perm_b32 v135, v67, v65, s47\n"
"v_perm_b32 v136, v71, v69, s47\n"
"v_perm_b32 v137, v79, v77, s47\n"
"v_perm_b32 v138, v75, v73, s47\n"
"v_perm_b32 v139, v83, v81, s47\n"
"ds_write_b128 v7, v[132:135] offset:4096\n"
"ds_write_b128 v7, v[136:139] offset:5120\n"
"v_perm_b32 v140, v55, v53, s48\n"
"v_perm_b32 v141, v63, v61, s48\n"
"v_perm_b32 v142, v59, v57, s48\n"
"v_perm_b32 v143, v67, v65, s48\n"
"v_perm_b32 v144, v71, v69, s48\n"
"v_perm_b32 v145, v79, v77, s48\n"
"v_perm_b32 v146, v75, v73, s48\n"
"v_perm_b32 v147, v83, v81, s48\n"
"ds_write_b128 v7, v[140:143] offset:6144\n"
"ds_write_b128 v7, v[144:147] offset:7168\n"
"s_waitcnt lgkmcnt(0)\n"
"s_mov_b32 s71, 524288\n"
"s_add_u32 s74, s71, 0\n"
"buffer_load_dwordx2 v[52:53], v18, s[16:19], s74 offen\n"
"s_add_u32 s74, s71, 8192\n"
"buffer_load_dwordx2 v[54:55], v18, s[16:19], s74 offen\n"
"s_add_u32 s74, s71, 16384\n"
"buffer_load_dwordx2 v[56:57], v18, s[16:19], s74 offen\n"
"s_add_u32 s74, s71, 24576\n"
"buffer_load_dwordx2 v[58:59], v18, s[16:19], s74 offen\n"
"s_add_u32 s74, s71, 65536\n"
"buffer_load_dwordx2 v[60:61], v18, s[16:19], s74 offen\n"
"s_add_u32 s74, s71, 73728\n"
"buffer_load_dwordx2 v[62:63], v18, s[16:19], s74 offen\n"
"s_add_u32 s74, s71, 81920\n"
"buffer_load_dwordx2 v[64:65], v18, s[16:19], s74 offen\n"
"s_add_u32 s74, s71, 90112\n"
"buffer_load_dwordx2 v[66:67], v18, s[16:19], s74 offen\n"
"s_add_u32 s74, s71, 262144\n"
"buffer_load_dwordx2 v[68:69], v18, s[16:19], s74 offen\n"
"s_add_u32 s74, s71, 270336\n"
"buffer_load_dwordx2 v[70:71], v18, s[16:19], s74 offen\n"
"s_add_u32 s74, s71, 278528\n"
"buffer_load_dwordx2 v[72:73], v18, s[16:19], s74 offen\n"
"s_add_u32 s74, s71, 286720\n"
"buffer_load_dwordx2 v[74:75], v18, s[16:19], s74 offen\n"
"s_add_u32 s74, s71, 327680\n"
"buffer_load_dwordx2 v[76:77], v18, s[16:19], s74 offen\n"
"s_add_u32 s74, s71, 335872\n"
"buffer_load_dwordx2 v[78:79], v18, s[16:19], s74 offen\n"
"s_add_u32 s74, s71, 344064\n"
"buffer_load_dwordx2 v[80:81], v18, s[16:19], s74 offen\n"
"s_add_u32 s74, s71, 352256\n"
"buffer_load_dwordx2 v[82:83], v18, s[16:19], s74 offen\n"
"s_waitcnt vmcnt(32)\n"
"v_perm_b32 v116, v86, v84, s47\n"
"v_perm_b32 v117, v94, v92, s47\n"
"v_perm_b32 v118, v90, v88, s47\n"
"v_perm_b32 v119, v98, v96, s47\n"
"v_perm_b32 v120, v102, v100, s47\n"
"v_perm_b32 v121, v110, v108, s47\n"
"v_perm_b32 v122, v106, v104, s47\n"
"v_perm_b32 v123, v114, v112, s47\n"
"ds_write_b128 v7, v[116:119] offset:8192\n"
"ds_write_b128 v7, v[120:123] offset:9216\n"
"v_perm_b32 v124, v86, v84, s48\n"
"v_perm_b32 v125, v94, v92, s48\n"
"v_perm_b32 v126, v90, v88, s48\n"
"v_perm_b32 v127, v98, v96, s48\n"
"v_perm_b32 v128, v102, v100, s48\n"
"v_perm_b32 v129, v110, v108, s48\n"
"v_perm_b32 v130, v106, v104, s48\n"
"v_perm_b32 v131, v114, v112, s48\n"
"ds_write_b128 v7, v[124:127] offset:10240\n"
"ds_write_b128 v7, v[128:131] offset:11264\n"
"s_waitcnt lgkmcnt(0)\n"
"s_mov_b32 s71, 1048576\n"
"s_add_u32 s74, s71, 0\n"
"buffer_load_dwordx2 v[84:85], v18, s[16:19], s74 offen\n"
"s_add_u32 s74, s71, 8192\n"
"buffer_load_dwordx2 v[86:87], v18, s[16:19], s74 offen\n"
"s_add_u32 s74, s71, 16384\n"
"buffer_load_dwordx2 v[88:89], v18, s[16:19], s74 offen\n"
"s_add_u32 s74, s71, 24576\n"
"buffer_load_dwordx2 v[90:91], v18, s[16:19], s74 offen\n"
"s_add_u32 s74, s71, 65536\n"
"buffer_load_dwordx2 v[92:93], v18, s[16:19], s74 offen\n"
"s_add_u32 s74, s71, 73728\n"
"buffer_load_dwordx2 v[94:95], v18, s[16:19], s74 offen\n"
"s_add_u32 s74, s71, 81920\n"
"buffer_load_dwordx2 v[96:97], v18, s[16:19], s74 offen\n"
"s_add_u32 s74, s71, 90112\n"
"buffer_load_dwordx2 v[98:99], v18, s[16:19], s74 offen\n"
"s_add_u32 s74, s71, 262144\n"
"buffer_load_dwordx2 v[100:101], v18, s[16:19], s74 offen\n"
"s_add_u32 s74, s71, 270336\n"
"buffer_load_dwordx2 v[102:103], v18, s[16:19], s74 offen\n"
"s_add_u32 s74, s71, 278528\n"
"buffer_load_dwordx2 v[104:105], v18, s[16:19], s74 offen\n"
"s_add_u32 s74, s71, 286720\n"
"buffer_load_dwordx2 v[106:107], v18, s[16:19], s74 offen\n"
"s_add_u32 s74, s71, 327680\n"
"buffer_load_dwordx2 v[108:109], v18, s[16:19], s74 offen\n"
"s_add_u32 s74, s71, 335872\n"
"buffer_load_dwordx2 v[110:111], v18, s[16:19], s74 offen\n"
"s_add_u32 s74, s71, 344064\n"
"buffer_load_dwordx2 v[112:113], v18, s[16:19], s74 offen\n"
"s_add_u32 s74, s71, 352256\n"
"buffer_load_dwordx2 v[114:115], v18, s[16:19], s74 offen\n"
"s_barrier\n"
"s_waitcnt vmcnt(32)\n"
"v_perm_b32 v116, v22, v20, s47\n"
"v_perm_b32 v117, v30, v28, s47\n"
"v_perm_b32 v118, v26, v24, s47\n"
"v_perm_b32 v119, v34, v32, s47\n"
"v_perm_b32 v120, v38, v36, s47\n"
"v_perm_b32 v121, v46, v44, s47\n"
"v_perm_b32 v122, v42, v40, s47\n"
"v_perm_b32 v123, v50, v48, s47\n"
"v_accvgpr_write_b32 a80, v116\n"
"v_accvgpr_write_b32 a81, v117\n"
"v_accvgpr_write_b32 a82, v118\n"
"v_accvgpr_write_b32 a83, v119\n"
"v_accvgpr_write_b32 a84, v120\n"
"v_accvgpr_write_b32 a85, v121\n"
"v_accvgpr_write_b32 a86, v122\n"
"v_accvgpr_write_b32 a87, v123\n"
"v_perm_b32 v124, v22, v20, s48\n"
"v_perm_b32 v125, v30, v28, s48\n"
"v_perm_b32 v126, v26, v24, s48\n"
"v_perm_b32 v127, v34, v32, s48\n"
"v_perm_b32 v128, v38, v36, s48\n"
"v_perm_b32 v129, v46, v44, s48\n"
"v_perm_b32 v130, v42, v40, s48\n"
"v_perm_b32 v131, v50, v48, s48\n"
"v_accvgpr_write_b32 a88, v124\n"
"v_accvgpr_write_b32 a89, v125\n"
"v_accvgpr_write_b32 a90, v126\n"
"v_accvgpr_write_b32 a91, v127\n"
"v_accvgpr_write_b32 a92, v128\n"
"v_accvgpr_write_b32 a93, v129\n"
"v_accvgpr_write_b32 a94, v130\n"
"v_accvgpr_write_b32 a95, v131\n"
"v_perm_b32 v132, v23, v21, s47\n"
"v_perm_b32 v133, v31, v29, s47\n"
"v_perm_b32 v134, v27, v25, s47\n"
"v_perm_b32 v135, v35, v33, s47\n"
"v_perm_b32 v136, v39, v37, s47\n"
"v_perm_b32 v137, v47, v45, s47\n"
"v_perm_b32 v138, v43, v41, s47\n"
"v_perm_b32 v139, v51, v49, s47\n"
"v_accvgpr_write_b32 a96, v132\n"
"v_accvgpr_write_b32 a97, v133\n"
"v_accvgpr_write_b32 a98, v134\n"
"v_accvgpr_write_b32 a99, v135\n"
"v_accvgpr_write_b32 a100, v136\n"
"v_accvgpr_write_b32 a101, v137\n"
"v_accvgpr_write_b32 a102, v138\n"
"v_accvgpr_write_b32 a103, v139\n"
"v_perm_b32 v140, v23, v21, s48\n"
"v_perm_b32 v141, v31, v29, s48\n"
"v_perm_b32 v142, v27, v25, s48\n"
"v_perm_b32 v143, v35, v33, s48\n"
"v_perm_b32 v144, v39, v37, s48\n"
"v_perm_b32 v145, v47, v45, s48\n"
"v_perm_b32 v146, v43, v41, s48\n"
"v_perm_b32 v147, v51, v49, s48\n"
"ds_write_b128 v6, v[140:143] offset:0\n"
"ds_write_b128 v6, v[144:147] offset:1024\n"
"s_waitcnt lgkmcnt(0)\n"
"s_mov_b32 s71, 1572864\n"
"s_add_u32 s74, s71, 0\n"
"buffer_load_dwordx2 v[20:21], v18, s[16:19], s74 offen\n"
"s_add_u32 s74, s71, 8192\n"
"buffer_load_dwordx2 v[22:23], v18, s[16:19], s74 offen\n"
"s_add_u32 s74, s71, 16384\n"
"buffer_load_dwordx2 v[24:25], v18, s[16:19], s74 offen\n"
"s_add_u32 s74, s71, 24576\n"
"buffer_load_dwordx2 v[26:27], v18, s[16:19], s74 offen\n"
"s_add_u32 s74, s71, 65536\n"
"buffer_load_dwordx2 v[28:29], v18, s[16:19], s74 offen\n"
"s_add_u32 s74, s71, 73728\n"
"buffer_load_dwordx2 v[30:31], v18, s[16:19], s74 offen\n"
"s_add_u32 s74, s71, 81920\n"
"buffer_load_dwordx2 v[32:33], v18, s[16:19], s74 offen\n"
"s_add_u32 s74, s71, 90112\n"
"buffer_load_dwordx2 v[34:35], v18, s[16:19], s74 offen\n"
"s_add_u32 s74, s71, 262144\n"
"buffer_load_dwordx2 v[36:37], v18, s[16:19], s74 offen\n"
"s_add_u32 s74, s71, 270336\n"
"buffer_load_dwordx2 v[38:39], v18, s[16:19], s74 offen\n"
"s_add_u32 s74, s71, 278528\n"
"buffer_load_dwordx2 v[40:41], v18, s[16:19], s74 offen\n"
"s_add_u32 s74, s71, 286720\n"
"buffer_load_dwordx2 v[42:43], v18, s[16:19], s74 offen\n"
"s_add_u32 s74, s71, 327680\n"
"buffer_load_dwordx2 v[44:45], v18, s[16:19], s74 offen\n"
"s_add_u32 s74, s71, 335872\n"
"buffer_load_dwordx2 v[46:47], v18, s[16:19], s74 offen\n"
"s_add_u32 s74, s71, 344064\n"
"buffer_load_dwordx2 v[48:49], v18, s[16:19], s74 offen\n"
"s_add_u32 s74, s71, 352256\n"
"buffer_load_dwordx2 v[50:51], v18, s[16:19], s74 offen\n"
"s_waitcnt vmcnt(32)\n"
"v_perm_b32 v116, v54, v52, s47\n"
"v_perm_b32 v117, v62, v60, s47\n"
"v_perm_b32 v118, v58, v56, s47\n"
"v_perm_b32 v119, v66, v64, s47\n"
"v_perm_b32 v120, v70, v68, s47\n"
"v_perm_b32 v121, v78, v76, s47\n"
"v_perm_b32 v122, v74, v72, s47\n"
"v_perm_b32 v123, v82, v80, s47\n"
"v_accvgpr_write_b32 a104, v116\n"
"v_accvgpr_write_b32 a105, v117\n"
"v_accvgpr_write_b32 a106, v118\n"
"v_accvgpr_write_b32 a107, v119\n"
"v_accvgpr_write_b32 a108, v120\n"
"v_accvgpr_write_b32 a109, v121\n"
"v_accvgpr_write_b32 a110, v122\n"
"v_accvgpr_write_b32 a111, v123\n"
"v_perm_b32 v124, v54, v52, s48\n"
"v_perm_b32 v125, v62, v60, s48\n"
"v_perm_b32 v126, v58, v56, s48\n"
"v_perm_b32 v127, v66, v64, s48\n"
"v_perm_b32 v128, v70, v68, s48\n"
"v_perm_b32 v129, v78, v76, s48\n"
"v_perm_b32 v130, v74, v72, s48\n"
"v_perm_b32 v131, v82, v80, s48\n"
"v_accvgpr_write_b32 a112, v124\n"
"v_accvgpr_write_b32 a113, v125\n"
"v_accvgpr_write_b32 a114, v126\n"
"v_accvgpr_write_b32 a115, v127\n"
"v_accvgpr_write_b32 a116, v128\n"
"v_accvgpr_write_b32 a117, v129\n"
"v_accvgpr_write_b32 a118, v130\n"
"v_accvgpr_write_b32 a119, v131\n"
"v_perm_b32 v132, v55, v53, s47\n"
"v_perm_b32 v133, v63, v61, s47\n"
"v_perm_b32 v134, v59, v57, s47\n"
"v_perm_b32 v135, v67, v65, s47\n"
"v_perm_b32 v136, v71, v69, s47\n"
"v_perm_b32 v137, v79, v77, s47\n"
"v_perm_b32 v138, v75, v73, s47\n"
"v_perm_b32 v139, v83, v81, s47\n"
"v_accvgpr_write_b32 a120, v132\n"
"v_accvgpr_write_b32 a121, v133\n"
"v_accvgpr_write_b32 a122, v134\n"
"v_accvgpr_write_b32 a123, v135\n"
"v_accvgpr_write_b32 a124, v136\n"
"v_accvgpr_write_b32 a125, v137\n"
"v_accvgpr_write_b32 a126, v138\n"
"v_accvgpr_write_b32 a127, v139\n"
"v_perm_b32 v140, v55, v53, s48\n"
"v_perm_b32 v141, v63, v61, s48\n"
"v_perm_b32 v142, v59, v57, s48\n"
"v_perm_b32 v143, v67, v65, s48\n"
"v_perm_b32 v144, v71, v69, s48\n"
"v_perm_b32 v145, v79, v77, s48\n"
"v_perm_b32 v146, v75, v73, s48\n"
"v_perm_b32 v147, v83, v81, s48\n"
"ds_write_b128 v6, v[140:143] offset:2048\n"
"ds_write_b128 v6, v[144:147] offset:3072\n"
"s_waitcnt lgkmcnt(0)\n"
"s_mov_b32 s71, 2097152\n"
"s_add_u32 s74, s71, 0\n"
"buffer_load_dwordx2 v[52:53], v18, s[16:19], s74 offen\n"
"s_add_u32 s74, s71, 8192\n"
"buffer_load_dwordx2 v[54:55], v18, s[16:19], s74 offen\n"
"s_add_u32 s74, s71, 16384\n"
"buffer_load_dwordx2 v[56:57], v18, s[16:19], s74 offen\n"
"s_add_u32 s74, s71, 24576\n"
"buffer_load_dwordx2 v[58:59], v18, s[16:19], s74 offen\n"
"s_add_u32 s74, s71, 65536\n"
"buffer_load_dwordx2 v[60:61], v18, s[16:19], s74 offen\n"
"s_add_u32 s74, s71, 73728\n"
"buffer_load_dwordx2 v[62:63], v18, s[16:19], s74 offen\n"
"s_add_u32 s74, s71, 81920\n"
"buffer_load_dwordx2 v[64:65], v18, s[16:19], s74 offen\n"
"s_add_u32 s74, s71, 90112\n"
"buffer_load_dwordx2 v[66:67], v18, s[16:19], s74 offen\n"
"s_add_u32 s74, s71, 262144\n"
"buffer_load_dwordx2 v[68:69], v18, s[16:19], s74 offen\n"
"s_add_u32 s74, s71, 270336\n"
"buffer_load_dwordx2 v[70:71], v18, s[16:19], s74 offen\n"
"s_add_u32 s74, s71, 278528\n"
"buffer_load_dwordx2 v[72:73], v18, s[16:19], s74 offen\n"
"s_add_u32 s74, s71, 286720\n"
"buffer_load_dwordx2 v[74:75], v18, s[16:19], s74 offen\n"
"s_add_u32 s74, s71, 327680\n"
"buffer_load_dwordx2 v[76:77], v18, s[16:19], s74 offen\n"
"s_add_u32 s74, s71, 335872\n"
"buffer_load_dwordx2 v[78:79], v18, s[16:19], s74 offen\n"
"s_add_u32 s74, s71, 344064\n"
"buffer_load_dwordx2 v[80:81], v18, s[16:19], s74 offen\n"
"s_add_u32 s74, s71, 352256\n"
"buffer_load_dwordx2 v[82:83], v18, s[16:19], s74 offen\n"
"s_waitcnt vmcnt(32)\n"
"v_perm_b32 v116, v86, v84, s47\n"
"v_perm_b32 v117, v94, v92, s47\n"
"v_perm_b32 v118, v90, v88, s47\n"
"v_perm_b32 v119, v98, v96, s47\n"
"v_perm_b32 v120, v102, v100, s47\n"
"v_perm_b32 v121, v110, v108, s47\n"
"v_perm_b32 v122, v106, v104, s47\n"
"v_perm_b32 v123, v114, v112, s47\n"
"v_accvgpr_write_b32 a128, v116\n"
"v_accvgpr_write_b32 a129, v117\n"
"v_accvgpr_write_b32 a130, v118\n"
"v_accvgpr_write_b32 a131, v119\n"
"v_accvgpr_write_b32 a132, v120\n"
"v_accvgpr_write_b32 a133, v121\n"
"v_accvgpr_write_b32 a134, v122\n"
"v_accvgpr_write_b32 a135, v123\n"
"v_perm_b32 v124, v86, v84, s48\n"
"v_perm_b32 v125, v94, v92, s48\n"
"v_perm_b32 v126, v90, v88, s48\n"
"v_perm_b32 v127, v98, v96, s48\n"
"v_perm_b32 v128, v102, v100, s48\n"
"v_perm_b32 v129, v110, v108, s48\n"
"v_perm_b32 v130, v106, v104, s48\n"
"v_perm_b32 v131, v114, v112, s48\n"
"v_accvgpr_write_b32 a136, v124\n"
"v_accvgpr_write_b32 a137, v125\n"
"v_accvgpr_write_b32 a138, v126\n"
"v_accvgpr_write_b32 a139, v127\n"
"v_accvgpr_write_b32 a140, v128\n"
"v_accvgpr_write_b32 a141, v129\n"
"v_accvgpr_write_b32 a142, v130\n"
"v_accvgpr_write_b32 a143, v131\n"
"v_perm_b32 v132, v87, v85, s47\n"
"v_perm_b32 v133, v95, v93, s47\n"
"v_perm_b32 v134, v91, v89, s47\n"
"v_perm_b32 v135, v99, v97, s47\n"
"v_perm_b32 v136, v103, v101, s47\n"
"v_perm_b32 v137, v111, v109, s47\n"
"v_perm_b32 v138, v107, v105, s47\n"
"v_perm_b32 v139, v115, v113, s47\n"
"v_accvgpr_write_b32 a144, v132\n"
"v_accvgpr_write_b32 a145, v133\n"
"v_accvgpr_write_b32 a146, v134\n"
"v_accvgpr_write_b32 a147, v135\n"
"v_accvgpr_write_b32 a148, v136\n"
"v_accvgpr_write_b32 a149, v137\n"
"v_accvgpr_write_b32 a150, v138\n"
"v_accvgpr_write_b32 a151, v139\n"
"v_perm_b32 v140, v87, v85, s48\n"
"v_perm_b32 v141, v95, v93, s48\n"
"v_perm_b32 v142, v91, v89, s48\n"
"v_perm_b32 v143, v99, v97, s48\n"
"v_perm_b32 v144, v103, v101, s48\n"
"v_perm_b32 v145, v111, v109, s48\n"
"v_perm_b32 v146, v107, v105, s48\n"
"v_perm_b32 v147, v115, v113, s48\n"
"ds_write_b128 v6, v[140:143] offset:4096\n"
"ds_write_b128 v6, v[144:147] offset:5120\n"
"s_waitcnt lgkmcnt(0)\n"
"s_mov_b32 s71, 2621440\n"
"s_add_u32 s74, s71, 0\n"
"buffer_load_dwordx2 v[84:85], v18, s[16:19], s74 offen\n"
"s_add_u32 s74, s71, 8192\n"
"buffer_load_dwordx2 v[86:87], v18, s[16:19], s74 offen\n"
"s_add_u32 s74, s71, 16384\n"
"buffer_load_dwordx2 v[88:89], v18, s[16:19], s74 offen\n"
"s_add_u32 s74, s71, 24576\n"
"buffer_load_dwordx2 v[90:91], v18, s[16:19], s74 offen\n"
"s_add_u32 s74, s71, 65536\n"
"buffer_load_dwordx2 v[92:93], v18, s[16:19], s74 offen\n"
"s_add_u32 s74, s71, 73728\n"
"buffer_load_dwordx2 v[94:95], v18, s[16:19], s74 offen\n"
"s_add_u32 s74, s71, 81920\n"
"buffer_load_dwordx2 v[96:97], v18, s[16:19], s74 offen\n"
"s_add_u32 s74, s71, 90112\n"
"buffer_load_dwordx2 v[98:99], v18, s[16:19], s74 offen\n"
"s_add_u32 s74, s71, 262144\n"
"buffer_load_dwordx2 v[100:101], v18, s[16:19], s74 offen\n"
"s_add_u32 s74, s71, 270336\n"
"buffer_load_dwordx2 v[102:103], v18, s[16:19], s74 offen\n"
"s_add_u32 s74, s71, 278528\n"
"buffer_load_dwordx2 v[104:105], v18, s[16:19], s74 offen\n"
"s_add_u32 s74, s71, 286720\n"
"buffer_load_dwordx2 v[106:107], v18, s[16:19], s74 offen\n"
"s_add_u32 s74, s71, 327680\n"
"buffer_load_dwordx2 v[108:109], v18, s[16:19], s74 offen\n"
"s_add_u32 s74, s71, 335872\n"
"buffer_load_dwordx2 v[110:111], v18, s[16:19], s74 offen\n"
"s_add_u32 s74, s71, 344064\n"
"buffer_load_dwordx2 v[112:113], v18, s[16:19], s74 offen\n"
"s_add_u32 s74, s71, 352256\n"
"buffer_load_dwordx2 v[114:115], v18, s[16:19], s74 offen\n"
"s_waitcnt vmcnt(32)\n"
"v_perm_b32 v116, v22, v20, s47\n"
"v_perm_b32 v117, v30, v28, s47\n"
"v_perm_b32 v118, v26, v24, s47\n"
"v_perm_b32 v119, v34, v32, s47\n"
"v_perm_b32 v120, v38, v36, s47\n"
"v_perm_b32 v121, v46, v44, s47\n"
"v_perm_b32 v122, v42, v40, s47\n"
"v_perm_b32 v123, v50, v48, s47\n"
"v_accvgpr_write_b32 a152, v116\n"
"v_accvgpr_write_b32 a153, v117\n"
"v_accvgpr_write_b32 a154, v118\n"
"v_accvgpr_write_b32 a155, v119\n"
"v_accvgpr_write_b32 a156, v120\n"
"v_accvgpr_write_b32 a157, v121\n"
"v_accvgpr_write_b32 a158, v122\n"
"v_accvgpr_write_b32 a159, v123\n"
"v_perm_b32 v124, v22, v20, s48\n"
"v_perm_b32 v125, v30, v28, s48\n"
"v_perm_b32 v126, v26, v24, s48\n"
"v_perm_b32 v127, v34, v32, s48\n"
"v_perm_b32 v128, v38, v36, s48\n"
"v_perm_b32 v129, v46, v44, s48\n"
"v_perm_b32 v130, v42, v40, s48\n"
"v_perm_b32 v131, v50, v48, s48\n"
"v_accvgpr_write_b32 a160, v124\n"
"v_accvgpr_write_b32 a161, v125\n"
"v_accvgpr_write_b32 a162, v126\n"
"v_accvgpr_write_b32 a163, v127\n"
"v_accvgpr_write_b32 a164, v128\n"
"v_accvgpr_write_b32 a165, v129\n"
"v_accvgpr_write_b32 a166, v130\n"
"v_accvgpr_write_b32 a167, v131\n"
"v_perm_b32 v132, v23, v21, s47\n"
"v_perm_b32 v133, v31, v29, s47\n"
"v_perm_b32 v134, v27, v25, s47\n"
"v_perm_b32 v135, v35, v33, s47\n"
"v_perm_b32 v136, v39, v37, s47\n"
"v_perm_b32 v137, v47, v45, s47\n"
"v_perm_b32 v138, v43, v41, s47\n"
"v_perm_b32 v139, v51, v49, s47\n"
"v_accvgpr_write_b32 a168, v132\n"
"v_accvgpr_write_b32 a169, v133\n"
"v_accvgpr_write_b32 a170, v134\n"
"v_accvgpr_write_b32 a171, v135\n"
"v_accvgpr_write_b32 a172, v136\n"
"v_accvgpr_write_b32 a173, v137\n"
"v_accvgpr_write_b32 a174, v138\n"
"v_accvgpr_write_b32 a175, v139\n"
"v_perm_b32 v140, v23, v21, s48\n"
"v_perm_b32 v141, v31, v29, s48\n"
"v_perm_b32 v142, v27, v25, s48\n"
"v_perm_b32 v143, v35, v33, s48\n"
"v_perm_b32 v144, v39, v37, s48\n"
"v_perm_b32 v145, v47, v45, s48\n"
"v_perm_b32 v146, v43, v41, s48\n"
"v_perm_b32 v147, v51, v49, s48\n"
"ds_write_b128 v6, v[140:143] offset:6144\n"
"ds_write_b128 v6, v[144:147] offset:7168\n"
"s_waitcnt lgkmcnt(0)\n"
"s_mov_b32 s71, 3145728\n"
"s_add_u32 s74, s71, 0\n"
"buffer_load_dwordx2 v[20:21], v18, s[16:19], s74 offen\n"
"s_add_u32 s74, s71, 8192\n"
"buffer_load_dwordx2 v[22:23], v18, s[16:19], s74 offen\n"
"s_add_u32 s74, s71, 16384\n"
"buffer_load_dwordx2 v[24:25], v18, s[16:19], s74 offen\n"
"s_add_u32 s74, s71, 24576\n"
"buffer_load_dwordx2 v[26:27], v18, s[16:19], s74 offen\n"
"s_add_u32 s74, s71, 65536\n"
"buffer_load_dwordx2 v[28:29], v18, s[16:19], s74 offen\n"
"s_add_u32 s74, s71, 73728\n"
"buffer_load_dwordx2 v[30:31], v18, s[16:19], s74 offen\n"
"s_add_u32 s74, s71, 81920\n"
"buffer_load_dwordx2 v[32:33], v18, s[16:19], s74 offen\n"
"s_add_u32 s74, s71, 90112\n"
"buffer_load_dwordx2 v[34:35], v18, s[16:19], s74 offen\n"
"s_add_u32 s74, s71, 262144\n"
"buffer_load_dwordx2 v[36:37], v18, s[16:19], s74 offen\n"
"s_add_u32 s74, s71, 270336\n"
"buffer_load_dwordx2 v[38:39], v18, s[16:19], s74 offen\n"
"s_add_u32 s74, s71, 278528\n"
"buffer_load_dwordx2 v[40:41], v18, s[16:19], s74 offen\n"
"s_add_u32 s74, s71, 286720\n"
"buffer_load_dwordx2 v[42:43], v18, s[16:19], s74 offen\n"
"s_add_u32 s74, s71, 327680\n"
"buffer_load_dwordx2 v[44:45], v18, s[16:19], s74 offen\n"
"s_add_u32 s74, s71, 335872\n"
"buffer_load_dwordx2 v[46:47], v18, s[16:19], s74 offen\n"
"s_add_u32 s74, s71, 344064\n"
"buffer_load_dwordx2 v[48:49], v18, s[16:19], s74 offen\n"
"s_add_u32 s74, s71, 352256\n"
"buffer_load_dwordx2 v[50:51], v18, s[16:19], s74 offen\n"
"s_waitcnt vmcnt(32)\n"
"v_perm_b32 v116, v54, v52, s47\n"
"v_perm_b32 v117, v62, v60, s47\n"
"v_perm_b32 v118, v58, v56, s47\n"
"v_perm_b32 v119, v66, v64, s47\n"
"v_perm_b32 v120, v70, v68, s47\n"
"v_perm_b32 v121, v78, v76, s47\n"
"v_perm_b32 v122, v74, v72, s47\n"
"v_perm_b32 v123, v82, v80, s47\n"
"v_accvgpr_write_b32 a176, v116\n"
"v_accvgpr_write_b32 a177, v117\n"
"v_accvgpr_write_b32 a178, v118\n"
"v_accvgpr_write_b32 a179, v119\n"
"v_accvgpr_write_b32 a180, v120\n"
"v_accvgpr_write_b32 a181, v121\n"
"v_accvgpr_write_b32 a182, v122\n"
"v_accvgpr_write_b32 a183, v123\n"
"v_perm_b32 v124, v54, v52, s48\n"
"v_perm_b32 v125, v62, v60, s48\n"
"v_perm_b32 v126, v58, v56, s48\n"
"v_perm_b32 v127, v66, v64, s48\n"
"v_perm_b32 v128, v70, v68, s48\n"
"v_perm_b32 v129, v78, v76, s48\n"
"v_perm_b32 v130, v74, v72, s48\n"
"v_perm_b32 v131, v82, v80, s48\n"
"v_accvgpr_write_b32 a184, v124\n"
"v_accvgpr_write_b32 a185, v125\n"
"v_accvgpr_write_b32 a186, v126\n"
"v_accvgpr_write_b32 a187, v127\n"
"v_accvgpr_write_b32 a188, v128\n"
"v_accvgpr_write_b32 a189, v129\n"
"v_accvgpr_write_b32 a190, v130\n"
"v_accvgpr_write_b32 a191, v131\n"
"v_perm_b32 v132, v55, v53, s47\n"
"v_perm_b32 v133, v63, v61, s47\n"
"v_perm_b32 v134, v59, v57, s47\n"
"v_perm_b32 v135, v67, v65, s47\n"
"v_perm_b32 v136, v71, v69, s47\n"
"v_perm_b32 v137, v79, v77, s47\n"
"v_perm_b32 v138, v75, v73, s47\n"
"v_perm_b32 v139, v83, v81, s47\n"
"v_accvgpr_write_b32 a192, v132\n"
"v_accvgpr_write_b32 a193, v133\n"
"v_accvgpr_write_b32 a194, v134\n"
"v_accvgpr_write_b32 a195, v135\n"
"v_accvgpr_write_b32 a196, v136\n"
"v_accvgpr_write_b32 a197, v137\n"
"v_accvgpr_write_b32 a198, v138\n"
"v_accvgpr_write_b32 a199, v139\n"
"v_perm_b32 v140, v55, v53, s48\n"
"v_perm_b32 v141, v63, v61, s48\n"
"v_perm_b32 v142, v59, v57, s48\n"
"v_perm_b32 v143, v67, v65, s48\n"
"v_perm_b32 v144, v71, v69, s48\n"
"v_perm_b32 v145, v79, v77, s48\n"
"v_perm_b32 v146, v75, v73, s48\n"
"v_perm_b32 v147, v83, v81, s48\n"
"ds_write_b128 v6, v[140:143] offset:8192\n"
"ds_write_b128 v6, v[144:147] offset:9216\n"
"s_waitcnt lgkmcnt(0)\n"
"s_mov_b32 s71, 3670016\n"
"s_add_u32 s74, s71, 0\n"
"buffer_load_dwordx2 v[52:53], v18, s[16:19], s74 offen\n"
"s_add_u32 s74, s71, 8192\n"
"buffer_load_dwordx2 v[54:55], v18, s[16:19], s74 offen\n"
"s_add_u32 s74, s71, 16384\n"
"buffer_load_dwordx2 v[56:57], v18, s[16:19], s74 offen\n"
"s_add_u32 s74, s71, 24576\n"
"buffer_load_dwordx2 v[58:59], v18, s[16:19], s74 offen\n"
"s_add_u32 s74, s71, 65536\n"
"buffer_load_dwordx2 v[60:61], v18, s[16:19], s74 offen\n"
"s_add_u32 s74, s71, 73728\n"
"buffer_load_dwordx2 v[62:63], v18, s[16:19], s74 offen\n"
"s_add_u32 s74, s71, 81920\n"
"buffer_load_dwordx2 v[64:65], v18, s[16:19], s74 offen\n"
"s_add_u32 s74, s71, 90112\n"
"buffer_load_dwordx2 v[66:67], v18, s[16:19], s74 offen\n"
"s_add_u32 s74, s71, 262144\n"
"buffer_load_dwordx2 v[68:69], v18, s[16:19], s74 offen\n"
"s_add_u32 s74, s71, 270336\n"
"buffer_load_dwordx2 v[70:71], v18, s[16:19], s74 offen\n"
"s_add_u32 s74, s71, 278528\n"
"buffer_load_dwordx2 v[72:73], v18, s[16:19], s74 offen\n"
"s_add_u32 s74, s71, 286720\n"
"buffer_load_dwordx2 v[74:75], v18, s[16:19], s74 offen\n"
"s_add_u32 s74, s71, 327680\n"
"buffer_load_dwordx2 v[76:77], v18, s[16:19], s74 offen\n"
"s_add_u32 s74, s71, 335872\n"
"buffer_load_dwordx2 v[78:79], v18, s[16:19], s74 offen\n"
"s_add_u32 s74, s71, 344064\n"
"buffer_load_dwordx2 v[80:81], v18, s[16:19], s74 offen\n"
"s_add_u32 s74, s71, 352256\n"
"buffer_load_dwordx2 v[82:83], v18, s[16:19], s74 offen\n"
"s_waitcnt vmcnt(32)\n"
"v_perm_b32 v116, v86, v84, s47\n"
"v_perm_b32 v117, v94, v92, s47\n"
"v_perm_b32 v118, v90, v88, s47\n"
"v_perm_b32 v119, v98, v96, s47\n"
"v_perm_b32 v120, v102, v100, s47\n"
"v_perm_b32 v121, v110, v108, s47\n"
"v_perm_b32 v122, v106, v104, s47\n"
"v_perm_b32 v123, v114, v112, s47\n"
"v_accvgpr_write_b32 a200, v116\n"
"v_accvgpr_write_b32 a201, v117\n"
"v_accvgpr_write_b32 a202, v118\n"
"v_accvgpr_write_b32 a203, v119\n"
"v_accvgpr_write_b32 a204, v120\n"
"v_accvgpr_write_b32 a205, v121\n"
"v_accvgpr_write_b32 a206, v122\n"
"v_accvgpr_write_b32 a207, v123\n"
"v_perm_b32 v124, v86, v84, s48\n"
"v_perm_b32 v125, v94, v92, s48\n"
"v_perm_b32 v126, v90, v88, s48\n"
"v_perm_b32 v127, v98, v96, s48\n"
"v_perm_b32 v128, v102, v100, s48\n"
"v_perm_b32 v129, v110, v108, s48\n"
"v_perm_b32 v130, v106, v104, s48\n"
"v_perm_b32 v131, v114, v112, s48\n"
"v_accvgpr_write_b32 a208, v124\n"
"v_accvgpr_write_b32 a209, v125\n"
"v_accvgpr_write_b32 a210, v126\n"
"v_accvgpr_write_b32 a211, v127\n"
"v_accvgpr_write_b32 a212, v128\n"
"v_accvgpr_write_b32 a213, v129\n"
"v_accvgpr_write_b32 a214, v130\n"
"v_accvgpr_write_b32 a215, v131\n"
"v_perm_b32 v132, v87, v85, s47\n"
"v_perm_b32 v133, v95, v93, s47\n"
"v_perm_b32 v134, v91, v89, s47\n"
"v_perm_b32 v135, v99, v97, s47\n"
"v_perm_b32 v136, v103, v101, s47\n"
"v_perm_b32 v137, v111, v109, s47\n"
"v_perm_b32 v138, v107, v105, s47\n"
"v_perm_b32 v139, v115, v113, s47\n"
"v_accvgpr_write_b32 a216, v132\n"
"v_accvgpr_write_b32 a217, v133\n"
"v_accvgpr_write_b32 a218, v134\n"
"v_accvgpr_write_b32 a219, v135\n"
"v_accvgpr_write_b32 a220, v136\n"
"v_accvgpr_write_b32 a221, v137\n"
"v_accvgpr_write_b32 a222, v138\n"
"v_accvgpr_write_b32 a223, v139\n"
"v_perm_b32 v140, v87, v85, s48\n"
"v_perm_b32 v141, v95, v93, s48\n"
"v_perm_b32 v142, v91, v89, s48\n"
"v_perm_b32 v143, v99, v97, s48\n"
"v_perm_b32 v144, v103, v101, s48\n"
"v_perm_b32 v145, v111, v109, s48\n"
"v_perm_b32 v146, v107, v105, s48\n"
"v_perm_b32 v147, v115, v113, s48\n"
"ds_write_b128 v6, v[140:143] offset:10240\n"
"ds_write_b128 v6, v[144:147] offset:11264\n"
"s_waitcnt lgkmcnt(0)\n"
"s_mov_b32 s71, 4194304\n"
"s_add_u32 s74, s71, 0\n"
"buffer_load_dwordx2 v[84:85], v18, s[16:19], s74 offen\n"
"s_add_u32 s74, s71, 8192\n"
"buffer_load_dwordx2 v[86:87], v18, s[16:19], s74 offen\n"
"s_add_u32 s74, s71, 16384\n"
"buffer_load_dwordx2 v[88:89], v18, s[16:19], s74 offen\n"
"s_add_u32 s74, s71, 24576\n"
"buffer_load_dwordx2 v[90:91], v18, s[16:19], s74 offen\n"
"s_add_u32 s74, s71, 65536\n"
"buffer_load_dwordx2 v[92:93], v18, s[16:19], s74 offen\n"
"s_add_u32 s74, s71, 73728\n"
"buffer_load_dwordx2 v[94:95], v18, s[16:19], s74 offen\n"
"s_add_u32 s74, s71, 81920\n"
"buffer_load_dwordx2 v[96:97], v18, s[16:19], s74 offen\n"
"s_add_u32 s74, s71, 90112\n"
"buffer_load_dwordx2 v[98:99], v18, s[16:19], s74 offen\n"
"s_add_u32 s74, s71, 262144\n"
"buffer_load_dwordx2 v[100:101], v18, s[16:19], s74 offen\n"
"s_add_u32 s74, s71, 270336\n"
"buffer_load_dwordx2 v[102:103], v18, s[16:19], s74 offen\n"
"s_add_u32 s74, s71, 278528\n"
"buffer_load_dwordx2 v[104:105], v18, s[16:19], s74 offen\n"
"s_add_u32 s74, s71, 286720\n"
"buffer_load_dwordx2 v[106:107], v18, s[16:19], s74 offen\n"
"s_add_u32 s74, s71, 327680\n"
"buffer_load_dwordx2 v[108:109], v18, s[16:19], s74 offen\n"
"s_add_u32 s74, s71, 335872\n"
"buffer_load_dwordx2 v[110:111], v18, s[16:19], s74 offen\n"
"s_add_u32 s74, s71, 344064\n"
"buffer_load_dwordx2 v[112:113], v18, s[16:19], s74 offen\n"
"s_add_u32 s74, s71, 352256\n"
"buffer_load_dwordx2 v[114:115], v18, s[16:19], s74 offen\n"
"s_waitcnt vmcnt(32)\n"
"v_perm_b32 v116, v22, v20, s47\n"
"v_perm_b32 v117, v30, v28, s47\n"
"v_perm_b32 v118, v26, v24, s47\n"
"v_perm_b32 v119, v34, v32, s47\n"
"v_perm_b32 v120, v38, v36, s47\n"
"v_perm_b32 v121, v46, v44, s47\n"
"v_perm_b32 v122, v42, v40, s47\n"
"v_perm_b32 v123, v50, v48, s47\n"
"v_accvgpr_write_b32 a224, v116\n"
"v_accvgpr_write_b32 a225, v117\n"
"v_accvgpr_write_b32 a226, v118\n"
"v_accvgpr_write_b32 a227, v119\n"
"v_accvgpr_write_b32 a228, v120\n"
"v_accvgpr_write_b32 a229, v121\n"
"v_accvgpr_write_b32 a230, v122\n"
"v_accvgpr_write_b32 a231, v123\n"
"v_perm_b32 v124, v22, v20, s48\n"
"v_perm_b32 v125, v30, v28, s48\n"
"v_perm_b32 v126, v26, v24, s48\n"
"v_perm_b32 v127, v34, v32, s48\n"
"v_perm_b32 v128, v38, v36, s48\n"
"v_perm_b32 v129, v46, v44, s48\n"
"v_perm_b32 v130, v42, v40, s48\n"
"v_perm_b32 v131, v50, v48, s48\n"
"v_accvgpr_write_b32 a232, v124\n"
"v_accvgpr_write_b32 a233, v125\n"
"v_accvgpr_write_b32 a234, v126\n"
"v_accvgpr_write_b32 a235, v127\n"
"v_accvgpr_write_b32 a236, v128\n"
"v_accvgpr_write_b32 a237, v129\n"
"v_accvgpr_write_b32 a238, v130\n"
"v_accvgpr_write_b32 a239, v131\n"
"v_perm_b32 v132, v23, v21, s47\n"
"v_perm_b32 v133, v31, v29, s47\n"
"v_perm_b32 v134, v27, v25, s47\n"
"v_perm_b32 v135, v35, v33, s47\n"
"v_perm_b32 v136, v39, v37, s47\n"
"v_perm_b32 v137, v47, v45, s47\n"
"v_perm_b32 v138, v43, v41, s47\n"
"v_perm_b32 v139, v51, v49, s47\n"
"v_accvgpr_write_b32 a240, v132\n"
"v_accvgpr_write_b32 a241, v133\n"
"v_accvgpr_write_b32 a242, v134\n"
"v_accvgpr_write_b32 a243, v135\n"
"v_accvgpr_write_b32 a244, v136\n"
"v_accvgpr_write_b32 a245, v137\n"
"v_accvgpr_write_b32 a246, v138\n"
"v_accvgpr_write_b32 a247, v139\n"
"v_perm_b32 v140, v23, v21, s48\n"
"v_perm_b32 v141, v31, v29, s48\n"
"v_perm_b32 v142, v27, v25, s48\n"
"v_perm_b32 v143, v35, v33, s48\n"
"v_perm_b32 v144, v39, v37, s48\n"
"v_perm_b32 v145, v47, v45, s48\n"
"v_perm_b32 v146, v43, v41, s48\n"
"v_perm_b32 v147, v51, v49, s48\n"
"ds_write_b128 v6, v[140:143] offset:12288\n"
"ds_write_b128 v6, v[144:147] offset:13312\n"
"s_waitcnt lgkmcnt(0)\n"
"s_mov_b32 s71, 4718592\n"
"s_add_u32 s74, s71, 0\n"
"buffer_load_dwordx2 v[20:21], v18, s[16:19], s74 offen\n"
"s_add_u32 s74, s71, 8192\n"
"buffer_load_dwordx2 v[22:23], v18, s[16:19], s74 offen\n"
"s_add_u32 s74, s71, 16384\n"
"buffer_load_dwordx2 v[24:25], v18, s[16:19], s74 offen\n"
"s_add_u32 s74, s71, 24576\n"
"buffer_load_dwordx2 v[26:27], v18, s[16:19], s74 offen\n"
"s_add_u32 s74, s71, 65536\n"
"buffer_load_dwordx2 v[28:29], v18, s[16:19], s74 offen\n"
"s_add_u32 s74, s71, 73728\n"
"buffer_load_dwordx2 v[30:31], v18, s[16:19], s74 offen\n"
"s_add_u32 s74, s71, 81920\n"
"buffer_load_dwordx2 v[32:33], v18, s[16:19], s74 offen\n"
"s_add_u32 s74, s71, 90112\n"
"buffer_load_dwordx2 v[34:35], v18, s[16:19], s74 offen\n"
"s_add_u32 s74, s71, 262144\n"
"buffer_load_dwordx2 v[36:37], v18, s[16:19], s74 offen\n"
"s_add_u32 s74, s71, 270336\n"
"buffer_load_dwordx2 v[38:39], v18, s[16:19], s74 offen\n"
"s_add_u32 s74, s71, 278528\n"
"buffer_load_dwordx2 v[40:41], v18, s[16:19], s74 offen\n"
"s_add_u32 s74, s71, 286720\n"
"buffer_load_dwordx2 v[42:43], v18, s[16:19], s74 offen\n"
"s_add_u32 s74, s71, 327680\n"
"buffer_load_dwordx2 v[44:45], v18, s[16:19], s74 offen\n"
"s_add_u32 s74, s71, 335872\n"
"buffer_load_dwordx2 v[46:47], v18, s[16:19], s74 offen\n"
"s_add_u32 s74, s71, 344064\n"
"buffer_load_dwordx2 v[48:49], v18, s[16:19], s74 offen\n"
"s_add_u32 s74, s71, 352256\n"
"buffer_load_dwordx2 v[50:51], v18, s[16:19], s74 offen\n"
"s_waitcnt vmcnt(32)\n"
"v_perm_b32 v116, v54, v52, s47\n"
"v_perm_b32 v117, v62, v60, s47\n"
"v_perm_b32 v118, v58, v56, s47\n"
"v_perm_b32 v119, v66, v64, s47\n"
"v_perm_b32 v120, v70, v68, s47\n"
"v_perm_b32 v121, v78, v76, s47\n"
"v_perm_b32 v122, v74, v72, s47\n"
"v_perm_b32 v123, v82, v80, s47\n"
"v_accvgpr_write_b32 a248, v116\n"
"v_accvgpr_write_b32 a249, v117\n"
"v_accvgpr_write_b32 a250, v118\n"
"v_accvgpr_write_b32 a251, v119\n"
"v_accvgpr_write_b32 a252, v120\n"
"v_accvgpr_write_b32 a253, v121\n"
"v_accvgpr_write_b32 a254, v122\n"
"v_accvgpr_write_b32 a255, v123\n"
"v_perm_b32 v192, v54, v52, s48\n"
"v_perm_b32 v193, v62, v60, s48\n"
"v_perm_b32 v194, v58, v56, s48\n"
"v_perm_b32 v195, v66, v64, s48\n"
"v_perm_b32 v196, v70, v68, s48\n"
"v_perm_b32 v197, v78, v76, s48\n"
"v_perm_b32 v198, v74, v72, s48\n"
"v_perm_b32 v199, v82, v80, s48\n"
"v_perm_b32 v200, v55, v53, s47\n"
"v_perm_b32 v201, v63, v61, s47\n"
"v_perm_b32 v202, v59, v57, s47\n"
"v_perm_b32 v203, v67, v65, s47\n"
"v_perm_b32 v204, v71, v69, s47\n"
"v_perm_b32 v205, v79, v77, s47\n"
"v_perm_b32 v206, v75, v73, s47\n"
"v_perm_b32 v207, v83, v81, s47\n"
"v_perm_b32 v140, v55, v53, s48\n"
"v_perm_b32 v141, v63, v61, s48\n"
"v_perm_b32 v142, v59, v57, s48\n"
"v_perm_b32 v143, v67, v65, s48\n"
"v_perm_b32 v144, v71, v69, s48\n"
"v_perm_b32 v145, v79, v77, s48\n"
"v_perm_b32 v146, v75, v73, s48\n"
"v_perm_b32 v147, v83, v81, s48\n"
"ds_write_b128 v6, v[140:143] offset:14336\n"
"ds_write_b128 v6, v[144:147] offset:15360\n"
"s_waitcnt lgkmcnt(0)\n"
"s_waitcnt vmcnt(16)\n"
"v_perm_b32 v208, v86, v84, s47\n"
"v_perm_b32 v209, v94, v92, s47\n"
"v_perm_b32 v210, v90, v88, s47\n"
"v_perm_b32 v211, v98, v96, s47\n"
"v_perm_b32 v212, v102, v100, s47\n"
"v_perm_b32 v213, v110, v108, s47\n"
"v_perm_b32 v214, v106, v104, s47\n"
"v_perm_b32 v215, v114, v112, s47\n"
"v_perm_b32 v216, v86, v84, s48\n"
"v_perm_b32 v217, v94, v92, s48\n"
"v_perm_b32 v218, v90, v88, s48\n"
"v_perm_b32 v219, v98, v96, s48\n"
"v_perm_b32 v220, v102, v100, s48\n"
"v_perm_b32 v221, v110, v108, s48\n"
"v_perm_b32 v222, v106, v104, s48\n"
"v_perm_b32 v223, v114, v112, s48\n"
"v_perm_b32 v224, v87, v85, s47\n"
"v_perm_b32 v225, v95, v93, s47\n"
"v_perm_b32 v226, v91, v89, s47\n"
"v_perm_b32 v227, v99, v97, s47\n"
"v_perm_b32 v228, v103, v101, s47\n"
"v_perm_b32 v229, v111, v109, s47\n"
"v_perm_b32 v230, v107, v105, s47\n"
"v_perm_b32 v231, v115, v113, s47\n"
"v_perm_b32 v140, v87, v85, s48\n"
"v_perm_b32 v141, v95, v93, s48\n"
"v_perm_b32 v142, v91, v89, s48\n"
"v_perm_b32 v143, v99, v97, s48\n"
"v_perm_b32 v144, v103, v101, s48\n"
"v_perm_b32 v145, v111, v109, s48\n"
"v_perm_b32 v146, v107, v105, s48\n"
"v_perm_b32 v147, v115, v113, s48\n"
"ds_write_b128 v6, v[140:143] offset:16384\n"
"ds_write_b128 v6, v[144:147] offset:17408\n"
"s_waitcnt lgkmcnt(0)\n"
"s_waitcnt vmcnt(0)\n"
"v_perm_b32 v232, v22, v20, s47\n"
"v_perm_b32 v233, v30, v28, s47\n"
"v_perm_b32 v234, v26, v24, s47\n"
"v_perm_b32 v235, v34, v32, s47\n"
"v_perm_b32 v236, v38, v36, s47\n"
"v_perm_b32 v237, v46, v44, s47\n"
"v_perm_b32 v238, v42, v40, s47\n"
"v_perm_b32 v239, v50, v48, s47\n"
"v_perm_b32 v240, v22, v20, s48\n"
"v_perm_b32 v241, v30, v28, s48\n"
"v_perm_b32 v242, v26, v24, s48\n"
"v_perm_b32 v243, v34, v32, s48\n"
"v_perm_b32 v244, v38, v36, s48\n"
"v_perm_b32 v245, v46, v44, s48\n"
"v_perm_b32 v246, v42, v40, s48\n"
"v_perm_b32 v247, v50, v48, s48\n"
"v_perm_b32 v248, v23, v21, s47\n"
"v_perm_b32 v249, v31, v29, s47\n"
"v_perm_b32 v250, v27, v25, s47\n"
"v_perm_b32 v251, v35, v33, s47\n"
"v_perm_b32 v252, v39, v37, s47\n"
"v_perm_b32 v253, v47, v45, s47\n"
"v_perm_b32 v254, v43, v41, s47\n"
"v_perm_b32 v255, v51, v49, s47\n"
"v_perm_b32 v140, v23, v21, s48\n"
"v_perm_b32 v141, v31, v29, s48\n"
"v_perm_b32 v142, v27, v25, s48\n"
"v_perm_b32 v143, v35, v33, s48\n"
"v_perm_b32 v144, v39, v37, s48\n"
"v_perm_b32 v145, v47, v45, s48\n"
"v_perm_b32 v146, v43, v41, s48\n"
"v_perm_b32 v147, v51, v49, s48\n"
"ds_write_b128 v6, v[140:143] offset:18432\n"
"ds_write_b128 v6, v[144:147] offset:19456\n"
"s_waitcnt lgkmcnt(0)\n"
"s_lshr_b32 s71, s21, 2\n"
"s_add_u32 s71, s71, 64\n"
"s_and_b32 s72, s21, 3\n"
"s_cmp_eq_u32 s72, 0\n"
"s_cselect_b32 s71, s71, -1\n"
"s_cmp_eq_u32 s20, 1\n"
"s_cselect_b32 s71, s71, -1\n"
"s_cmp_eq_u32 s20, 0\n"
"s_cselect_b32 s71, s21, s71\n"
"s_lshl_b32 s72, s71, 14\n"
"s_lshl_b32 s73, s71, 7\n"
"s_add_u32 s74, s73, 8192\n"
"s_cmp_lt_u32 s71, 64\n"
"s_cselect_b32 s73, s73, s74\n"
"s_cmp_lt_u32 s71, 80\n"
"s_cselect_b32 s33, s72, s27\n"
"s_cselect_b32 s35, s73, s27\n"
"s_lshr_b32 s71, s21, 2\n"
"s_add_u32 s71, s71, 64\n"
"s_and_b32 s72, s21, 3\n"
"s_cmp_eq_u32 s72, 2\n"
"s_cselect_b32 s71, s71, -1\n"
"s_cmp_eq_u32 s20, 3\n"
"s_cselect_b32 s71, s71, -1\n"
"s_cmp_eq_u32 s20, 2\n"
"s_cselect_b32 s71, s21, s71\n"
"s_lshl_b32 s72, s71, 14\n"
"s_lshl_b32 s73, s71, 7\n"
"s_add_u32 s74, s73, 8192\n"
"s_cmp_lt_u32 s71, 64\n"
"s_cselect_b32 s73, s73, s74\n"
"s_cmp_lt_u32 s71, 80\n"
"s_cselect_b32 s34, s72, s27\n"
"s_cselect_b32 s36, s73, s27\n"
"s_lshl_b32 s71, s22, 14\n"
"s_lshl_b32 s39, s20, 10\n"
"s_add_u32 s71, s71, s39\n"
"s_add_u32 s37, s71, 10485760\n"
"s_lshl_b32 s72, s23, 12\n"
"s_add_u32 s38, s72, 524288\n"
"s_add_u32 s96, s39, 131072\n"
"s_cmp_lt_u32 s20, 2\n"
"s_cselect_b32 s53, 16384, s27\n"
"s_lshl_b32 s71, s23, 11\n"
"s_lshl_b32 s72, s20, 10\n"
"s_add_u32 s71, s71, s72\n"
"s_cmp_lt_u32 s20, 2\n"
"s_cselect_b32 s72, 0, 2048\n"
"s_add_u32 s71, s71, s72\n"
"s_lshl_b32 s72, s22, 14\n"
"s_add_u32 s71, s71, s72\n"
"s_add_u32 s40, s71, 11534336\n"
"s_lshl_b32 s41, s20, 17\n"
"s_lshr_b32 s68, s20, 1\n"
"s_add_u32 s68, s68, 8\n"
"s_sub_u32 s68, s68, s20\n"
"s_lshl_b32 s68, s68, 17\n"
"s_mov_b32 s18, s46\n"
"s_mov_b32 s19, s46\n"
"s_add_u32 s71, s21, 384\n"
"s_lshl_b32 s71, s71, 7\n"
"s_cmp_eq_u32 s20, 2\n"
"s_cselect_b32 s55, s71, s27\n"
"s_add_u32 s71, s71, 4\n"
"s_cmp_eq_u32 s20, 0\n"
"s_cselect_b32 s42, s71, s27\n"
"v_mbcnt_lo_u32_b32 v20, -1, 0\n"
"v_mbcnt_hi_u32_b32 v20, -1, v20\n"
"v_add_u32 v21, 16, v20\n"
"v_lshlrev_b32 v21, 7, v21\n"
"v_cmp_gt_u32 vcc, 32, v20\n"
"s_nop 1\n"
"v_cndmask_b32 v21, 0, v21, vcc\n"
"s_mov_b32 s25, 0\n"
"XP_wi_%=:\n"
"buffer_load_dwordx4 v[22:25], v21, s[12:15], 0 offen sc1\n"
"s_waitcnt vmcnt(0)\n"
"v_min3_u32 v26, v22, v23, v24\n"
"v_min_u32 v26, v26, v25\n"
"v_cmp_eq_u32 vcc, 0, v26\n"
"s_cbranch_vccz XP_wid_%=\n"
"s_sleep 2\n"
"s_add_u32 s25, s25, 1\n"
"s_cmp_lt_u32 s25, 200000\n"
"s_cbranch_scc1 XP_wi_%=\n"
"XP_wid_%=:\n"
"v_readlane_b32 s71, v22, 63\n"
"s_nop 0\n"
"s_bitcmp1_b32 s71, 5\n"
"s_cselect_b32 s72, 8192, 0\n"
"s_xor_b32 s73, s72, 8192\n"
"s_add_u32 s28, s72, 2097152\n"
"s_add_u32 s29, s73, 4194304\n"
"s_add_u32 s30, s72, 6291456\n"
"s_add_u32 s31, s73, 8388608\n"
"s_add_u32 s43, s72, 3407872\n"
"s_bitcmp1_b32 s71, 5\n"
"s_cselect_b32 s72, 8192, 0\n"
"s_add_u32 s37, s37, s72\n"
"s_add_u32 s40, s40, s72\n"
"s_add_u32 s42, s42, s73\n"
"s_add_u32 s55, s55, s73\n"
"v_add_u32 v5, s73, v5\n"
"s_barrier\n"
"s_mov_b32 s25, 0\n"
"s_xor_b32 s34, s33, s34\n"
"s_xor_b32 s36, s35, s36\n"
"s_xor_b32 s35, s35, s36\n"
"s_xor_b32 s55, s42, s55\n"
"s_mov_b32 s69, 16\n"
"s_mov_b32 s65, 0\n"
"s_mov_b32 s64, 0\n"
"s_mov_b32 s98, -1\n"
"s_mov_b32 s22, -8\n"
"s_mov_b32 s24, -2\n"
"s_mov_b32 s97, s27\n"
"s_mov_b32 s49, 0\n"
"s_mov_b32 s70, 0\n"
"v_mov_b64 v[76:77], 0\n"
"v_mov_b64 v[78:79], 0\n"
"s_cmp_lg_u32 s49, 0\n"
"s_cbranch_scc1 X_kvslow1_%=\n"
"X_kvslow_ret1_%=:\n"
"v_mfma_f32_32x32x16_bf16 v[60:75], v[76:79], v[76:79], 0\n"
"s_cmp_eq_u32 s70, 0\n"
"s_cbranch_scc1 X_light_%=\n"
"s_cmp_lt_u32 s97, s27\n"
"s_cbranch_scc1 X_bodyR_%=\n"
"s_branch X_bodyN_%=\n"
"X_bodyN_%=:\n"
"s_barrier\n"
"ds_read_b128 v[20:23], v2 offset:0\n"
"ds_read_b128 v[24:27], v2 offset:1024\n"
"ds_read_b128 v[28:31], v2 offset:2048\n"
"s_add_u32 s71, s98, 1\n"
"s_mov_b32 s98, s22\n"
"s_mov_b32 s22, s71\n"
"s_xor_b32 s69, s69, 16\n"
"s_mov_b32 s71, s65\n"
"s_mov_b32 s65, s64\n"
"s_waitcnt lgkmcnt(2)\n"
"v_smfmac_f32_16x16x64_bf16 v[60:63], v[20:23], a[80:87], v1\n"
"ds_read_b128 v[32:35], v2 offset:3072\n"
"s_mov_b32 s64, s71\n"
"v_smfmac_f32_16x16x64_bf16 v[64:67], v[20:23], a[88:95], v1\n"
"s_cmp_gt_u32 s65, s22\n"
"s_cselect_b32 s23, s27, s69\n"
"v_smfmac_f32_16x16x64_bf16 v[68:71], v[20:23], a[96:103], v1\n"
"buffer_load_dword v18, v5, s[12:15], s23 offen sc1\n"
"v_smfmac_f32_16x16x64_bf16 v[72:75], v[20:23], v[144:151], v1\n"
"ds_read_b128 v[144:147], v6 offset:4096\n"
"ds_read_b128 v[148:151], v6 offset:5120\n"
"v_smfmac_f32_16x16x64_bf16 v[76:79], v[20:23], a[0:7], v1\n"
"s_xor_b32 s42, s42, s55\n"
"s_sub_u32 s71, s22, 1\n"
"s_waitcnt lgkmcnt(4)\n"
"v_smfmac_f32_16x16x64_bf16 v[60:63], v[24:27], a[104:111], v1\n"
"ds_read_b128 v[36:39], v2 offset:4096\n"
"s_cmp_lt_u32 s71, 64\n"
"v_smfmac_f32_16x16x64_bf16 v[64:67], v[24:27], a[112:119], v1\n"
"s_cselect_b32 s67, s42, s27\n"
"v_mov_b32 v111, s22\n"
"v_smfmac_f32_16x16x64_bf16 v[68:71], v[24:27], a[120:127], v1\n"
"buffer_store_dword v111, off, s[12:15], s67 sc1\n"
"v_smfmac_f32_16x16x64_bf16 v[72:75], v[24:27], v[152:159], v1\n"
"ds_read_b128 v[152:155], v6 offset:6144\n"
"ds_read_b128 v[156:159], v6 offset:7168\n"
"v_smfmac_f32_16x16x64_bf16 v[76:79], v[24:27], a[8:15], v1\n"
"s_add_u32 s24, s24, 1\n"
"s_xor_b32 s33, s33, s34\n"
"s_waitcnt lgkmcnt(6)\n"
"v_smfmac_f32_16x16x64_bf16 v[60:63], v[28:31], a[128:135], v1\n"
"ds_read_b128 v[40:43], v2 offset:5120\n"
"s_xor_b32 s35, s35, s36\n"
"v_smfmac_f32_16x16x64_bf16 v[64:67], v[28:31], a[136:143], v1\n"
"s_bitcmp1_b32 s24, 0\n"
"s_cselect_b32 s71, s30, s28\n"
"v_smfmac_f32_16x16x64_bf16 v[68:71], v[28:31], a[144:151], v1\n"
"s_cselect_b32 s72, s31, s29\n"
"s_waitcnt lgkmcnt(4)\n"
"v_smfmac_f32_16x16x64_bf16 v[72:75], v[28:31], v[144:151], v1\n"
"ds_read_b128 v[144:147], v6 offset:8192\n"
"ds_read_b128 v[148:151], v6 offset:9216\n"
"v_smfmac_f32_16x16x64_bf16 v[76:79], v[28:31], a[16:23], v1\n"
"s_bitcmp1_b32 s24, 1\n"
"s_cselect_b32 s71, s72, s71\n"
"v_smfmac_f32_16x16x64_bf16 v[60:63], v[32:35], a[152:159], v1\n"
"ds_read_b128 v[44:47], v2 offset:6144\n"
"s_cmp_lt_u32 s98, 63\n"
"v_smfmac_f32_16x16x64_bf16 v[64:67], v[32:35], a[160:167], v1\n"
"s_cselect_b32 s71, s71, s27\n"
"s_add_u32 s97, s71, s33\n"
"v_smfmac_f32_16x16x64_bf16 v[68:71], v[32:35], a[168:175], v1\n"
"s_add_u32 s56, s71, s41\n"
"s_waitcnt lgkmcnt(4)\n"
"v_smfmac_f32_16x16x64_bf16 v[72:75], v[32:35], v[152:159], v1\n"
"ds_read_b128 v[152:155], v6 offset:10240\n"
"ds_read_b128 v[156:159], v6 offset:11264\n"
"v_smfmac_f32_16x16x64_bf16 v[76:79], v[32:35], a[24:31], v1\n"
"s_add_u32 s57, s56, 524288\n"
"s_add_u32 s58, s56, s68\n"
"v_smfmac_f32_16x16x64_bf16 v[60:63], v[36:39], a[176:183], v1\n"
"ds_read_b128 v[48:51], v2 offset:7168\n"
"s_add_u32 s72, s24, 1\n"
"v_smfmac_f32_16x16x64_bf16 v[64:67], v[36:39], a[184:191], v1\n"
"ds_read_b128 v[112:115], v8 offset:0\n"
"s_and_b32 s71, s72, 7\n"
"v_smfmac_f32_16x16x64_bf16 v[68:71], v[36:39], a[192:199], v1\n"
"s_lshl_b32 s71, s71, 15\n"
"s_waitcnt lgkmcnt(5)\n"
"v_smfmac_f32_16x16x64_bf16 v[72:75], v[36:39], v[144:151], v1\n"
"ds_read_b128 v[144:147], v6 offset:12288\n"
"ds_read_b128 v[148:151], v6 offset:13312\n"
"v_smfmac_f32_16x16x64_bf16 v[76:79], v[36:39], a[32:39], v1\n"
"s_add_u32 s71, s71, s43\n"
"s_and_b32 s66, s24, 31\n"
"v_smfmac_f32_16x16x64_bf16 v[60:63], v[40:43], a[200:207], v1\n"
"ds_read_b128 v[52:55], v2 offset:8192\n"
"s_lshl_b32 s66, s66, 22\n"
"v_smfmac_f32_16x16x64_bf16 v[64:67], v[40:43], a[208:215], v1\n"
"ds_read_b128 v[116:119], v8 offset:1024\n"
"s_add_u32 s66, s66, s40\n"
"v_smfmac_f32_16x16x64_bf16 v[68:71], v[40:43], a[216:223], v1\n"
"s_and_b32 s72, s72, 31\n"
"s_waitcnt lgkmcnt(6)\n"
"v_smfmac_f32_16x16x64_bf16 v[72:75], v[40:43], v[152:159], v1\n"
"ds_read_b128 v[152:155], v6 offset:14336\n"
"ds_read_b128 v[156:159], v6 offset:15360\n"
"v_smfmac_f32_16x16x64_bf16 v[76:79], v[40:43], a[40:47], v1\n"
"s_lshl_b32 s72, s72, 22\n"
"s_add_u32 s63, s72, s37\n"
"v_smfmac_f32_16x16x64_bf16 v[60:63], v[44:47], a[224:231], v1\n"
"ds_read_b128 v[56:59], v2 offset:9216\n"
"s_add_u32 s70, s22, 1\n"
"v_smfmac_f32_16x16x64_bf16 v[64:67], v[44:47], a[232:239], v1\n"
"ds_read_b128 v[120:123], v8 offset:2048\n"
"s_cmp_lt_u32 s22, 64\n"
"v_smfmac_f32_16x16x64_bf16 v[68:71], v[44:47], a[240:247], v1\n"
"s_cselect_b32 s71, s71, s27\n"
"s_waitcnt lgkmcnt(6)\n"
"v_smfmac_f32_16x16x64_bf16 v[72:75], v[44:47], v[144:151], v1\n"
"ds_read_b128 v[144:147], v6 offset:16384\n"
"ds_read_b128 v[148:151], v6 offset:17408\n"
"v_smfmac_f32_16x16x64_bf16 v[76:79], v[44:47], a[48:55], v1\n"
"s_cselect_b32 s63, s63, s27\n"
"s_cselect_b32 s70, s70, 0\n"
"v_smfmac_f32_16x16x64_bf16 v[60:63], v[48:51], a[248:255], v1\n"
"ds_read_b128 v[20:23], v8 offset:8192\n"
"s_cmp_gt_u32 s65, s70\n"
"v_smfmac_f32_16x16x64_bf16 v[64:67], v[48:51], v[192:199], v1\n"
"ds_read_b128 v[24:27], v8 offset:9216\n"
"s_cselect_b32 s23, s27, s69\n"
"v_smfmac_f32_16x16x64_bf16 v[68:71], v[48:51], v[200:207], v1\n"
"ds_read_b128 v[28:31], v8 offset:10240\n"
"s_waitcnt lgkmcnt(7)\n"
"v_smfmac_f32_16x16x64_bf16 v[72:75], v[48:51], v[152:159], v1\n"
"ds_read_b128 v[152:155], v6 offset:18432\n"
"ds_read_b128 v[156:159], v6 offset:19456\n"
"v_smfmac_f32_16x16x64_bf16 v[76:79], v[48:51], a[56:63], v1\n"
"s_add_u32 s50, s71, s39\n"
"s_add_u32 s51, s50, 4096\n"
"v_smfmac_f32_16x16x64_bf16 v[60:63], v[52:55], v[208:215], v1\n"
"ds_read_b128 v[32:35], v8 offset:11264\n"
"s_add_u32 s52, s50, s53\n"
"v_smfmac_f32_16x16x64_bf16 v[64:67], v[52:55], v[216:223], v1\n"
"ds_read_b128 v[124:127], v8 offset:3072\n"
"v_smfmac_f32_16x16x64_bf16 v[68:71], v[52:55], v[224:231], v1\n"
"ds_read_b128 v[128:131], v8 offset:4096\n"
"s_waitcnt lgkmcnt(8)\n"
"v_smfmac_f32_16x16x64_bf16 v[72:75], v[52:55], v[144:151], v1\n"
"v_smfmac_f32_16x16x64_bf16 v[76:79], v[52:55], a[64:71], v1\n"
"v_smfmac_f32_16x16x64_bf16 v[76:79], v[56:59], a[72:79], v1\n"
"ds_read_b128 v[132:135], v8 offset:5120\n"
"v_smfmac_f32_16x16x64_bf16 v[60:63], v[56:59], v[232:239], v1\n"
"ds_read_b128 v[136:139], v8 offset:6144\n"
"v_smfmac_f32_16x16x64_bf16 v[64:67], v[56:59], v[240:247], v1\n"
"ds_read_b128 v[140:143], v8 offset:7168\n"
"v_smfmac_f32_16x16x64_bf16 v[68:71], v[56:59], v[248:255], v1\n"
"s_waitcnt lgkmcnt(6)\n"
"v_smfmac_f32_16x16x64_bf16 v[72:75], v[56:59], v[152:159], v1\n"
"v_pk_add_f32 v[76:77], v[76:77], v[78:79]\n"
"v_cvt_pk_bf16_f32 v76, v76, v77\n"
"ds_write_b32 v9, v76\n"
"v_pk_add_f32 v[60:61], v[60:61], v[62:63]\n"
"v_pk_add_f32 v[64:65], v[64:65], v[66:67]\n"
"v_cvt_pk_bf16_f32 v62, v60, v64\n"
"v_pk_add_f32 v[68:69], v[68:69], v[70:71]\n"
"v_cvt_pk_bf16_f32 v66, v61, v65\n"
"v_pk_add_f32 v[72:73], v[72:73], v[74:75]\n"
"v_cvt_pk_bf16_f32 v63, v68, v72\n"
"ds_write_b64 v10, v[62:63]\n"
"v_cvt_pk_bf16_f32 v67, v69, v73\n"
"ds_write_b64 v10, v[66:67] offset:288\n"
"v_mfma_f32_16x16x32_bf16 v[68:71], v[14:17], v[14:17], v[68:71]\n"
"v_mfma_f32_16x16x32_bf16 v[68:71], v[14:17], v[14:17], v[68:71]\n"
"v_mfma_f32_16x16x32_bf16 v[68:71], v[14:17], v[14:17], v[68:71]\n"
"s_waitcnt lgkmcnt(0)\n"
"s_barrier\n"
"ds_read_b128 v[144:147], v11 offset:0\n"
"ds_read_b128 v[148:151], v11 offset:2304\n"
"ds_read_b128 v[152:155], v11 offset:64\n"
"ds_read_b128 v[156:159], v11 offset:2368\n"
"v_mfma_f32_16x16x32_bf16 v[68:71], v[14:17], v[14:17], v[68:71]\n"
"ds_read_b128 v[36:39], v11 offset:128\n"
"v_mfma_f32_16x16x32_bf16 v[68:71], v[14:17], v[14:17], v[68:71]\n"
"ds_read_b128 v[40:43], v11 offset:2432\n"
"v_mfma_f32_16x16x32_bf16 v[68:71], v[14:17], v[14:17], v[68:71]\n"
"ds_read_b128 v[44:47], v11 offset:192\n"
"v_mfma_f32_16x16x32_bf16 v[68:71], v[14:17], v[14:17], v[68:71]\n"
"ds_read_b128 v[48:51], v11 offset:2496\n"
"s_waitcnt lgkmcnt(6)\n"
"v_mfma_f32_16x16x32_bf16 v[60:63], v[148:151], v[144:147], 0\n"
"ds_read_b128 v[52:55], v8 offset:16896\n"
"ds_read_b128 v[56:59], v8 offset:17920\n"
"v_mfma_f32_16x16x32_bf16 v[64:67], v[112:115], v[144:147], 0\n"
"ds_read_b128 v[76:79], v13\n"
"s_cmp_lt_u32 s65, s70\n"
"s_cbranch_scc1 X_chkN_%=\n"
"X_chk_retN_%=:\n"
"v_mfma_f32_16x16x32_bf16 v[68:71], v[116:119], v[144:147], 0\n"
"s_or_b32 s76, s63, s49\n"
"s_add_u32 s77, s76, 4096\n"
"s_waitcnt lgkmcnt(7)\n"
"v_mfma_f32_16x16x32_bf16 v[60:63], v[156:159], v[152:155], v[60:63]\n"
"s_add_u32 s78, s76, s38\n"
"s_add_u32 m0, s96, 0\n"
"v_mfma_f32_16x16x32_bf16 v[64:67], v[120:123], v[152:155], v[64:67]\n"
"buffer_load_dwordx4 v0, s[12:15], s76 offen sc1 lds\n"
"s_add_u32 m0, s96, 4096\n"
"v_mfma_f32_16x16x32_bf16 v[68:71], v[124:127], v[152:155], v[68:71]\n"
"buffer_load_dwordx4 v0, s[12:15], s77 offen sc1 lds\n"
"s_add_u32 m0, s96, 8192\n"
"s_waitcnt lgkmcnt(5)\n"
"v_mfma_f32_16x16x32_bf16 v[60:63], v[40:43], v[36:39], v[60:63]\n"
"buffer_load_dwordx4 v0, s[12:15], s78 offen sc1 lds\n"
"s_waitcnt lgkmcnt(0)\n"
"v_mfma_f32_16x16x32_bf16 v[64:67], v[128:131], v[36:39], v[64:67]\n"
"buffer_store_dwordx4 v[76:79], v0, s[12:15], s66 offen sc1\n"
"buffer_load_dword v18, v5, s[12:15], s23 offen sc1\n"
"v_mfma_f32_16x16x32_bf16 v[68:71], v[132:135], v[36:39], v[68:71]\n"
"v_mfma_f32_16x16x32_bf16 v[60:63], v[48:51], v[44:47], v[60:63]\n"
"v_mfma_f32_16x16x32_bf16 v[64:67], v[136:139], v[44:47], v[64:67]\n"
"v_mfma_f32_16x16x32_bf16 v[68:71], v[140:143], v[44:47], v[68:71]\n"
"v_mov_b64 v[84:85], 0\n"
"v_mov_b64 v[86:87], 0\n"
"v_mov_b64 v[80:81], 0\n"
"v_mov_b32 v83, 0\n"
"s_nop 2\n"
"v_max3_f32 v42, v60, v61, v64\n"
"v_max3_f32 v40, v65, v66, v67\n"
"v_max3_f32 v41, v68, v69, v70\n"
"v_max3_f32 v42, v42, v40, v71\n"
"v_max_f32 v42, v42, v41\n"
"v_mov_b32 v40, v42\n"
"v_mfma_f32_32x32x16_bf16 v[96:111], v[84:87], v[84:87], 0\n"
"ds_read_b128 v[112:115], v7 offset:0\n"
"v_permlane32_swap_b32 v42, v40\n"
"v_max_f32 v42, v42, v40\n"
"v_mov_b32 v40, v42\n"
"ds_read_b128 v[116:119], v7 offset:1024\n"
"v_mfma_f32_32x32x16_bf16 v[144:159], v[84:87], v[84:87], 0\n"
"v_permlane16_swap_b32 v42, v40\n"
"v_max_f32 v42, v42, v40\n"
"v_mul_f32 v42, s46, v42\n"
"v_pk_fma_f32 v[60:61], v[60:61], s[18:19], v[42:43] op_sel_hi:[1,1,0] neg_lo:[0,0,1] neg_hi:[0,0,1]\n"
"v_pk_fma_f32 v[64:65], v[64:65], s[18:19], v[42:43] op_sel_hi:[1,1,0] neg_lo:[0,0,1] neg_hi:[0,0,1]\n"
"v_exp_f32 v60, v60\n"
"v_exp_f32 v61, v61\n"
"v_pk_fma_f32 v[66:67], v[66:67], s[18:19], v[42:43] op_sel_hi:[1,1,0] neg_lo:[0,0,1] neg_hi:[0,0,1]\n"
"v_exp_f32 v64, v64\n"
"v_exp_f32 v65, v65\n"
"v_cvt_pk_bf16_f32 v82, v60, v61\n"
"v_pk_fma_f32 v[68:69], v[68:69], s[18:19], v[42:43] op_sel_hi:[1,1,0] neg_lo:[0,0,1] neg_hi:[0,0,1]\n"
"v_exp_f32 v66, v66\n"
"v_mfma_f32_16x16x32_bf16 v[44:47], v[52:55], v[82:85], 0\n"
"v_exp_f32 v67, v67\n"
"v_cvt_pk_bf16_f32 v36, v64, v65\n"
"v_pk_fma_f32 v[70:71], v[70:71], s[18:19], v[42:43] op_sel_hi:[1,1,0] neg_lo:[0,0,1] neg_hi:[0,0,1]\n"
"v_mfma_f32_16x16x32_bf16 v[48:51], v[52:55], v[80:83], 0\n"
"v_exp_f32 v68, v68\n"
"v_exp_f32 v69, v69\n"
"v_mfma_f32_16x16x32_bf16 v[88:91], v[56:59], v[82:85], 0\n"
"v_cvt_pk_bf16_f32 v37, v66, v67\n"
"v_exp_f32 v70, v70\n"
"v_mfma_f32_16x16x32_bf16 v[92:95], v[56:59], v[80:83], 0\n"
"v_exp_f32 v71, v71\n"
"v_cvt_pk_bf16_f32 v38, v68, v69\n"
"v_cvt_pk_bf16_f32 v39, v70, v71\n"
"v_pk_add_f32 v[60:61], v[60:61], v[64:65]\n"
"v_pk_add_f32 v[66:67], v[66:67], v[68:69]\n"
"v_mfma_f32_16x16x32_bf16 v[44:47], v[20:23], v[36:39], v[44:47]\n"
"v_pk_add_f32 v[60:61], v[60:61], v[70:71]\n"
"v_pk_add_f32 v[60:61], v[60:61], v[66:67]\n"
"v_mfma_f32_16x16x32_bf16 v[88:91], v[28:31], v[36:39], v[88:91]\n"
"v_add_f32 v40, v60, v61\n"
"v_mov_b32 v42, v40\n"
"v_mfma_f32_16x16x32_bf16 v[48:51], v[24:27], v[36:39], v[48:51]\n"
"ds_read_b128 v[120:123], v7 offset:2048\n"
"v_permlane32_swap_b32 v40, v42\n"
"v_mfma_f32_16x16x32_bf16 v[92:95], v[32:35], v[36:39], v[92:95]\n"
"v_add_f32 v40, v40, v42\n"
"v_mov_b32 v42, v40\n"
"ds_read_b128 v[124:127], v7 offset:3072\n"
"s_nop 0\n"
"v_permlane16_swap_b32 v40, v42\n"
"v_add_f32 v40, v40, v42\n"
"v_rcp_f32 v40, v40\n"
"v_cndmask_b32 v44, v44, v88, s[44:45]\n"
"v_cndmask_b32 v45, v45, v89, s[44:45]\n"
"v_cndmask_b32 v46, v46, v90, s[44:45]\n"
"v_cndmask_b32 v47, v47, v91, s[44:45]\n"
"ds_read_b128 v[128:131], v7 offset:4096\n"
"v_cndmask_b32 v48, v48, v92, s[44:45]\n"
"v_cndmask_b32 v49, v49, v93, s[44:45]\n"
"v_cndmask_b32 v50, v50, v94, s[44:45]\n"
"v_cndmask_b32 v51, v51, v95, s[44:45]\n"
"ds_read_b128 v[132:135], v7 offset:5120\n"
"v_pk_mul_f32 v[44:45], v[44:45], v[40:41] op_sel_hi:[1,0]\n"
"v_pk_mul_f32 v[46:47], v[46:47], v[40:41] op_sel_hi:[1,0]\n"
"v_pk_mul_f32 v[48:49], v[48:49], v[40:41] op_sel_hi:[1,0]\n"
"v_pk_mul_f32 v[50:51], v[50:51], v[40:41] op_sel_hi:[1,0]\n"
"v_cvt_pk_bf16_f32 v76, v44, v45\n"
"v_cvt_pk_bf16_f32 v77, v46, v47\n"
"v_cvt_pk_bf16_f32 v78, v48, v49\n"
"v_cvt_pk_bf16_f32 v79, v50, v51\n"
"ds_read_b128 v[136:139], v7 offset:6144\n"
"ds_read_b128 v[140:143], v7 offset:7168\n"
"s_add_u32 m0, s96, 18944\n"
"s_nop 0\n"
"buffer_load_dwordx4 v0, s[12:15], s50 offen sc1 lds\n"
"s_add_u32 m0, s96, 23040\n"
"s_nop 0\n"
"buffer_load_dwordx4 v0, s[12:15], s51 offen sc1 lds\n"
"s_add_u32 m0, s96, 27136\n"
"s_nop 0\n"
"buffer_load_dwordx4 v0, s[12:15], s52 offen sc1 lds\n"
"v_smfmac_f32_16x16x64_bf16 v[96:99], v[76:79], v[160:167], v1\n"
"v_smfmac_f32_16x16x64_bf16 v[100:103], v[76:79], v[168:175], v1\n"
"ds_read_b128 v[80:83], v7 offset:8192\n"
"v_mov_b64 v[60:61], 0\n"
"v_smfmac_f32_16x16x64_bf16 v[104:107], v[76:79], v[176:183], v1\n"
"ds_read_b128 v[84:87], v7 offset:9216\n"
"v_mov_b64 v[62:63], 0\n"
"v_smfmac_f32_16x16x64_bf16 v[108:111], v[76:79], v[184:191], v1\n"
"ds_read_b128 v[88:91], v7 offset:10240\n"
"s_waitcnt lgkmcnt(3)\n"
"v_smfmac_f32_16x16x64_bf16 v[144:147], v[76:79], v[112:119], v1\n"
"ds_read_b128 v[92:95], v7 offset:11264\n"
"v_mov_b64 v[64:65], 0\n"
"v_smfmac_f32_16x16x64_bf16 v[148:151], v[76:79], v[120:127], v1\n"
"v_mov_b64 v[66:67], 0\n"
"v_pk_add_f32 v[98:99], v[96:97], v[98:99]\n"
"v_smfmac_f32_16x16x64_bf16 v[152:155], v[76:79], v[128:135], v1\n"
"v_pk_add_f32 v[100:101], v[100:101], v[102:103]\n"
"v_pk_add_f32 v[104:105], v[104:105], v[106:107]\n"
"v_smfmac_f32_16x16x64_bf16 v[156:159], v[76:79], v[136:143], v1\n"
"v_pk_add_f32 v[108:109], v[108:109], v[110:111]\n"
"v_cvt_pk_bf16_f32 v96, v98, v100\n"
"s_waitcnt lgkmcnt(0)\n"
"v_smfmac_f32_16x16x64_bf16 v[60:63], v[76:79], v[80:87], v1\n"
"v_cvt_pk_bf16_f32 v97, v104, v108\n"
"v_cvt_pk_bf16_f32 v98, v99, v101\n"
"v_smfmac_f32_16x16x64_bf16 v[64:67], v[76:79], v[88:95], v1\n"
"v_cvt_pk_bf16_f32 v99, v105, v109\n"
"buffer_store_dwordx4 v[96:99], v3, s[12:15], s56 offen sc1\n"
"v_pk_add_f32 v[146:147], v[144:145], v[146:147]\n"
"v_pk_add_f32 v[148:149], v[148:149], v[150:151]\n"
"v_pk_add_f32 v[152:153], v[152:153], v[154:155]\n"
"v_pk_add_f32 v[156:157], v[156:157], v[158:159]\n"
"v_cvt_pk_bf16_f32 v144, v146, v148\n"
"v_cvt_pk_bf16_f32 v145, v152, v156\n"
"v_cvt_pk_bf16_f32 v146, v147, v149\n"
"v_cvt_pk_bf16_f32 v147, v153, v157\n"
"buffer_store_dwordx4 v[144:147], v3, s[12:15], s57 offen sc1\n"
"v_pk_add_f32 v[60:61], v[60:61], v[62:63]\n"
"v_pk_add_f32 v[64:65], v[64:65], v[66:67]\n"
"ds_read_b128 v[152:155], v6 offset:2048\n"
"ds_read_b128 v[156:159], v6 offset:3072\n"
"v_cvt_pk_bf16_f32 v60, v60, v64\n"
"v_cvt_pk_bf16_f32 v61, v61, v65\n"
"buffer_store_dwordx2 v[60:61], v19, s[12:15], s58 offen sc1\n"
"ds_read_b128 v[144:147], v6 offset:0\n"
"ds_read_b128 v[148:151], v6 offset:1024\n"
"v_mov_b64 v[76:77], 0\n"
"v_mov_b64 v[78:79], 0\n"
"s_cmp_lg_u32 s49, 0\n"
"s_cbranch_scc1 X_kvslow2_%=\n"
"X_kvslow_ret2_%=:\n"
"v_mfma_f32_32x32x16_bf16 v[60:75], v[76:79], v[76:79], 0\n"
"s_waitcnt vmcnt(3)\n"
"ds_read_b128 v[112:115], v12 offset:18944\n"
"ds_read_b128 v[116:119], v12 offset:23040\n"
"ds_read_b128 v[120:123], v12 offset:27136\n"
"v_min_u32_dpp v18, v18, v18 quad_perm:[1,0,3,2] row_mask:0xf bank_mask:0xf\n"
"s_nop 1\n"
"v_min_u32_dpp v18, v18, v18 quad_perm:[2,3,0,1] row_mask:0xf bank_mask:0xf\n"
"s_nop 1\n"
"v_min_u32_dpp v18, v18, v18 row_half_mirror row_mask:0xf bank_mask:0xf\n"
"s_nop 0\n"
"v_readfirstlane_b32 s72, v18\n"
"s_nop 0\n"
"s_max_u32 s65, s65, s72\n"
"s_waitcnt lgkmcnt(0)\n"
"v_max3_u32 v124, v112, v113, v114\n"
"v_max3_u32 v125, v115, v116, v117\n"
"v_max3_u32 v124, v124, v118, v119\n"
"v_max3_u32 v125, v125, v120, v121\n"
"v_max3_u32 v124, v124, v122, v123\n"
"v_max_u32 v124, v124, v125\n"
"v_cmp_eq_u32 vcc, -1, v124\n"
"s_cbranch_vccnz X_mslow2_%=\n"
"X_mslow_ret2_%=:\n"
"s_cmp_eq_u32 s70, 0\n"
"s_cbranch_scc1 X_light_%=\n"
"s_cmp_lt_u32 s97, s27\n"
"s_cbranch_scc1 X_bodyR_%=\n"
"s_branch X_bodyN_%=\n"
"X_chkN_%=:\n"
"s_waitcnt vmcnt(1)\n"
"v_cmp_gt_u32 s[72:73], s70, v18\n"
"s_nop 0\n"
"s_and_b32 s72, s72, 0xff\n"
"s_cselect_b32 s49, s27, 0\n"
"s_branch X_chk_retN_%=\n"
"X_bodyZ_%=:\n"
"s_barrier\n"
"ds_read_b128 v[20:23], v2 offset:0\n"
"ds_read_b128 v[24:27], v2 offset:1024\n"
"ds_read_b128 v[28:31], v2 offset:2048\n"
"s_add_u32 s71, s98, 1\n"
"s_mov_b32 s98, s22\n"
"s_mov_b32 s22, s71\n"
"s_xor_b32 s69, s69, 16\n"
"s_mov_b32 s71, s65\n"
"s_mov_b32 s65, s64\n"
"s_waitcnt lgkmcnt(2)\n"
"v_smfmac_f32_16x16x64_bf16 v[60:63], v[20:23], a[80:87], v1\n"
"ds_read_b128 v[32:35], v2 offset:3072\n"
"s_mov_b32 s64, s71\n"
"v_smfmac_f32_16x16x64_bf16 v[64:67], v[20:23], a[88:95], v1\n"
"s_cmp_gt_u32 s65, s22\n"
"s_cselect_b32 s23, s27, s69\n"
"v_smfmac_f32_16x16x64_bf16 v[68:71], v[20:23], a[96:103], v1\n"
"buffer_load_dword v18, v5, s[12:15], s23 offen sc1\n"
"v_smfmac_f32_16x16x64_bf16 v[72:75], v[20:23], v[144:151], v1\n"
"ds_read_b128 v[144:147], v6 offset:4096\n"
"ds_read_b128 v[148:151], v6 offset:5120\n"
"v_smfmac_f32_16x16x64_bf16 v[76:79], v[20:23], a[0:7], v1\n"
"s_xor_b32 s42, s42, s55\n"
"s_sub_u32 s71, s22, 1\n"
"s_waitcnt lgkmcnt(4)\n"
"v_smfmac_f32_16x16x64_bf16 v[60:63], v[24:27], a[104:111], v1\n"
"ds_read_b128 v[36:39], v2 offset:4096\n"
"s_cmp_lt_u32 s71, 64\n"
"v_smfmac_f32_16x16x64_bf16 v[64:67], v[24:27], a[112:119], v1\n"
"s_cselect_b32 s67, s42, s27\n"
"v_mov_b32 v111, s22\n"
"v_smfmac_f32_16x16x64_bf16 v[68:71], v[24:27], a[120:127], v1\n"
"buffer_store_dword v111, off, s[12:15], s67 sc1\n"
"v_smfmac_f32_16x16x64_bf16 v[72:75], v[24:27], v[152:159], v1\n"
"ds_read_b128 v[152:155], v6 offset:6144\n"
"ds_read_b128 v[156:159], v6 offset:7168\n"
"v_smfmac_f32_16x16x64_bf16 v[76:79], v[24:27], a[8:15], v1\n"
"s_add_u32 s24, s24, 1\n"
"s_xor_b32 s33, s33, s34\n"
"s_waitcnt lgkmcnt(6)\n"
"v_smfmac_f32_16x16x64_bf16 v[60:63], v[28:31], a[128:135], v1\n"
"ds_read_b128 v[40:43], v2 offset:5120\n"
"s_xor_b32 s35, s35, s36\n"
"v_smfmac_f32_16x16x64_bf16 v[64:67], v[28:31], a[136:143], v1\n"
"s_bitcmp1_b32 s24, 0\n"
"s_cselect_b32 s71, s30, s28\n"
"v_smfmac_f32_16x16x64_bf16 v[68:71], v[28:31], a[144:151], v1\n"
"s_cselect_b32 s72, s31, s29\n"
"s_waitcnt lgkmcnt(4)\n"
"v_smfmac_f32_16x16x64_bf16 v[72:75], v[28:31], v[144:151], v1\n"
"ds_read_b128 v[144:147], v6 offset:8192\n"
"ds_read_b128 v[148:151], v6 offset:9216\n"
"v_smfmac_f32_16x16x64_bf16 v[76:79], v[28:31], a[16:23], v1\n"
"s_bitcmp1_b32 s24, 1\n"
"s_cselect_b32 s71, s72, s71\n"
"v_smfmac_f32_16x16x64_bf16 v[60:63], v[32:35], a[152:159], v1\n"
"ds_read_b128 v[44:47], v2 offset:6144\n"
"s_cmp_lt_u32 s98, 63\n"
"v_smfmac_f32_16x16x64_bf16 v[64:67], v[32:35], a[160:167], v1\n"
"s_cselect_b32 s71, s71, s27\n"
"s_add_u32 s97, s71, s33\n"
"v_smfmac_f32_16x16x64_bf16 v[68:71], v[32:35], a[168:175], v1\n"
"s_add_u32 s56, s71, s41\n"
"s_waitcnt lgkmcnt(4)\n"
"v_smfmac_f32_16x16x64_bf16 v[72:75], v[32:35], v[152:159], v1\n"
"ds_read_b128 v[152:155], v6 offset:10240\n"
"ds_read_b128 v[156:159], v6 offset:11264\n"
"v_smfmac_f32_16x16x64_bf16 v[76:79], v[32:35], a[24:31], v1\n"
"s_add_u32 s57, s56, 524288\n"
"s_add_u32 s58, s56, s68\n"
"v_smfmac_f32_16x16x64_bf16 v[60:63], v[36:39], a[176:183], v1\n"
"ds_read_b128 v[48:51], v2 offset:7168\n"
"s_add_u32 s72, s24, 1\n"
"v_smfmac_f32_16x16x64_bf16 v[64:67], v[36:39], a[184:191], v1\n"
"ds_read_b128 v[112:115], v8 offset:0\n"
"s_and_b32 s71, s72, 7\n"
"v_smfmac_f32_16x16x64_bf16 v[68:71], v[36:39], a[192:199], v1\n"
"s_lshl_b32 s71, s71, 15\n"
"s_waitcnt lgkmcnt(5)\n"
"v_smfmac_f32_16x16x64_bf16 v[72:75], v[36:39], v[144:151], v1\n"
"ds_read_b128 v[144:147], v6 offset:12288\n"
"ds_read_b128 v[148:151], v6 offset:13312\n"
"v_smfmac_f32_16x16x64_bf16 v[76:79], v[36:39], a[32:39], v1\n"
"s_add_u32 s71, s71, s43\n"
"s_and_b32 s66, s24, 31\n"
"v_smfmac_f32_16x16x64_bf16 v[60:63], v[40:43], a[200:207], v1\n"
"ds_read_b128 v[52:55], v2 offset:8192\n"
"s_lshl_b32 s66, s66, 22\n"
"v_smfmac_f32_16x16x64_bf16 v[64:67], v[40:43], a[208:215], v1\n"
"ds_read_b128 v[116:119], v8 offset:1024\n"
"s_add_u32 s66, s66, s40\n"
"v_smfmac_f32_16x16x64_bf16 v[68:71], v[40:43], a[216:223], v1\n"
"s_and_b32 s72, s72, 31\n"
"s_waitcnt lgkmcnt(6)\n"
"v_smfmac_f32_16x16x64_bf16 v[72:75], v[40:43], v[152:159], v1\n"
"ds_read_b128 v[152:155], v6 offset:14336\n"
"ds_read_b128 v[156:159], v6 offset:15360\n"
"v_smfmac_f32_16x16x64_bf16 v[76:79], v[40:43], a[40:47], v1\n"
"s_lshl_b32 s72, s72, 22\n"
"s_add_u32 s63, s72, s37\n"
"v_smfmac_f32_16x16x64_bf16 v[60:63], v[44:47], a[224:231], v1\n"
"ds_read_b128 v[56:59], v2 offset:9216\n"
"s_add_u32 s70, s22, 1\n"
"v_smfmac_f32_16x16x64_bf16 v[64:67], v[44:47], a[232:239], v1\n"
"ds_read_b128 v[120:123], v8 offset:2048\n"
"s_cmp_lt_u32 s22, 64\n"
"v_smfmac_f32_16x16x64_bf16 v[68:71], v[44:47], a[240:247], v1\n"
"s_cselect_b32 s71, s71, s27\n"
"s_waitcnt lgkmcnt(6)\n"
"v_smfmac_f32_16x16x64_bf16 v[72:75], v[44:47], v[144:151], v1\n"
"ds_read_b128 v[144:147], v6 offset:16384\n"
"ds_read_b128 v[148:151], v6 offset:17408\n"
"v_smfmac_f32_16x16x64_bf16 v[76:79], v[44:47], a[48:55], v1\n"
"s_cselect_b32 s63, s63, s27\n"
"s_cselect_b32 s70, s70, 0\n"
"v_smfmac_f32_16x16x64_bf16 v[60:63], v[48:51], a[248:255], v1\n"
"ds_read_b128 v[20:23], v8 offset:8192\n"
"s_cmp_gt_u32 s65, s70\n"
"v_smfmac_f32_16x16x64_bf16 v[64:67], v[48:51], v[192:199], v1\n"
"ds_read_b128 v[24:27], v8 offset:9216\n"
"s_cselect_b32 s23, s27, s69\n"
"v_smfmac_f32_16x16x64_bf16 v[68:71], v[48:51], v[200:207], v1\n"
"ds_read_b128 v[28:31], v8 offset:10240\n"
"s_waitcnt lgkmcnt(7)\n"
"v_smfmac_f32_16x16x64_bf16 v[72:75], v[48:51], v[152:159], v1\n"
"ds_read_b128 v[152:155], v6 offset:18432\n"
"ds_read_b128 v[156:159], v6 offset:19456\n"
"v_smfmac_f32_16x16x64_bf16 v[76:79], v[48:51], a[56:63], v1\n"
"s_add_u32 s50, s71, s39\n"
"s_add_u32 s51, s50, 4096\n"
"v_smfmac_f32_16x16x64_bf16 v[60:63], v[52:55], v[208:215], v1\n"
"ds_read_b128 v[32:35], v8 offset:11264\n"
"s_add_u32 s52, s50, s53\n"
"v_smfmac_f32_16x16x64_bf16 v[64:67], v[52:55], v[216:223], v1\n"
"ds_read_b128 v[124:127], v8 offset:3072\n"
"v_mov_b64 v[92:93], -1\n"
"v_smfmac_f32_16x16x64_bf16 v[68:71], v[52:55], v[224:231], v1\n"
"ds_read_b128 v[128:131], v8 offset:4096\n"
"s_waitcnt lgkmcnt(8)\n"
"v_smfmac_f32_16x16x64_bf16 v[72:75], v[52:55], v[144:151], v1\n"
"v_mov_b64 v[94:95], -1\n"
"v_smfmac_f32_16x16x64_bf16 v[76:79], v[52:55], a[64:71], v1\n"
"v_smfmac_f32_16x16x64_bf16 v[76:79], v[56:59], a[72:79], v1\n"
"ds_read_b128 v[132:135], v8 offset:5120\n"
"v_smfmac_f32_16x16x64_bf16 v[60:63], v[56:59], v[232:239], v1\n"
"ds_read_b128 v[136:139], v8 offset:6144\n"
"v_smfmac_f32_16x16x64_bf16 v[64:67], v[56:59], v[240:247], v1\n"
"ds_read_b128 v[140:143], v8 offset:7168\n"
"v_smfmac_f32_16x16x64_bf16 v[68:71], v[56:59], v[248:255], v1\n"
"s_waitcnt lgkmcnt(6)\n"
"v_smfmac_f32_16x16x64_bf16 v[72:75], v[56:59], v[152:159], v1\n"
"v_pk_add_f32 v[76:77], v[76:77], v[78:79]\n"
"v_cvt_pk_bf16_f32 v76, v76, v77\n"
"ds_write_b32 v9, v76\n"
"v_pk_add_f32 v[60:61], v[60:61], v[62:63]\n"
"v_pk_add_f32 v[64:65], v[64:65], v[66:67]\n"
"v_cvt_pk_bf16_f32 v62, v60, v64\n"
"v_pk_add_f32 v[68:69], v[68:69], v[70:71]\n"
"v_cvt_pk_bf16_f32 v66, v61, v65\n"
"v_pk_add_f32 v[72:73], v[72:73], v[74:75]\n"
"v_cvt_pk_bf16_f32 v63, v68, v72\n"
"ds_write_b64 v10, v[62:63]\n"
"v_cvt_pk_bf16_f32 v67, v69, v73\n"
"ds_write_b64 v10, v[66:67] offset:288\n"
"buffer_store_dwordx4 v[92:95], v0, s[12:15], s59 offen offset:0 sc1\n"
"buffer_store_dwordx4 v[92:95], v0, s[12:15], s59 offen offset:1024 sc1\n"
"buffer_store_dwordx4 v[92:95], v0, s[12:15], s59 offen offset:2048 sc1\n"
"s_waitcnt lgkmcnt(0)\n"
"s_barrier\n"
"ds_read_b128 v[144:147], v11 offset:0\n"
"ds_read_b128 v[148:151], v11 offset:2304\n"
"ds_read_b128 v[152:155], v11 offset:64\n"
"ds_read_b128 v[156:159], v11 offset:2368\n"
"buffer_store_dwordx4 v[92:95], v0, s[12:15], s59 offen offset:3072 sc1\n"
"ds_read_b128 v[36:39], v11 offset:128\n"
"buffer_store_dwordx4 v[92:95], v0, s[12:15], s60 offen offset:0 sc1\n"
"ds_read_b128 v[40:43], v11 offset:2432\n"
"buffer_store_dwordx4 v[92:95], v0, s[12:15], s60 offen offset:1024 sc1\n"
"ds_read_b128 v[44:47], v11 offset:192\n"
"buffer_store_dwordx4 v[92:95], v0, s[12:15], s60 offen offset:2048 sc1\n"
"ds_read_b128 v[48:51], v11 offset:2496\n"
"s_waitcnt lgkmcnt(6)\n"
"v_mfma_f32_16x16x32_bf16 v[60:63], v[148:151], v[144:147], 0\n"
"buffer_store_dwordx4 v[92:95], v0, s[12:15], s60 offen offset:3072 sc1\n"
"buffer_store_dwordx2 v[92:93], v4, s[12:15], s62 offen sc1\n"
"v_mfma_f32_16x16x32_bf16 v[64:67], v[112:115], v[144:147], 0\n"
"ds_read_b128 v[52:55], v8 offset:16896\n"
"ds_read_b128 v[56:59], v8 offset:17920\n"
"v_mfma_f32_16x16x32_bf16 v[68:71], v[116:119], v[144:147], 0\n"
"ds_read_b128 v[76:79], v13\n"
"s_cmp_lt_u32 s65, s70\n"
"s_cbranch_scc1 X_chkZ_%=\n"
"X_chk_retZ_%=:\n"
"s_waitcnt lgkmcnt(7)\n"
"v_mfma_f32_16x16x32_bf16 v[60:63], v[156:159], v[152:155], v[60:63]\n"
"s_or_b32 s76, s63, s49\n"
"s_add_u32 s77, s76, 4096\n"
"v_mfma_f32_16x16x32_bf16 v[64:67], v[120:123], v[152:155], v[64:67]\n"
"s_add_u32 s78, s76, s38\n"
"s_add_u32 m0, s96, 0\n"
"v_mfma_f32_16x16x32_bf16 v[68:71], v[124:127], v[152:155], v[68:71]\n"
"buffer_load_dwordx4 v0, s[12:15], s76 offen sc1 lds\n"
"s_add_u32 m0, s96, 4096\n"
"s_waitcnt lgkmcnt(5)\n"
"v_mfma_f32_16x16x32_bf16 v[60:63], v[40:43], v[36:39], v[60:63]\n"
"buffer_load_dwordx4 v0, s[12:15], s77 offen sc1 lds\n"
"s_add_u32 m0, s96, 8192\n"
"v_mfma_f32_16x16x32_bf16 v[64:67], v[128:131], v[36:39], v[64:67]\n"
"buffer_load_dwordx4 v0, s[12:15], s78 offen sc1 lds\n"
"s_waitcnt lgkmcnt(0)\n"
"v_mfma_f32_16x16x32_bf16 v[68:71], v[132:135], v[36:39], v[68:71]\n"
"buffer_store_dwordx4 v[76:79], v0, s[12:15], s66 offen sc1\n"
"buffer_load_dword v18, v5, s[12:15], s23 offen sc1\n"
"v_mfma_f32_16x16x32_bf16 v[60:63], v[48:51], v[44:47], v[60:63]\n"
"v_mfma_f32_16x16x32_bf16 v[64:67], v[136:139], v[44:47], v[64:67]\n"
"v_mfma_f32_16x16x32_bf16 v[68:71], v[140:143], v[44:47], v[68:71]\n"
"v_mov_b64 v[84:85], 0\n"
"v_mov_b64 v[86:87], 0\n"
"v_mov_b64 v[80:81], 0\n"
"v_mov_b32 v83, 0\n"
"s_nop 2\n"
"v_max3_f32 v42, v60, v61, v64\n"
"v_max3_f32 v40, v65, v66, v67\n"
"v_max3_f32 v41, v68, v69, v70\n"
"v_max3_f32 v42, v42, v40, v71\n"
"v_max_f32 v42, v42, v41\n"
"v_mov_b32 v40, v42\n"
"v_mfma_f32_32x32x16_bf16 v[96:111], v[84:87], v[84:87], 0\n"
"ds_read_b128 v[112:115], v7 offset:0\n"
"v_permlane32_swap_b32 v42, v40\n"
"v_max_f32 v42, v42, v40\n"
"v_mov_b32 v40, v42\n"
"ds_read_b128 v[116:119], v7 offset:1024\n"
"v_mfma_f32_32x32x16_bf16 v[144:159], v[84:87], v[84:87], 0\n"
"v_permlane16_swap_b32 v42, v40\n"
"v_max_f32 v42, v42, v40\n"
"v_mul_f32 v42, s46, v42\n"
"v_pk_fma_f32 v[60:61], v[60:61], s[18:19], v[42:43] op_sel_hi:[1,1,0] neg_lo:[0,0,1] neg_hi:[0,0,1]\n"
"v_pk_fma_f32 v[64:65], v[64:65], s[18:19], v[42:43] op_sel_hi:[1,1,0] neg_lo:[0,0,1] neg_hi:[0,0,1]\n"
"v_exp_f32 v60, v60\n"
"v_exp_f32 v61, v61\n"
"v_pk_fma_f32 v[66:67], v[66:67], s[18:19], v[42:43] op_sel_hi:[1,1,0] neg_lo:[0,0,1] neg_hi:[0,0,1]\n"
"v_exp_f32 v64, v64\n"
"v_exp_f32 v65, v65\n"
"v_cvt_pk_bf16_f32 v82, v60, v61\n"
"v_pk_fma_f32 v[68:69], v[68:69], s[18:19], v[42:43] op_sel_hi:[1,1,0] neg_lo:[0,0,1] neg_hi:[0,0,1]\n"
"v_exp_f32 v66, v66\n"
"v_mfma_f32_16x16x32_bf16 v[44:47], v[52:55], v[82:85], 0\n"
"v_exp_f32 v67, v67\n"
"v_cvt_pk_bf16_f32 v36, v64, v65\n"
"v_pk_fma_f32 v[70:71], v[70:71], s[18:19], v[42:43] op_sel_hi:[1,1,0] neg_lo:[0,0,1] neg_hi:[0,0,1]\n"
"v_mfma_f32_16x16x32_bf16 v[48:51], v[52:55], v[80:83], 0\n"
"v_exp_f32 v68, v68\n"
"v_exp_f32 v69, v69\n"
"v_mfma_f32_16x16x32_bf16 v[88:91], v[56:59], v[82:85], 0\n"
"v_cvt_pk_bf16_f32 v37, v66, v67\n"
"v_exp_f32 v70, v70\n"
"v_mfma_f32_16x16x32_bf16 v[92:95], v[56:59], v[80:83], 0\n"
"v_exp_f32 v71, v71\n"
"v_cvt_pk_bf16_f32 v38, v68, v69\n"
"v_cvt_pk_bf16_f32 v39, v70, v71\n"
"v_pk_add_f32 v[60:61], v[60:61], v[64:65]\n"
"v_pk_add_f32 v[66:67], v[66:67], v[68:69]\n"
"v_mfma_f32_16x16x32_bf16 v[44:47], v[20:23], v[36:39], v[44:47]\n"
"v_pk_add_f32 v[60:61], v[60:61], v[70:71]\n"
"v_pk_add_f32 v[60:61], v[60:61], v[66:67]\n"
"v_mfma_f32_16x16x32_bf16 v[88:91], v[28:31], v[36:39], v[88:91]\n"
"v_add_f32 v40, v60, v61\n"
"v_mov_b32 v42, v40\n"
"v_mfma_f32_16x16x32_bf16 v[48:51], v[24:27], v[36:39], v[48:51]\n"
"ds_read_b128 v[120:123], v7 offset:2048\n"
"v_permlane32_swap_b32 v40, v42\n"
"v_mfma_f32_16x16x32_bf16 v[92:95], v[32:35], v[36:39], v[92:95]\n"
"v_add_f32 v40, v40, v42\n"
"v_mov_b32 v42, v40\n"
"ds_read_b128 v[124:127], v7 offset:3072\n"
"s_nop 0\n"
"v_permlane16_swap_b32 v40, v42\n"
"v_add_f32 v40, v40, v42\n"
"v_rcp_f32 v40, v40\n"
"v_cndmask_b32 v44, v44, v88, s[44:45]\n"
"v_cndmask_b32 v45, v45, v89, s[44:45]\n"
"v_cndmask_b32 v46, v46, v90, s[44:45]\n"
"v_cndmask_b32 v47, v47, v91, s[44:45]\n"
"ds_read_b128 v[128:131], v7 offset:4096\n"
"v_cndmask_b32 v48, v48, v92, s[44:45]\n"
"v_cndmask_b32 v49, v49, v93, s[44:45]\n"
"v_cndmask_b32 v50, v50, v94, s[44:45]\n"
"v_cndmask_b32 v51, v51, v95, s[44:45]\n"
"ds_read_b128 v[132:135], v7 offset:5120\n"
"v_pk_mul_f32 v[44:45], v[44:45], v[40:41] op_sel_hi:[1,0]\n"
"v_pk_mul_f32 v[46:47], v[46:47], v[40:41] op_sel_hi:[1,0]\n"
"v_pk_mul_f32 v[48:49], v[48:49], v[40:41] op_sel_hi:[1,0]\n"
"v_pk_mul_f32 v[50:51], v[50:51], v[40:41] op_sel_hi:[1,0]\n"
"v_cvt_pk_bf16_f32 v76, v44, v45\n"
"v_cvt_pk_bf16_f32 v77, v46, v47\n"
"v_cvt_pk_bf16_f32 v78, v48, v49\n"
"v_cvt_pk_bf16_f32 v79, v50, v51\n"
"ds_read_b128 v[136:139], v7 offset:6144\n"
"ds_read_b128 v[140:143], v7 offset:7168\n"
"s_add_u32 m0, s96, 18944\n"
"s_nop 0\n"
"buffer_load_dwordx4 v0, s[12:15], s50 offen sc1 lds\n"
"s_add_u32 m0, s96, 23040\n"
"s_nop 0\n"
"buffer_load_dwordx4 v0, s[12:15], s51 offen sc1 lds\n"
"s_add_u32 m0, s96, 27136\n"
"s_nop 0\n"
"buffer_load_dwordx4 v0, s[12:15], s52 offen sc1 lds\n"
"v_smfmac_f32_16x16x64_bf16 v[96:99], v[76:79], v[160:167], v1\n"
"v_smfmac_f32_16x16x64_bf16 v[100:103], v[76:79], v[168:175], v1\n"
"ds_read_b128 v[80:83], v7 offset:8192\n"
"v_mov_b64 v[60:61], 0\n"
"v_smfmac_f32_16x16x64_bf16 v[104:107], v[76:79], v[176:183], v1\n"
"ds_read_b128 v[84:87], v7 offset:9216\n"
"v_mov_b64 v[62:63], 0\n"
"v_smfmac_f32_16x16x64_bf16 v[108:111], v[76:79], v[184:191], v1\n"
"ds_read_b128 v[88:91], v7 offset:10240\n"
"s_waitcnt lgkmcnt(3)\n"
"v_smfmac_f32_16x16x64_bf16 v[144:147], v[76:79], v[112:119], v1\n"
"ds_read_b128 v[92:95], v7 offset:11264\n"
"v_mov_b64 v[64:65], 0\n"
"v_smfmac_f32_16x16x64_bf16 v[148:151], v[76:79], v[120:127], v1\n"
"v_mov_b64 v[66:67], 0\n"
"v_pk_add_f32 v[98:99], v[96:97], v[98:99]\n"
"v_smfmac_f32_16x16x64_bf16 v[152:155], v[76:79], v[128:135], v1\n"
"v_pk_add_f32 v[100:101], v[100:101], v[102:103]\n"
"v_pk_add_f32 v[104:105], v[104:105], v[106:107]\n"
"v_smfmac_f32_16x16x64_bf16 v[156:159], v[76:79], v[136:143], v1\n"
"v_pk_add_f32 v[108:109], v[108:109], v[110:111]\n"
"v_cvt_pk_bf16_f32 v96, v98, v100\n"
"s_waitcnt lgkmcnt(0)\n"
"v_smfmac_f32_16x16x64_bf16 v[60:63], v[76:79], v[80:87], v1\n"
"v_cvt_pk_bf16_f32 v97, v104, v108\n"
"v_cvt_pk_bf16_f32 v98, v99, v101\n"
"v_smfmac_f32_16x16x64_bf16 v[64:67], v[76:79], v[88:95], v1\n"
"v_cvt_pk_bf16_f32 v99, v105, v109\n"
"buffer_store_dwordx4 v[96:99], v3, s[12:15], s56 offen sc1\n"
"v_pk_add_f32 v[146:147], v[144:145], v[146:147]\n"
"v_pk_add_f32 v[148:149], v[148:149], v[150:151]\n"
"v_pk_add_f32 v[152:153], v[152:153], v[154:155]\n"
"v_pk_add_f32 v[156:157], v[156:157], v[158:159]\n"
"v_cvt_pk_bf16_f32 v144, v146, v148\n"
"v_cvt_pk_bf16_f32 v145, v152, v156\n"
"v_cvt_pk_bf16_f32 v146, v147, v149\n"
"v_cvt_pk_bf16_f32 v147, v153, v157\n"
"buffer_store_dwordx4 v[144:147], v3, s[12:15], s57 offen sc1\n"
"v_pk_add_f32 v[60:61], v[60:61], v[62:63]\n"
"v_pk_add_f32 v[64:65], v[64:65], v[66:67]\n"
"ds_read_b128 v[152:155], v6 offset:2048\n"
"ds_read_b128 v[156:159], v6 offset:3072\n"
"v_cvt_pk_bf16_f32 v60, v60, v64\n"
"v_cvt_pk_bf16_f32 v61, v61, v65\n"
"buffer_store_dwordx2 v[60:61], v19, s[12:15], s58 offen sc1\n"
"ds_read_b128 v[144:147], v6 offset:0\n"
"ds_read_b128 v[148:151], v6 offset:1024\n"
"v_mov_b64 v[76:77], 0\n"
"v_mov_b64 v[78:79], 0\n"
"s_cmp_lg_u32 s49, 0\n"
"s_cbranch_scc1 X_kvslow3_%=\n"
"X_kvslow_ret3_%=:\n"
"v_mfma_f32_32x32x16_bf16 v[60:75], v[76:79], v[76:79], 0\n"
"s_waitcnt vmcnt(3)\n"
"ds_read_b128 v[112:115], v12 offset:18944\n"
"ds_read_b128 v[116:119], v12 offset:23040\n"
"ds_read_b128 v[120:123], v12 offset:27136\n"
"v_min_u32_dpp v18, v18, v18 quad_perm:[1,0,3,2] row_mask:0xf bank_mask:0xf\n"
"s_nop 1\n"
"v_min_u32_dpp v18, v18, v18 quad_perm:[2,3,0,1] row_mask:0xf bank_mask:0xf\n"
"s_nop 1\n"
"v_min_u32_dpp v18, v18, v18 row_half_mirror row_mask:0xf bank_mask:0xf\n"
"s_nop 0\n"
"v_readfirstlane_b32 s72, v18\n"
"s_nop 0\n"
"s_max_u32 s65, s65, s72\n"
"s_waitcnt lgkmcnt(0)\n"
"v_max3_u32 v124, v112, v113, v114\n"
"v_max3_u32 v125, v115, v116, v117\n"
"v_max3_u32 v124, v124, v118, v119\n"
"v_max3_u32 v125, v125, v120, v121\n"
"v_max3_u32 v124, v124, v122, v123\n"
"v_max_u32 v124, v124, v125\n"
"v_cmp_eq_u32 vcc, -1, v124\n"
"s_cbranch_vccnz X_mslow3_%=\n"
"X_mslow_ret3_%=:\n"
"s_cmp_eq_u32 s70, 0\n"
"s_cbranch_scc1 X_light_%=\n"
"s_cmp_lt_u32 s97, s27\n"
"s_cbranch_scc1 X_bodyR_%=\n"
"s_branch X_bodyN_%=\n"
"X_chkZ_%=:\n"
"s_waitcnt vmcnt(10)\n"
"v_cmp_gt_u32 s[72:73], s70, v18\n"
"s_nop 0\n"
"s_and_b32 s72, s72, 0xff\n"
"s_cselect_b32 s49, s27, 0\n"
"s_branch X_chk_retZ_%=\n"
"X_bodyR_%=:\n"
"s_barrier\n"
"ds_read_b128 v[20:23], v2 offset:0\n"
"ds_read_b128 v[24:27], v2 offset:1024\n"
"ds_read_b128 v[28:31], v2 offset:2048\n"
"s_mov_b32 s59, s97\n"
"s_add_u32 s60, s97, 4096\n"
"s_add_u32 s71, s98, 1\n"
"s_mov_b32 s98, s22\n"
"s_mov_b32 s22, s71\n"
"s_xor_b32 s69, s69, 16\n"
"s_waitcnt lgkmcnt(2)\n"
"v_smfmac_f32_16x16x64_bf16 v[60:63], v[20:23], a[80:87], v1\n"
"ds_read_b128 v[32:35], v2 offset:3072\n"
"s_mov_b32 s71, s65\n"
"v_smfmac_f32_16x16x64_bf16 v[64:67], v[20:23], a[88:95], v1\n"
"s_mov_b32 s65, s64\n"
"s_mov_b32 s64, s71\n"
"v_smfmac_f32_16x16x64_bf16 v[68:71], v[20:23], a[96:103], v1\n"
"s_cmp_gt_u32 s65, s22\n"
"v_smfmac_f32_16x16x64_bf16 v[72:75], v[20:23], v[144:151], v1\n"
"ds_read_b128 v[144:147], v6 offset:4096\n"
"ds_read_b128 v[148:151], v6 offset:5120\n"
"v_smfmac_f32_16x16x64_bf16 v[76:79], v[20:23], a[0:7], v1\n"
"s_cselect_b32 s23, s27, s69\n"
"buffer_load_dword v18, v5, s[12:15], s23 offen sc1\n"
"s_waitcnt lgkmcnt(4)\n"
"v_smfmac_f32_16x16x64_bf16 v[60:63], v[24:27], a[104:111], v1\n"
"ds_read_b128 v[36:39], v2 offset:4096\n"
"s_xor_b32 s42, s42, s55\n"
"v_smfmac_f32_16x16x64_bf16 v[64:67], v[24:27], a[112:119], v1\n"
"s_sub_u32 s71, s22, 1\n"
"s_cmp_lt_u32 s71, 64\n"
"v_smfmac_f32_16x16x64_bf16 v[68:71], v[24:27], a[120:127], v1\n"
"s_cselect_b32 s67, s42, s27\n"
"v_smfmac_f32_16x16x64_bf16 v[72:75], v[24:27], v[152:159], v1\n"
"ds_read_b128 v[152:155], v6 offset:6144\n"
"ds_read_b128 v[156:159], v6 offset:7168\n"
"v_smfmac_f32_16x16x64_bf16 v[76:79], v[24:27], a[8:15], v1\n"
"s_add_u32 s24, s24, 1\n"
"s_xor_b32 s33, s33, s34\n"
"s_waitcnt lgkmcnt(6)\n"
"v_smfmac_f32_16x16x64_bf16 v[60:63], v[28:31], a[128:135], v1\n"
"ds_read_b128 v[40:43], v2 offset:5120\n"
"s_xor_b32 s35, s35, s36\n"
"v_smfmac_f32_16x16x64_bf16 v[64:67], v[28:31], a[136:143], v1\n"
"s_bitcmp1_b32 s24, 0\n"
"s_cselect_b32 s71, s30, s28\n"
"v_smfmac_f32_16x16x64_bf16 v[68:71], v[28:31], a[144:151], v1\n"
"s_cselect_b32 s72, s31, s29\n"
"s_waitcnt lgkmcnt(4)\n"
"v_smfmac_f32_16x16x64_bf16 v[72:75], v[28:31], v[144:151], v1\n"
"ds_read_b128 v[144:147], v6 offset:8192\n"
"ds_read_b128 v[148:151], v6 offset:9216\n"
"v_smfmac_f32_16x16x64_bf16 v[76:79], v[28:31], a[16:23], v1\n"
"s_bitcmp1_b32 s24, 1\n"
"s_cselect_b32 s71, s72, s71\n"
"v_smfmac_f32_16x16x64_bf16 v[60:63], v[32:35], a[152:159], v1\n"
"ds_read_b128 v[44:47], v2 offset:6144\n"
"buffer_load_dwordx4 v[80:83], v0, s[12:15], s59 offen offset:0 sc1\n"
"v_smfmac_f32_16x16x64_bf16 v[64:67], v[32:35], a[160:167], v1\n"
"s_cmp_lt_u32 s98, 63\n"
"buffer_load_dwordx4 v[84:87], v0, s[12:15], s59 offen offset:1024 sc1\n"
"v_smfmac_f32_16x16x64_bf16 v[68:71], v[32:35], a[168:175], v1\n"
"s_cselect_b32 s71, s71, s27\n"
"s_waitcnt lgkmcnt(4)\n"
"v_smfmac_f32_16x16x64_bf16 v[72:75], v[32:35], v[152:159], v1\n"
"ds_read_b128 v[152:155], v6 offset:10240\n"
"ds_read_b128 v[156:159], v6 offset:11264\n"
"v_smfmac_f32_16x16x64_bf16 v[76:79], v[32:35], a[24:31], v1\n"
"s_add_u32 s97, s71, s33\n"
"s_add_u32 s56, s71, s41\n"
"v_smfmac_f32_16x16x64_bf16 v[60:63], v[36:39], a[176:183], v1\n"
"ds_read_b128 v[48:51], v2 offset:7168\n"
"buffer_load_dwordx4 v[88:91], v0, s[12:15], s59 offen offset:2048 sc1\n"
"v_smfmac_f32_16x16x64_bf16 v[64:67], v[36:39], a[184:191], v1\n"
"ds_read_b128 v[112:115], v8 offset:0\n"
"buffer_load_dwordx4 v[92:95], v0, s[12:15], s59 offen offset:3072 sc1\n"
"v_smfmac_f32_16x16x64_bf16 v[68:71], v[36:39], a[192:199], v1\n"
"s_add_u32 s57, s56, 524288\n"
"s_waitcnt lgkmcnt(5)\n"
"v_smfmac_f32_16x16x64_bf16 v[72:75], v[36:39], v[144:151], v1\n"
"ds_read_b128 v[144:147], v6 offset:12288\n"
"ds_read_b128 v[148:151], v6 offset:13312\n"
"v_smfmac_f32_16x16x64_bf16 v[76:79], v[36:39], a[32:39], v1\n"
"s_add_u32 s58, s56, s68\n"
"s_add_u32 s72, s24, 1\n"
"v_smfmac_f32_16x16x64_bf16 v[60:63], v[40:43], a[200:207], v1\n"
"ds_read_b128 v[52:55], v2 offset:8192\n"
"buffer_load_dwordx4 v[96:99], v0, s[12:15], s60 offen offset:0 sc1\n"
"v_smfmac_f32_16x16x64_bf16 v[64:67], v[40:43], a[208:215], v1\n"
"ds_read_b128 v[116:119], v8 offset:1024\n"
"buffer_load_dwordx4 v[100:103], v0, s[12:15], s60 offen offset:1024 sc1\n"
"v_smfmac_f32_16x16x64_bf16 v[68:71], v[40:43], a[216:223], v1\n"
"s_and_b32 s71, s72, 7\n"
"s_waitcnt lgkmcnt(6)\n"
"v_smfmac_f32_16x16x64_bf16 v[72:75], v[40:43], v[152:159], v1\n"
"ds_read_b128 v[152:155], v6 offset:14336\n"
"ds_read_b128 v[156:159], v6 offset:15360\n"
"v_smfmac_f32_16x16x64_bf16 v[76:79], v[40:43], a[40:47], v1\n"
"s_lshl_b32 s71, s71, 15\n"
"s_add_u32 s71, s71, s43\n"
"v_smfmac_f32_16x16x64_bf16 v[60:63], v[44:47], a[224:231], v1\n"
"ds_read_b128 v[56:59], v2 offset:9216\n"
"buffer_load_dwordx4 v[104:107], v0, s[12:15], s60 offen offset:2048 sc1\n"
"v_smfmac_f32_16x16x64_bf16 v[64:67], v[44:47], a[232:239], v1\n"
"ds_read_b128 v[120:123], v8 offset:2048\n"
"buffer_load_dwordx4 v[108:111], v0, s[12:15], s60 offen offset:3072 sc1\n"
"v_smfmac_f32_16x16x64_bf16 v[68:71], v[44:47], a[240:247], v1\n"
"s_add_u32 s61, s71, s35\n"
"s_waitcnt lgkmcnt(6)\n"
"v_smfmac_f32_16x16x64_bf16 v[72:75], v[44:47], v[144:151], v1\n"
"ds_read_b128 v[144:147], v6 offset:16384\n"
"ds_read_b128 v[148:151], v6 offset:17408\n"
"v_smfmac_f32_16x16x64_bf16 v[76:79], v[44:47], a[48:55], v1\n"
"s_sub_u32 s73, s24, 1\n"
"s_and_b32 s73, s73, 7\n"
"v_smfmac_f32_16x16x64_bf16 v[60:63], v[48:51], a[248:255], v1\n"
"ds_read_b128 v[20:23], v8 offset:8192\n"
"s_lshl_b32 s73, s73, 15\n"
"v_smfmac_f32_16x16x64_bf16 v[64:67], v[48:51], v[192:199], v1\n"
"ds_read_b128 v[24:27], v8 offset:9216\n"
"s_add_u32 s73, s73, s43\n"
"v_smfmac_f32_16x16x64_bf16 v[68:71], v[48:51], v[200:207], v1\n"
"ds_read_b128 v[28:31], v8 offset:10240\n"
"s_waitcnt lgkmcnt(7)\n"
"v_smfmac_f32_16x16x64_bf16 v[72:75], v[48:51], v[152:159], v1\n"
"ds_read_b128 v[152:155], v6 offset:18432\n"
"ds_read_b128 v[156:159], v6 offset:19456\n"
"v_smfmac_f32_16x16x64_bf16 v[76:79], v[48:51], a[56:63], v1\n"
"s_add_u32 s62, s73, s35\n"
"s_and_b32 s66, s24, 31\n"
"v_smfmac_f32_16x16x64_bf16 v[60:63], v[52:55], v[208:215], v1\n"
"ds_read_b128 v[32:35], v8 offset:11264\n"
"s_lshl_b32 s66, s66, 22\n"
"v_smfmac_f32_16x16x64_bf16 v[64:67], v[52:55], v[216:223], v1\n"
"ds_read_b128 v[124:127], v8 offset:3072\n"
"s_add_u32 s66, s66, s40\n"
"v_smfmac_f32_16x16x64_bf16 v[68:71], v[52:55], v[224:231], v1\n"
"ds_read_b128 v[128:131], v8 offset:4096\n"
"s_waitcnt lgkmcnt(8)\n"
"v_smfmac_f32_16x16x64_bf16 v[72:75], v[52:55], v[144:151], v1\n"
"s_and_b32 s72, s72, 31\n"
"s_lshl_b32 s72, s72, 22\n"
"v_smfmac_f32_16x16x64_bf16 v[76:79], v[52:55], a[64:71], v1\n"
"s_add_u32 s63, s72, s37\n"
"s_add_u32 s70, s22, 1\n"
"v_smfmac_f32_16x16x64_bf16 v[76:79], v[56:59], a[72:79], v1\n"
"ds_read_b128 v[132:135], v8 offset:5120\n"
"s_cmp_lt_u32 s22, 64\n"
"v_smfmac_f32_16x16x64_bf16 v[60:63], v[56:59], v[232:239], v1\n"
"ds_read_b128 v[136:139], v8 offset:6144\n"
"s_cselect_b32 s71, s71, s27\n"
"v_smfmac_f32_16x16x64_bf16 v[64:67], v[56:59], v[240:247], v1\n"
"ds_read_b128 v[140:143], v8 offset:7168\n"
"s_cselect_b32 s63, s63, s27\n"
"v_smfmac_f32_16x16x64_bf16 v[68:71], v[56:59], v[248:255], v1\n"
"s_cselect_b32 s70, s70, 0\n"
"s_waitcnt lgkmcnt(6)\n"
"v_smfmac_f32_16x16x64_bf16 v[72:75], v[56:59], v[152:159], v1\n"
"v_pk_add_f32 v[76:77], v[76:77], v[78:79]\n"
"v_cvt_pk_bf16_f32 v76, v76, v77\n"
"ds_write_b32 v9, v76\n"
"v_pk_add_f32 v[60:61], v[60:61], v[62:63]\n"
"v_pk_add_f32 v[64:65], v[64:65], v[66:67]\n"
"v_cvt_pk_bf16_f32 v62, v60, v64\n"
"v_pk_add_f32 v[68:69], v[68:69], v[70:71]\n"
"v_cvt_pk_bf16_f32 v66, v61, v65\n"
"v_pk_add_f32 v[72:73], v[72:73], v[74:75]\n"
"v_cvt_pk_bf16_f32 v63, v68, v72\n"
"ds_write_b64 v10, v[62:63]\n"
"v_cvt_pk_bf16_f32 v67, v69, v73\n"
"ds_write_b64 v10, v[66:67] offset:288\n"
"v_mov_b64 v[72:73], 0\n"
"v_mov_b64 v[74:75], 0\n"
"s_cmp_gt_u32 s65, s70\n"
"s_cselect_b32 s23, s27, s69\n"
"s_add_u32 s50, s71, s39\n"
"s_add_u32 s51, s50, 4096\n"
"s_add_u32 s52, s50, s53\n"
"s_waitcnt vmcnt(6)\n"
"v_smfmac_f32_16x16x64_bf16 v[72:75], v[14:17], v[80:87], v1 abid:1\n"
"s_waitcnt vmcnt(4)\n"
"v_smfmac_f32_16x16x64_bf16 v[72:75], v[14:17], v[88:95], v1 abid:1\n"
"s_waitcnt vmcnt(2)\n"
"v_smfmac_f32_16x16x64_bf16 v[72:75], v[14:17], v[96:103], v1 abid:1\n"
"s_waitcnt lgkmcnt(0)\n"
"s_barrier\n"
"ds_read_b128 v[144:147], v11 offset:0\n"
"ds_read_b128 v[148:151], v11 offset:2304\n"
"ds_read_b128 v[152:155], v11 offset:64\n"
"ds_read_b128 v[156:159], v11 offset:2368\n"
"s_waitcnt vmcnt(0)\n"
"v_smfmac_f32_16x16x64_bf16 v[72:75], v[14:17], v[104:111], v1 abid:1\n"
"ds_read_b128 v[36:39], v11 offset:128\n"
"s_nop 6\n"
"v_add_f32_dpp v72, v72, v72 row_ror:8 row_mask:0xf bank_mask:0xf\n"
"v_add_f32_dpp v73, v73, v73 row_ror:8 row_mask:0xf bank_mask:0xf\n"
"v_add_f32_dpp v74, v74, v74 row_ror:8 row_mask:0xf bank_mask:0xf\n"
"ds_read_b128 v[40:43], v11 offset:2432\n"
"v_add_f32_dpp v75, v75, v75 row_ror:8 row_mask:0xf bank_mask:0xf\n"
"v_pk_add_f32 v[76:77], v[72:73], v[74:75]\n"
"v_cmp_u_f32 vcc, v76, v77\n"
"ds_read_b128 v[44:47], v11 offset:192\n"
"s_cbranch_vccnz X_rretry_%=\n"
"X_rretry_ret_%=:\n"
"v_cvt_pk_bf16_f32 v72, v72, v73\n"
"v_cvt_pk_bf16_f32 v73, v74, v75\n"
"ds_read_b128 v[48:51], v11 offset:2496\n"
"s_waitcnt lgkmcnt(6)\n"
"v_mfma_f32_16x16x32_bf16 v[60:63], v[148:151], v[144:147], 0\n"
"buffer_store_dwordx2 v[72:73], v4, s[12:15], s61 offen sc1\n"
"ds_read_b128 v[52:55], v8 offset:16896\n"
"v_mfma_f32_16x16x32_bf16 v[64:67], v[112:115], v[144:147], 0\n"
"ds_read_b128 v[56:59], v8 offset:17920\n"
"ds_read_b128 v[76:79], v13\n"
"v_mfma_f32_16x16x32_bf16 v[68:71], v[116:119], v[144:147], 0\n"
"s_cmp_lt_u32 s65, s70\n"
"s_cbranch_scc1 X_chkR_%=\n"
"X_chk_retR_%=:\n"
"s_or_b32 s76, s63, s49\n"
"s_waitcnt lgkmcnt(7)\n"
"v_mfma_f32_16x16x32_bf16 v[60:63], v[156:159], v[152:155], v[60:63]\n"
"s_add_u32 s77, s76, 4096\n"
"s_add_u32 s78, s76, s38\n"
"v_mfma_f32_16x16x32_bf16 v[64:67], v[120:123], v[152:155], v[64:67]\n"
"s_add_u32 m0, s96, 0\n"
"v_mfma_f32_16x16x32_bf16 v[68:71], v[124:127], v[152:155], v[68:71]\n"
"buffer_load_dwordx4 v0, s[12:15], s76 offen sc1 lds\n"
"s_add_u32 m0, s96, 4096\n"
"s_waitcnt lgkmcnt(5)\n"
"v_mfma_f32_16x16x32_bf16 v[60:63], v[40:43], v[36:39], v[60:63]\n"
"buffer_load_dwordx4 v0, s[12:15], s77 offen sc1 lds\n"
"s_add_u32 m0, s96, 8192\n"
"v_mfma_f32_16x16x32_bf16 v[64:67], v[128:131], v[36:39], v[64:67]\n"
"buffer_load_dwordx4 v0, s[12:15], s78 offen sc1 lds\n"
"s_waitcnt lgkmcnt(0)\n"
"v_mfma_f32_16x16x32_bf16 v[68:71], v[132:135], v[36:39], v[68:71]\n"
"buffer_store_dwordx4 v[76:79], v0, s[12:15], s66 offen sc1\n"
"buffer_load_dword v18, v5, s[12:15], s23 offen sc1\n"
"v_mfma_f32_16x16x32_bf16 v[60:63], v[48:51], v[44:47], v[60:63]\n"
"v_mfma_f32_16x16x32_bf16 v[64:67], v[136:139], v[44:47], v[64:67]\n"
"v_mfma_f32_16x16x32_bf16 v[68:71], v[140:143], v[44:47], v[68:71]\n"
"v_mov_b64 v[84:85], 0\n"
"v_mov_b64 v[86:87], 0\n"
"v_mov_b64 v[80:81], 0\n"
"v_mov_b32 v83, 0\n"
"s_nop 2\n"
"v_max3_f32 v42, v60, v61, v64\n"
"v_max3_f32 v40, v65, v66, v67\n"
"v_max3_f32 v41, v68, v69, v70\n"
"v_max3_f32 v42, v42, v40, v71\n"
"v_max_f32 v42, v42, v41\n"
"v_mov_b32 v40, v42\n"
"v_mfma_f32_32x32x16_bf16 v[96:111], v[84:87], v[84:87], 0\n"
"ds_read_b128 v[112:115], v7 offset:0\n"
"v_permlane32_swap_b32 v42, v40\n"
"v_max_f32 v42, v42, v40\n"
"v_mov_b32 v40, v42\n"
"ds_read_b128 v[116:119], v7 offset:1024\n"
"v_mfma_f32_32x32x16_bf16 v[144:159], v[84:87], v[84:87], 0\n"
"v_permlane16_swap_b32 v42, v40\n"
"v_max_f32 v42, v42, v40\n"
"v_mul_f32 v42, s46, v42\n"
"v_pk_fma_f32 v[60:61], v[60:61], s[18:19], v[42:43] op_sel_hi:[1,1,0] neg_lo:[0,0,1] neg_hi:[0,0,1]\n"
"v_pk_fma_f32 v[64:65], v[64:65], s[18:19], v[42:43] op_sel_hi:[1,1,0] neg_lo:[0,0,1] neg_hi:[0,0,1]\n"
"v_exp_f32 v60, v60\n"
"v_exp_f32 v61, v61\n"
"v_pk_fma_f32 v[66:67], v[66:67], s[18:19], v[42:43] op_sel_hi:[1,1,0] neg_lo:[0,0,1] neg_hi:[0,0,1]\n"
"v_exp_f32 v64, v64\n"
"v_exp_f32 v65, v65\n"
"v_cvt_pk_bf16_f32 v82, v60, v61\n"
"v_pk_fma_f32 v[68:69], v[68:69], s[18:19], v[42:43] op_sel_hi:[1,1,0] neg_lo:[0,0,1] neg_hi:[0,0,1]\n"
"v_exp_f32 v66, v66\n"
"v_mfma_f32_16x16x32_bf16 v[44:47], v[52:55], v[82:85], 0\n"
"v_exp_f32 v67, v67\n"
"v_cvt_pk_bf16_f32 v36, v64, v65\n"
"v_pk_fma_f32 v[70:71], v[70:71], s[18:19], v[42:43] op_sel_hi:[1,1,0] neg_lo:[0,0,1] neg_hi:[0,0,1]\n"
"v_mfma_f32_16x16x32_bf16 v[48:51], v[52:55], v[80:83], 0\n"
"v_exp_f32 v68, v68\n"
"v_exp_f32 v69, v69\n"
"v_mfma_f32_16x16x32_bf16 v[88:91], v[56:59], v[82:85], 0\n"
"v_cvt_pk_bf16_f32 v37, v66, v67\n"
"v_exp_f32 v70, v70\n"
"v_mfma_f32_16x16x32_bf16 v[92:95], v[56:59], v[80:83], 0\n"
"v_exp_f32 v71, v71\n"
"v_cvt_pk_bf16_f32 v38, v68, v69\n"
"v_cvt_pk_bf16_f32 v39, v70, v71\n"
"v_pk_add_f32 v[60:61], v[60:61], v[64:65]\n"
"v_pk_add_f32 v[66:67], v[66:67], v[68:69]\n"
"v_mfma_f32_16x16x32_bf16 v[44:47], v[20:23], v[36:39], v[44:47]\n"
"v_pk_add_f32 v[60:61], v[60:61], v[70:71]\n"
"v_pk_add_f32 v[60:61], v[60:61], v[66:67]\n"
"v_mfma_f32_16x16x32_bf16 v[88:91], v[28:31], v[36:39], v[88:91]\n"
"v_add_f32 v40, v60, v61\n"
"v_mov_b32 v42, v40\n"
"v_mfma_f32_16x16x32_bf16 v[48:51], v[24:27], v[36:39], v[48:51]\n"
"ds_read_b128 v[120:123], v7 offset:2048\n"
"v_permlane32_swap_b32 v40, v42\n"
"v_mfma_f32_16x16x32_bf16 v[92:95], v[32:35], v[36:39], v[92:95]\n"
"v_add_f32 v40, v40, v42\n"
"v_mov_b32 v42, v40\n"
"ds_read_b128 v[124:127], v7 offset:3072\n"
"s_nop 0\n"
"v_permlane16_swap_b32 v40, v42\n"
"v_add_f32 v40, v40, v42\n"
"v_rcp_f32 v40, v40\n"
"v_cndmask_b32 v44, v44, v88, s[44:45]\n"
"v_cndmask_b32 v45, v45, v89, s[44:45]\n"
"v_cndmask_b32 v46, v46, v90, s[44:45]\n"
"v_cndmask_b32 v47, v47, v91, s[44:45]\n"
"ds_read_b128 v[128:131], v7 offset:4096\n"
"v_cndmask_b32 v48, v48, v92, s[44:45]\n"
"v_cndmask_b32 v49, v49, v93, s[44:45]\n"
"v_cndmask_b32 v50, v50, v94, s[44:45]\n"
"v_cndmask_b32 v51, v51, v95, s[44:45]\n"
"ds_read_b128 v[132:135], v7 offset:5120\n"
"v_pk_mul_f32 v[44:45], v[44:45], v[40:41] op_sel_hi:[1,0]\n"
"v_pk_mul_f32 v[46:47], v[46:47], v[40:41] op_sel_hi:[1,0]\n"
"v_pk_mul_f32 v[48:49], v[48:49], v[40:41] op_sel_hi:[1,0]\n"
"v_pk_mul_f32 v[50:51], v[50:51], v[40:41] op_sel_hi:[1,0]\n"
"v_cvt_pk_bf16_f32 v76, v44, v45\n"
"v_cvt_pk_bf16_f32 v77, v46, v47\n"
"v_cvt_pk_bf16_f32 v78, v48, v49\n"
"v_cvt_pk_bf16_f32 v79, v50, v51\n"
"ds_read_b128 v[136:139], v7 offset:6144\n"
"ds_read_b128 v[140:143], v7 offset:7168\n"
"s_add_u32 m0, s96, 18944\n"
"s_nop 0\n"
"buffer_load_dwordx4 v0, s[12:15], s50 offen sc1 lds\n"
"s_add_u32 m0, s96, 23040\n"
"s_nop 0\n"
"buffer_load_dwordx4 v0, s[12:15], s51 offen sc1 lds\n"
"s_add_u32 m0, s96, 27136\n"
"s_nop 0\n"
"buffer_load_dwordx4 v0, s[12:15], s52 offen sc1 lds\n"
"v_smfmac_f32_16x16x64_bf16 v[96:99], v[76:79], v[160:167], v1\n"
"v_smfmac_f32_16x16x64_bf16 v[100:103], v[76:79], v[168:175], v1\n"
"ds_read_b128 v[80:83], v7 offset:8192\n"
"v_mov_b64 v[60:61], 0\n"
"v_smfmac_f32_16x16x64_bf16 v[104:107], v[76:79], v[176:183], v1\n"
"ds_read_b128 v[84:87], v7 offset:9216\n"
"v_mov_b64 v[62:63], 0\n"
"v_smfmac_f32_16x16x64_bf16 v[108:111], v[76:79], v[184:191], v1\n"
"ds_read_b128 v[88:91], v7 offset:10240\n"
"s_waitcnt lgkmcnt(3)\n"
"v_smfmac_f32_16x16x64_bf16 v[144:147], v[76:79], v[112:119], v1\n"
"ds_read_b128 v[92:95], v7 offset:11264\n"
"v_mov_b64 v[64:65], 0\n"
"v_smfmac_f32_16x16x64_bf16 v[148:151], v[76:79], v[120:127], v1\n"
"v_mov_b64 v[66:67], 0\n"
"v_pk_add_f32 v[98:99], v[96:97], v[98:99]\n"
"v_smfmac_f32_16x16x64_bf16 v[152:155], v[76:79], v[128:135], v1\n"
"v_pk_add_f32 v[100:101], v[100:101], v[102:103]\n"
"v_pk_add_f32 v[104:105], v[104:105], v[106:107]\n"
"v_smfmac_f32_16x16x64_bf16 v[156:159], v[76:79], v[136:143], v1\n"
"v_pk_add_f32 v[108:109], v[108:109], v[110:111]\n"
"v_cvt_pk_bf16_f32 v96, v98, v100\n"
"s_waitcnt lgkmcnt(0)\n"
"v_smfmac_f32_16x16x64_bf16 v[60:63], v[76:79], v[80:87], v1\n"
"v_cvt_pk_bf16_f32 v97, v104, v108\n"
"v_cvt_pk_bf16_f32 v98, v99, v101\n"
"v_smfmac_f32_16x16x64_bf16 v[64:67], v[76:79], v[88:95], v1\n"
"v_cvt_pk_bf16_f32 v99, v105, v109\n"
"buffer_store_dwordx4 v[96:99], v3, s[12:15], s56 offen sc1\n"
"v_pk_add_f32 v[146:147], v[144:145], v[146:147]\n"
"v_pk_add_f32 v[148:149], v[148:149], v[150:151]\n"
"v_pk_add_f32 v[152:153], v[152:153], v[154:155]\n"
"v_pk_add_f32 v[156:157], v[156:157], v[158:159]\n"
"v_cvt_pk_bf16_f32 v144, v146, v148\n"
"v_cvt_pk_bf16_f32 v145, v152, v156\n"
"v_cvt_pk_bf16_f32 v146, v147, v149\n"
"v_cvt_pk_bf16_f32 v147, v153, v157\n"
"buffer_store_dwordx4 v[144:147], v3, s[12:15], s57 offen sc1\n"
"v_pk_add_f32 v[60:61], v[60:61], v[62:63]\n"
"v_pk_add_f32 v[64:65], v[64:65], v[66:67]\n"
"ds_read_b128 v[152:155], v6 offset:2048\n"
"ds_read_b128 v[156:159], v6 offset:3072\n"
"v_cvt_pk_bf16_f32 v60, v60, v64\n"
"v_cvt_pk_bf16_f32 v61, v61, v65\n"
"buffer_store_dwordx2 v[60:61], v19, s[12:15], s58 offen sc1\n"
"ds_read_b128 v[144:147], v6 offset:0\n"
"ds_read_b128 v[148:151], v6 offset:1024\n"
"v_mov_b64 v[76:77], 0\n"
"v_mov_b64 v[78:79], 0\n"
"s_cmp_lg_u32 s49, 0\n"
"s_cbranch_scc1 X_kvslow4_%=\n"
"X_kvslow_ret4_%=:\n"
"v_mfma_f32_32x32x16_bf16 v[60:75], v[76:79], v[76:79], 0\n"
"s_waitcnt vmcnt(3)\n"
"ds_read_b128 v[112:115], v12 offset:18944\n"
"ds_read_b128 v[116:119], v12 offset:23040\n"
"ds_read_b128 v[120:123], v12 offset:27136\n"
"v_min_u32_dpp v18, v18, v18 quad_perm:[1,0,3,2] row_mask:0xf bank_mask:0xf\n"
"s_nop 1\n"
"v_min_u32_dpp v18, v18, v18 quad_perm:[2,3,0,1] row_mask:0xf bank_mask:0xf\n"
"s_nop 1\n"
"v_min_u32_dpp v18, v18, v18 row_half_mirror row_mask:0xf bank_mask:0xf\n"
"s_nop 0\n"
"v_readfirstlane_b32 s72, v18\n"
"s_nop 0\n"
"s_max_u32 s65, s65, s72\n"
"s_waitcnt lgkmcnt(0)\n"
"v_max3_u32 v124, v112, v113, v114\n"
"v_max3_u32 v125, v115, v116, v117\n"
"v_max3_u32 v124, v124, v118, v119\n"
"v_max3_u32 v125, v125, v120, v121\n"
"v_max3_u32 v124, v124, v122, v123\n"
"v_max_u32 v124, v124, v125\n"
"v_cmp_eq_u32 vcc, -1, v124\n"
"s_cbranch_vccnz X_mslow4_%=\n"
"X_mslow_ret4_%=:\n"
"s_cmp_eq_u32 s70, 0\n"
"s_cbranch_scc1 X_lightZ_%=\n"
"s_branch X_bodyZ_%=\n"
"X_rretry_%=:\n"
"s_nop 15\n"
"s_waitcnt vmcnt(0)\n"
"X_rretry_l_%=:\n"
"v_max3_u32 v72, v80, v81, v82\n"
"v_max3_u32 v72, v72, v83, v84\n"
"v_max3_u32 v72, v72, v85, v86\n"
"v_max3_u32 v72, v72, v87, v88\n"
"v_max3_u32 v72, v72, v89, v90\n"
"v_max3_u32 v72, v72, v91, v92\n"
"v_max3_u32 v72, v72, v93, v94\n"
"v_max3_u32 v72, v72, v95, v96\n"
"v_max3_u32 v72, v72, v97, v98\n"
"v_max3_u32 v72, v72, v99, v100\n"
"v_max3_u32 v72, v72, v101, v102\n"
"v_max3_u32 v72, v72, v103, v104\n"
"v_max3_u32 v72, v72, v105, v106\n"
"v_max3_u32 v72, v72, v107, v108\n"
"v_max3_u32 v72, v72, v109, v110\n"
"v_max_u32 v72, v72, v111\n"
"v_cmp_eq_u32 vcc, -1, v72\n"
"s_cbranch_vccz X_rretry_g_%=\n"
"buffer_load_dwordx4 v[80:83], v0, s[12:15], s59 offen offset:0 sc1\n"
"buffer_load_dwordx4 v[84:87], v0, s[12:15], s59 offen offset:1024 sc1\n"
"buffer_load_dwordx4 v[88:91], v0, s[12:15], s59 offen offset:2048 sc1\n"
"buffer_load_dwordx4 v[92:95], v0, s[12:15], s59 offen offset:3072 sc1\n"
"buffer_load_dwordx4 v[96:99], v0, s[12:15], s60 offen offset:0 sc1\n"
"buffer_load_dwordx4 v[100:103], v0, s[12:15], s60 offen offset:1024 sc1\n"
"buffer_load_dwordx4 v[104:107], v0, s[12:15], s60 offen offset:2048 sc1\n"
"buffer_load_dwordx4 v[108:111], v0, s[12:15], s60 offen offset:3072 sc1\n"
"s_waitcnt vmcnt(0)\n"
"s_add_u32 s25, s25, 1\n"
"s_cmp_lt_u32 s25, 200000\n"
"s_cbranch_scc1 X_rretry_l_%=\n"
"X_rretry_g_%=:\n"
"v_mov_b64 v[72:73], 0\n"
"v_mov_b64 v[74:75], 0\n"
"s_nop 1\n"
"v_smfmac_f32_16x16x64_bf16 v[72:75], v[14:17], v[80:87], v1 abid:1\n"
"v_smfmac_f32_16x16x64_bf16 v[72:75], v[14:17], v[88:95], v1 abid:1\n"
"v_smfmac_f32_16x16x64_bf16 v[72:75], v[14:17], v[96:103], v1 abid:1\n"
"v_smfmac_f32_16x16x64_bf16 v[72:75], v[14:17], v[104:111], v1 abid:1\n"
"s_nop 7\n"
"v_add_f32_dpp v72, v72, v72 row_ror:8 row_mask:0xf bank_mask:0xf\n"
"v_add_f32_dpp v73, v73, v73 row_ror:8 row_mask:0xf bank_mask:0xf\n"
"v_add_f32_dpp v74, v74, v74 row_ror:8 row_mask:0xf bank_mask:0xf\n"
"v_add_f32_dpp v75, v75, v75 row_ror:8 row_mask:0xf bank_mask:0xf\n"
"s_nop 15\n"
"s_branch X_rretry_ret_%=\n"
"X_chkR_%=:\n"
"v_cmp_gt_u32 s[72:73], s70, v18\n"
"s_nop 0\n"
"s_and_b32 s72, s72, 0xff\n"
"s_cselect_b32 s49, s27, 0\n"
"s_branch X_chk_retR_%=\n"
"X_lightZ_%=:\n"
"s_barrier\n"
"s_add_u32 s71, s98, 1\n"
"s_mov_b32 s98, s22\n"
"s_mov_b32 s22, s71\n"
"s_xor_b32 s69, s69, 16\n"
"s_mov_b32 s71, s65\n"
"s_mov_b32 s65, s64\n"
"s_mov_b32 s64, s71\n"
"buffer_load_dword v18, v5, s[12:15], s69 offen sc1\n"
"s_cmp_gt_u32 s65, s22\n"
"s_cselect_b32 s23, s27, s69\n"
"s_xor_b32 s42, s42, s55\n"
"s_sub_u32 s71, s22, 1\n"
"s_cmp_lt_u32 s71, 64\n"
"s_cselect_b32 s67, s42, s27\n"
"v_mov_b32 v111, s22\n"
"buffer_store_dword v111, off, s[12:15], s67 sc1\n"
"s_add_u32 s24, s24, 1\n"
"s_xor_b32 s33, s33, s34\n"
"s_xor_b32 s35, s35, s36\n"
"s_bitcmp1_b32 s24, 0\n"
"s_cselect_b32 s71, s30, s28\n"
"s_cselect_b32 s72, s31, s29\n"
"s_bitcmp1_b32 s24, 1\n"
"s_cselect_b32 s71, s72, s71\n"
"s_cmp_lt_u32 s98, 63\n"
"s_cselect_b32 s71, s71, s27\n"
"s_add_u32 s97, s71, s33\n"
"s_add_u32 s56, s71, s41\n"
"s_add_u32 s57, s56, 524288\n"
"s_add_u32 s58, s56, s68\n"
"s_add_u32 s72, s24, 1\n"
"s_and_b32 s71, s72, 7\n"
"s_lshl_b32 s71, s71, 15\n"
"s_add_u32 s71, s71, s43\n"
"s_and_b32 s66, s24, 31\n"
"s_lshl_b32 s66, s66, 22\n"
"s_add_u32 s66, s66, s40\n"
"s_and_b32 s72, s72, 31\n"
"s_lshl_b32 s72, s72, 22\n"
"s_add_u32 s63, s72, s37\n"
"s_add_u32 s70, s22, 1\n"
"s_cmp_lt_u32 s22, 64\n"
"s_cselect_b32 s71, s71, s27\n"
"s_cselect_b32 s63, s63, s27\n"
"s_cselect_b32 s70, s70, 0\n"
"s_cmp_gt_u32 s65, s70\n"
"s_cselect_b32 s23, s27, s69\n"
"s_add_u32 s50, s71, s39\n"
"s_add_u32 s51, s50, 4096\n"
"s_add_u32 s52, s50, s53\n"
"s_cmp_ge_i32 s24, 142\n"
"s_cbranch_scc1 X_end_%=\n"
"s_waitcnt vmcnt(1) lgkmcnt(0)\n"
"v_cmp_gt_u32 vcc, s70, v18\n"
"s_nop 0\n"
"s_and_b32 s72, vcc_lo, 0xff\n"
"s_cselect_b32 s49, s27, 0\n"
"s_or_b32 s76, s63, s49\n"
"s_add_u32 s77, s76, 4096\n"
"s_add_u32 s78, s76, s38\n"
"s_add_u32 m0, s96, 0\n"
"s_nop 0\n"
"buffer_load_dwordx4 v0, s[12:15], s76 offen sc1 lds\n"
"s_add_u32 m0, s96, 4096\n"
"s_nop 0\n"
"buffer_load_dwordx4 v0, s[12:15], s77 offen sc1 lds\n"
"s_add_u32 m0, s96, 8192\n"
"s_nop 0\n"
"buffer_load_dwordx4 v0, s[12:15], s78 offen sc1 lds\n"
"v_mov_b64 v[92:93], -1\n"
"v_mov_b64 v[94:95], -1\n"
"buffer_store_dwordx4 v[92:95], v0, s[12:15], s59 offen offset:0 sc1\n"
"buffer_store_dwordx4 v[92:95], v0, s[12:15], s59 offen offset:1024 sc1\n"
"buffer_store_dwordx4 v[92:95], v0, s[12:15], s59 offen offset:2048 sc1\n"
"buffer_store_dwordx4 v[92:95], v0, s[12:15], s59 offen offset:3072 sc1\n"
"buffer_store_dwordx4 v[92:95], v0, s[12:15], s60 offen offset:0 sc1\n"
"buffer_store_dwordx4 v[92:95], v0, s[12:15], s60 offen offset:1024 sc1\n"
"buffer_store_dwordx4 v[92:95], v0, s[12:15], s60 offen offset:2048 sc1\n"
"buffer_store_dwordx4 v[92:95], v0, s[12:15], s60 offen offset:3072 sc1\n"
"buffer_store_dwordx2 v[92:93], v4, s[12:15], s62 offen sc1\n"
"s_barrier\n"
"s_nop 15\n"
"s_nop 15\n"
"s_nop 15\n"
"s_nop 15\n"
"s_nop 15\n"
"s_nop 15\n"
"s_nop 15\n"
"s_nop 15\n"
"s_add_u32 m0, s96, 18944\n"
"s_nop 0\n"
"buffer_load_dwordx4 v0, s[12:15], s50 offen sc1 lds\n"
"s_add_u32 m0, s96, 23040\n"
"s_nop 0\n"
"buffer_load_dwordx4 v0, s[12:15], s51 offen sc1 lds\n"
"s_add_u32 m0, s96, 27136\n"
"s_nop 0\n"
"buffer_load_dwordx4 v0, s[12:15], s52 offen sc1 lds\n"
"ds_read_b128 v[144:147], v6 offset:0\n"
"ds_read_b128 v[148:151], v6 offset:1024\n"
"ds_read_b128 v[152:155], v6 offset:2048\n"
"ds_read_b128 v[156:159], v6 offset:3072\n"
"v_mov_b64 v[76:77], 0\n"
"v_mov_b64 v[78:79], 0\n"
"s_cmp_lg_u32 s49, 0\n"
"s_cbranch_scc1 X_kvslow5_%=\n"
"X_kvslow_ret5_%=:\n"
"v_mfma_f32_32x32x16_bf16 v[60:75], v[76:79], v[76:79], 0\n"
"s_waitcnt vmcnt(0)\n"
"ds_read_b128 v[112:115], v12 offset:18944\n"
"ds_read_b128 v[116:119], v12 offset:23040\n"
"ds_read_b128 v[120:123], v12 offset:27136\n"
"v_min_u32_dpp v18, v18, v18 quad_perm:[1,0,3,2] row_mask:0xf bank_mask:0xf\n"
"s_nop 1\n"
"v_min_u32_dpp v18, v18, v18 quad_perm:[2,3,0,1] row_mask:0xf bank_mask:0xf\n"
"s_nop 1\n"
"v_min_u32_dpp v18, v18, v18 row_half_mirror row_mask:0xf bank_mask:0xf\n"
"s_nop 0\n"
"v_readfirstlane_b32 s72, v18\n"
"s_nop 0\n"
"s_max_u32 s65, s65, s72\n"
"s_waitcnt lgkmcnt(0)\n"
"v_max3_u32 v124, v112, v113, v114\n"
"v_max3_u32 v125, v115, v116, v117\n"
"v_max3_u32 v124, v124, v118, v119\n"
"v_max3_u32 v125, v125, v120, v121\n"
"v_max3_u32 v124, v124, v122, v123\n"
"v_max_u32 v124, v124, v125\n"
"v_cmp_eq_u32 vcc, -1, v124\n"
"s_cbranch_vccnz X_mslow5_%=\n"
"X_mslow_ret5_%=:\n"
"s_cmp_eq_u32 s70, 0\n"
"s_cbranch_scc1 X_light_%=\n"
"s_cmp_lt_u32 s97, s27\n"
"s_cbranch_scc1 X_bodyR_%=\n"
"s_branch X_bodyN_%=\n"
"X_light_%=:\n"
"s_barrier\n"
"s_cmp_lt_u32 s97, s27\n"
"s_cbranch_scc1 X_lightR_%=\n"
"s_add_u32 s71, s98, 1\n"
"s_mov_b32 s98, s22\n"
"s_mov_b32 s22, s71\n"
"s_xor_b32 s69, s69, 16\n"
"s_mov_b32 s71, s65\n"
"s_mov_b32 s65, s64\n"
"s_mov_b32 s64, s71\n"
"buffer_load_dword v18, v5, s[12:15], s69 offen sc1\n"
"s_cmp_gt_u32 s65, s22\n"
"s_cselect_b32 s23, s27, s69\n"
"s_xor_b32 s42, s42, s55\n"
"s_sub_u32 s71, s22, 1\n"
"s_cmp_lt_u32 s71, 64\n"
"s_cselect_b32 s67, s42, s27\n"
"v_mov_b32 v111, s22\n"
"buffer_store_dword v111, off, s[12:15], s67 sc1\n"
"s_add_u32 s24, s24, 1\n"
"s_xor_b32 s33, s33, s34\n"
"s_xor_b32 s35, s35, s36\n"
"s_bitcmp1_b32 s24, 0\n"
"s_cselect_b32 s71, s30, s28\n"
"s_cselect_b32 s72, s31, s29\n"
"s_bitcmp1_b32 s24, 1\n"
"s_cselect_b32 s71, s72, s71\n"
"s_cmp_lt_u32 s98, 63\n"
"s_cselect_b32 s71, s71, s27\n"
"s_add_u32 s97, s71, s33\n"
"s_add_u32 s56, s71, s41\n"
"s_add_u32 s57, s56, 524288\n"
"s_add_u32 s58, s56, s68\n"
"s_add_u32 s72, s24, 1\n"
"s_and_b32 s71, s72, 7\n"
"s_lshl_b32 s71, s71, 15\n"
"s_add_u32 s71, s71, s43\n"
"s_and_b32 s66, s24, 31\n"
"s_lshl_b32 s66, s66, 22\n"
"s_add_u32 s66, s66, s40\n"
"s_and_b32 s72, s72, 31\n"
"s_lshl_b32 s72, s72, 22\n"
"s_add_u32 s63, s72, s37\n"
"s_add_u32 s70, s22, 1\n"
"s_cmp_lt_u32 s22, 64\n"
"s_cselect_b32 s71, s71, s27\n"
"s_cselect_b32 s63, s63, s27\n"
"s_cselect_b32 s70, s70, 0\n"
"s_cmp_gt_u32 s65, s70\n"
"s_cselect_b32 s23, s27, s69\n"
"s_add_u32 s50, s71, s39\n"
"s_add_u32 s51, s50, 4096\n"
"s_add_u32 s52, s50, s53\n"
"s_cmp_ge_i32 s24, 142\n"
"s_cbranch_scc1 X_end_%=\n"
"s_waitcnt vmcnt(1) lgkmcnt(0)\n"
"v_cmp_gt_u32 vcc, s70, v18\n"
"s_nop 0\n"
"s_and_b32 s72, vcc_lo, 0xff\n"
"s_cselect_b32 s49, s27, 0\n"
"s_or_b32 s76, s63, s49\n"
"s_add_u32 s77, s76, 4096\n"
"s_add_u32 s78, s76, s38\n"
"s_add_u32 m0, s96, 0\n"
"s_nop 0\n"
"buffer_load_dwordx4 v0, s[12:15], s76 offen sc1 lds\n"
"s_add_u32 m0, s96, 4096\n"
"s_nop 0\n"
"buffer_load_dwordx4 v0, s[12:15], s77 offen sc1 lds\n"
"s_add_u32 m0, s96, 8192\n"
"s_nop 0\n"
"buffer_load_dwordx4 v0, s[12:15], s78 offen sc1 lds\n"
"s_barrier\n"
"s_nop 15\n"
"s_nop 15\n"
"s_nop 15\n"
"s_nop 15\n"
"s_nop 15\n"
"s_nop 15\n"
"s_nop 15\n"
"s_nop 15\n"
"s_add_u32 m0, s96, 18944\n"
"s_nop 0\n"
"buffer_load_dwordx4 v0, s[12:15], s50 offen sc1 lds\n"
"s_add_u32 m0, s96, 23040\n"
"s_nop 0\n"
"buffer_load_dwordx4 v0, s[12:15], s51 offen sc1 lds\n"
"s_add_u32 m0, s96, 27136\n"
"s_nop 0\n"
"buffer_load_dwordx4 v0, s[12:15], s52 offen sc1 lds\n"
"ds_read_b128 v[144:147], v6 offset:0\n"
"ds_read_b128 v[148:151], v6 offset:1024\n"
"ds_read_b128 v[152:155], v6 offset:2048\n"
"ds_read_b128 v[156:159], v6 offset:3072\n"
"v_mov_b64 v[76:77], 0\n"
"v_mov_b64 v[78:79], 0\n"
"s_cmp_lg_u32 s49, 0\n"
"s_cbranch_scc1 X_kvslow6_%=\n"
"X_kvslow_ret6_%=:\n"
"v_mfma_f32_32x32x16_bf16 v[60:75], v[76:79], v[76:79], 0\n"
"s_waitcnt vmcnt(0)\n"
"ds_read_b128 v[112:115], v12 offset:18944\n"
"ds_read_b128 v[116:119], v12 offset:23040\n"
"ds_read_b128 v[120:123], v12 offset:27136\n"
"v_min_u32_dpp v18, v18, v18 quad_perm:[1,0,3,2] row_mask:0xf bank_mask:0xf\n"
"s_nop 1\n"
"v_min_u32_dpp v18, v18, v18 quad_perm:[2,3,0,1] row_mask:0xf bank_mask:0xf\n"
"s_nop 1\n"
"v_min_u32_dpp v18, v18, v18 row_half_mirror row_mask:0xf bank_mask:0xf\n"
"s_nop 0\n"
"v_readfirstlane_b32 s72, v18\n"
"s_nop 0\n"
"s_max_u32 s65, s65, s72\n"
"s_waitcnt lgkmcnt(0)\n"
"v_max3_u32 v124, v112, v113, v114\n"
"v_max3_u32 v125, v115, v116, v117\n"
"v_max3_u32 v124, v124, v118, v119\n"
"v_max3_u32 v125, v125, v120, v121\n"
"v_max3_u32 v124, v124, v122, v123\n"
"v_max_u32 v124, v124, v125\n"
"v_cmp_eq_u32 vcc, -1, v124\n"
"s_cbranch_vccnz X_mslow6_%=\n"
"X_mslow_ret6_%=:\n"
"s_cmp_eq_u32 s70, 0\n"
"s_cbranch_scc1 X_light_%=\n"
"s_cmp_lt_u32 s97, s27\n"
"s_cbranch_scc1 X_bodyR_%=\n"
"s_branch X_bodyN_%=\n"
"X_lightR_%=:\n"
"s_mov_b32 s59, s97\n"
"s_add_u32 s60, s97, 4096\n"
"s_add_u32 s71, s98, 1\n"
"s_mov_b32 s98, s22\n"
"s_mov_b32 s22, s71\n"
"s_xor_b32 s69, s69, 16\n"
"s_mov_b32 s71, s65\n"
"s_mov_b32 s65, s64\n"
"s_mov_b32 s64, s71\n"
"buffer_load_dword v18, v5, s[12:15], s69 offen sc1\n"
"s_cmp_gt_u32 s65, s22\n"
"s_cselect_b32 s23, s27, s69\n"
"s_xor_b32 s42, s42, s55\n"
"s_sub_u32 s71, s22, 1\n"
"s_cmp_lt_u32 s71, 64\n"
"s_cselect_b32 s67, s42, s27\n"
"v_mov_b32 v111, s22\n"
"buffer_store_dword v111, off, s[12:15], s67 sc1\n"
"s_add_u32 s24, s24, 1\n"
"s_xor_b32 s33, s33, s34\n"
"s_xor_b32 s35, s35, s36\n"
"s_bitcmp1_b32 s24, 0\n"
"s_cselect_b32 s71, s30, s28\n"
"s_cselect_b32 s72, s31, s29\n"
"s_bitcmp1_b32 s24, 1\n"
"s_cselect_b32 s71, s72, s71\n"
"s_cmp_lt_u32 s98, 63\n"
"s_cselect_b32 s71, s71, s27\n"
"s_add_u32 s97, s71, s33\n"
"s_add_u32 s56, s71, s41\n"
"s_add_u32 s57, s56, 524288\n"
"s_add_u32 s58, s56, s68\n"
"s_add_u32 s72, s24, 1\n"
"s_and_b32 s71, s72, 7\n"
"s_lshl_b32 s71, s71, 15\n"
"s_add_u32 s71, s71, s43\n"
"s_add_u32 s61, s71, s35\n"
"s_sub_u32 s73, s24, 1\n"
"s_and_b32 s73, s73, 7\n"
"s_lshl_b32 s73, s73, 15\n"
"s_add_u32 s73, s73, s43\n"
"s_add_u32 s62, s73, s35\n"
"s_and_b32 s66, s24, 31\n"
"s_lshl_b32 s66, s66, 22\n"
"s_add_u32 s66, s66, s40\n"
"s_and_b32 s72, s72, 31\n"
"s_lshl_b32 s72, s72, 22\n"
"s_add_u32 s63, s72, s37\n"
"s_add_u32 s70, s22, 1\n"
"s_cmp_lt_u32 s22, 64\n"
"s_cselect_b32 s71, s71, s27\n"
"s_cselect_b32 s63, s63, s27\n"
"s_cselect_b32 s70, s70, 0\n"
"s_cmp_gt_u32 s65, s70\n"
"s_cselect_b32 s23, s27, s69\n"
"s_add_u32 s50, s71, s39\n"
"s_add_u32 s51, s50, 4096\n"
"s_add_u32 s52, s50, s53\n"
"s_cmp_ge_i32 s24, 142\n"
"s_cbranch_scc1 X_end_%=\n"
"s_waitcnt vmcnt(1) lgkmcnt(0)\n"
"v_cmp_gt_u32 vcc, s70, v18\n"
"s_nop 0\n"
"s_and_b32 s72, vcc_lo, 0xff\n"
"s_cselect_b32 s49, s27, 0\n"
"s_or_b32 s76, s63, s49\n"
"s_add_u32 s77, s76, 4096\n"
"s_add_u32 s78, s76, s38\n"
"s_add_u32 m0, s96, 0\n"
"s_nop 0\n"
"buffer_load_dwordx4 v0, s[12:15], s76 offen sc1 lds\n"
"s_add_u32 m0, s96, 4096\n"
"s_nop 0\n"
"buffer_load_dwordx4 v0, s[12:15], s77 offen sc1 lds\n"
"s_add_u32 m0, s96, 8192\n"
"s_nop 0\n"
"buffer_load_dwordx4 v0, s[12:15], s78 offen sc1 lds\n"
"s_nop 15\n"
"buffer_load_dwordx4 v[80:83], v0, s[12:15], s59 offen offset:0 sc1\n"
"buffer_load_dwordx4 v[84:87], v0, s[12:15], s59 offen offset:1024 sc1\n"
"buffer_load_dwordx4 v[88:91], v0, s[12:15], s59 offen offset:2048 sc1\n"
"buffer_load_dwordx4 v[92:95], v0, s[12:15], s59 offen offset:3072 sc1\n"
"buffer_load_dwordx4 v[96:99], v0, s[12:15], s60 offen offset:0 sc1\n"
"buffer_load_dwordx4 v[100:103], v0, s[12:15], s60 offen offset:1024 sc1\n"
"buffer_load_dwordx4 v[104:107], v0, s[12:15], s60 offen offset:2048 sc1\n"
"buffer_load_dwordx4 v[108:111], v0, s[12:15], s60 offen offset:3072 sc1\n"
"s_waitcnt vmcnt(6)\n"
"v_smfmac_f32_16x16x64_bf16 v[72:75], v[14:17], v[80:87], v1 abid:1\n"
"s_waitcnt vmcnt(4)\n"
"v_smfmac_f32_16x16x64_bf16 v[72:75], v[14:17], v[88:95], v1 abid:1\n"
"s_waitcnt vmcnt(2)\n"
"v_smfmac_f32_16x16x64_bf16 v[72:75], v[14:17], v[96:103], v1 abid:1\n"
"s_waitcnt vmcnt(0)\n"
"v_smfmac_f32_16x16x64_bf16 v[72:75], v[14:17], v[104:111], v1 abid:1\n"
"s_nop 7\n"
"v_add_f32_dpp v72, v72, v72 row_ror:8 row_mask:0xf bank_mask:0xf\n"
"v_add_f32_dpp v73, v73, v73 row_ror:8 row_mask:0xf bank_mask:0xf\n"
"v_add_f32_dpp v74, v74, v74 row_ror:8 row_mask:0xf bank_mask:0xf\n"
"v_add_f32_dpp v75, v75, v75 row_ror:8 row_mask:0xf bank_mask:0xf\n"
"v_pk_add_f32 v[60:61], v[72:73], v[74:75]\n"
"v_cmp_u_f32 vcc, v60, v61\n"
"s_cbranch_vccnz X_lretry_%=\n"
"X_lretry_ret_%=:\n"
"v_cvt_pk_bf16_f32 v72, v72, v73\n"
"v_cvt_pk_bf16_f32 v73, v74, v75\n"
"buffer_store_dwordx2 v[72:73], v4, s[12:15], s61 offen sc1\n"
"s_barrier\n"
"s_nop 15\n"
"s_nop 15\n"
"s_nop 15\n"
"s_nop 15\n"
"s_nop 15\n"
"s_nop 15\n"
"s_nop 15\n"
"s_nop 15\n"
"s_add_u32 m0, s96, 18944\n"
"s_nop 0\n"
"buffer_load_dwordx4 v0, s[12:15], s50 offen sc1 lds\n"
"s_add_u32 m0, s96, 23040\n"
"s_nop 0\n"
"buffer_load_dwordx4 v0, s[12:15], s51 offen sc1 lds\n"
"s_add_u32 m0, s96, 27136\n"
"s_nop 0\n"
"buffer_load_dwordx4 v0, s[12:15], s52 offen sc1 lds\n"
"ds_read_b128 v[144:147], v6 offset:0\n"
"ds_read_b128 v[148:151], v6 offset:1024\n"
"ds_read_b128 v[152:155], v6 offset:2048\n"
"ds_read_b128 v[156:159], v6 offset:3072\n"
"v_mov_b64 v[76:77], 0\n"
"v_mov_b64 v[78:79], 0\n"
"s_cmp_lg_u32 s49, 0\n"
"s_cbranch_scc1 X_kvslow7_%=\n"
"X_kvslow_ret7_%=:\n"
"v_mfma_f32_32x32x16_bf16 v[60:75], v[76:79], v[76:79], 0\n"
"s_waitcnt vmcnt(0)\n"
"ds_read_b128 v[112:115], v12 offset:18944\n"
"ds_read_b128 v[116:119], v12 offset:23040\n"
"ds_read_b128 v[120:123], v12 offset:27136\n"
"v_min_u32_dpp v18, v18, v18 quad_perm:[1,0,3,2] row_mask:0xf bank_mask:0xf\n"
"s_nop 1\n"
"v_min_u32_dpp v18, v18, v18 quad_perm:[2,3,0,1] row_mask:0xf bank_mask:0xf\n"
"s_nop 1\n"
"v_min_u32_dpp v18, v18, v18 row_half_mirror row_mask:0xf bank_mask:0xf\n"
"s_nop 0\n"
"v_readfirstlane_b32 s72, v18\n"
"s_nop 0\n"
"s_max_u32 s65, s65, s72\n"
"s_waitcnt lgkmcnt(0)\n"
"v_max3_u32 v124, v112, v113, v114\n"
"v_max3_u32 v125, v115, v116, v117\n"
"v_max3_u32 v124, v124, v118, v119\n"
"v_max3_u32 v125, v125, v120, v121\n"
"v_max3_u32 v124, v124, v122, v123\n"
"v_max_u32 v124, v124, v125\n"
"v_cmp_eq_u32 vcc, -1, v124\n"
"s_cbranch_vccnz X_mslow7_%=\n"
"X_mslow_ret7_%=:\n"
"s_cmp_eq_u32 s70, 0\n"
"s_cbranch_scc1 X_lightZ_%=\n"
"s_branch X_bodyZ_%=\n"
"X_lretry_%=:\n"
"s_nop 15\n"
"s_waitcnt vmcnt(0)\n"
"X_lretry_l_%=:\n"
"v_max3_u32 v72, v80, v81, v82\n"
"v_max3_u32 v72, v72, v83, v84\n"
"v_max3_u32 v72, v72, v85, v86\n"
"v_max3_u32 v72, v72, v87, v88\n"
"v_max3_u32 v72, v72, v89, v90\n"
"v_max3_u32 v72, v72, v91, v92\n"
"v_max3_u32 v72, v72, v93, v94\n"
"v_max3_u32 v72, v72, v95, v96\n"
"v_max3_u32 v72, v72, v97, v98\n"
"v_max3_u32 v72, v72, v99, v100\n"
"v_max3_u32 v72, v72, v101, v102\n"
"v_max3_u32 v72, v72, v103, v104\n"
"v_max3_u32 v72, v72, v105, v106\n"
"v_max3_u32 v72, v72, v107, v108\n"
"v_max3_u32 v72, v72, v109, v110\n"
"v_max_u32 v72, v72, v111\n"
"v_cmp_eq_u32 vcc, -1, v72\n"
"s_cbranch_vccz X_lretry_g_%=\n"
"buffer_load_dwordx4 v[80:83], v0, s[12:15], s59 offen offset:0 sc1\n"
"buffer_load_dwordx4 v[84:87], v0, s[12:15], s59 offen offset:1024 sc1\n"
"buffer_load_dwordx4 v[88:91], v0, s[12:15], s59 offen offset:2048 sc1\n"
"buffer_load_dwordx4 v[92:95], v0, s[12:15], s59 offen offset:3072 sc1\n"
"buffer_load_dwordx4 v[96:99], v0, s[12:15], s60 offen offset:0 sc1\n"
"buffer_load_dwordx4 v[100:103], v0, s[12:15], s60 offen offset:1024 sc1\n"
"buffer_load_dwordx4 v[104:107], v0, s[12:15], s60 offen offset:2048 sc1\n"
"buffer_load_dwordx4 v[108:111], v0, s[12:15], s60 offen offset:3072 sc1\n"
"s_waitcnt vmcnt(0)\n"
"s_add_u32 s25, s25, 1\n"
"s_cmp_lt_u32 s25, 200000\n"
"s_cbranch_scc1 X_lretry_l_%=\n"
"X_lretry_g_%=:\n"
"v_mov_b64 v[72:73], 0\n"
"v_mov_b64 v[74:75], 0\n"
"s_nop 1\n"
"v_smfmac_f32_16x16x64_bf16 v[72:75], v[14:17], v[80:87], v1 abid:1\n"
"v_smfmac_f32_16x16x64_bf16 v[72:75], v[14:17], v[88:95], v1 abid:1\n"
"v_smfmac_f32_16x16x64_bf16 v[72:75], v[14:17], v[96:103], v1 abid:1\n"
"v_smfmac_f32_16x16x64_bf16 v[72:75], v[14:17], v[104:111], v1 abid:1\n"
"s_nop 7\n"
"v_add_f32_dpp v72, v72, v72 row_ror:8 row_mask:0xf bank_mask:0xf\n"
"v_add_f32_dpp v73, v73, v73 row_ror:8 row_mask:0xf bank_mask:0xf\n"
"v_add_f32_dpp v74, v74, v74 row_ror:8 row_mask:0xf bank_mask:0xf\n"
"v_add_f32_dpp v75, v75, v75 row_ror:8 row_mask:0xf bank_mask:0xf\n"
"s_nop 15\n"
"s_branch X_lretry_ret_%=\n"
"X_mslow2_%=:\n"
"X_mslow_l2_%=:\n"
"s_add_u32 m0, s96, 18944\n"
"s_nop 0\n"
"buffer_load_dwordx4 v0, s[12:15], s50 offen sc1 lds\n"
"s_add_u32 m0, s96, 23040\n"
"s_nop 0\n"
"buffer_load_dwordx4 v0, s[12:15], s51 offen sc1 lds\n"
"s_add_u32 m0, s96, 27136\n"
"s_nop 0\n"
"buffer_load_dwordx4 v0, s[12:15], s52 offen sc1 lds\n"
"s_waitcnt vmcnt(0)\n"
"ds_read_b128 v[112:115], v12 offset:18944\n"
"ds_read_b128 v[116:119], v12 offset:23040\n"
"ds_read_b128 v[120:123], v12 offset:27136\n"
"s_waitcnt lgkmcnt(0)\n"
"v_max3_u32 v124, v112, v113, v114\n"
"v_max3_u32 v125, v115, v116, v117\n"
"v_max3_u32 v124, v124, v118, v119\n"
"v_max3_u32 v125, v125, v120, v121\n"
"v_max3_u32 v124, v124, v122, v123\n"
"v_max_u32 v124, v124, v125\n"
"v_cmp_eq_u32 vcc, -1, v124\n"
"s_cbranch_vccz X_mslow_g2_%=\n"
"s_add_u32 s25, s25, 1\n"
"s_cmp_lt_u32 s25, 200000\n"
"s_cbranch_scc1 X_mslow_l2_%=\n"
"X_mslow_g2_%=:\n"
"s_branch X_mslow_ret2_%=\n"
"X_mslow3_%=:\n"
"X_mslow_l3_%=:\n"
"s_add_u32 m0, s96, 18944\n"
"s_nop 0\n"
"buffer_load_dwordx4 v0, s[12:15], s50 offen sc1 lds\n"
"s_add_u32 m0, s96, 23040\n"
"s_nop 0\n"
"buffer_load_dwordx4 v0, s[12:15], s51 offen sc1 lds\n"
"s_add_u32 m0, s96, 27136\n"
"s_nop 0\n"
"buffer_load_dwordx4 v0, s[12:15], s52 offen sc1 lds\n"
"s_waitcnt vmcnt(0)\n"
"ds_read_b128 v[112:115], v12 offset:18944\n"
"ds_read_b128 v[116:119], v12 offset:23040\n"
"ds_read_b128 v[120:123], v12 offset:27136\n"
"s_waitcnt lgkmcnt(0)\n"
"v_max3_u32 v124, v112, v113, v114\n"
"v_max3_u32 v125, v115, v116, v117\n"
"v_max3_u32 v124, v124, v118, v119\n"
"v_max3_u32 v125, v125, v120, v121\n"
"v_max3_u32 v124, v124, v122, v123\n"
"v_max_u32 v124, v124, v125\n"
"v_cmp_eq_u32 vcc, -1, v124\n"
"s_cbranch_vccz X_mslow_g3_%=\n"
"s_add_u32 s25, s25, 1\n"
"s_cmp_lt_u32 s25, 200000\n"
"s_cbranch_scc1 X_mslow_l3_%=\n"
"X_mslow_g3_%=:\n"
"s_branch X_mslow_ret3_%=\n"
"X_mslow4_%=:\n"
"X_mslow_l4_%=:\n"
"s_add_u32 m0, s96, 18944\n"
"s_nop 0\n"
"buffer_load_dwordx4 v0, s[12:15], s50 offen sc1 lds\n"
"s_add_u32 m0, s96, 23040\n"
"s_nop 0\n"
"buffer_load_dwordx4 v0, s[12:15], s51 offen sc1 lds\n"
"s_add_u32 m0, s96, 27136\n"
"s_nop 0\n"
"buffer_load_dwordx4 v0, s[12:15], s52 offen sc1 lds\n"
"s_waitcnt vmcnt(0)\n"
"ds_read_b128 v[112:115], v12 offset:18944\n"
"ds_read_b128 v[116:119], v12 offset:23040\n"
"ds_read_b128 v[120:123], v12 offset:27136\n"
"s_waitcnt lgkmcnt(0)\n"
"v_max3_u32 v124, v112, v113, v114\n"
"v_max3_u32 v125, v115, v116, v117\n"
"v_max3_u32 v124, v124, v118, v119\n"
"v_max3_u32 v125, v125, v120, v121\n"
"v_max3_u32 v124, v124, v122, v123\n"
"v_max_u32 v124, v124, v125\n"
"v_cmp_eq_u32 vcc, -1, v124\n"
"s_cbranch_vccz X_mslow_g4_%=\n"
"s_add_u32 s25, s25, 1\n"
"s_cmp_lt_u32 s25, 200000\n"
"s_cbranch_scc1 X_mslow_l4_%=\n"
"X_mslow_g4_%=:\n"
"s_branch X_mslow_ret4_%=\n"
"X_mslow5_%=:\n"
"X_mslow_l5_%=:\n"
"s_add_u32 m0, s96, 18944\n"
"s_nop 0\n"
"buffer_load_dwordx4 v0, s[12:15], s50 offen sc1 lds\n"
"s_add_u32 m0, s96, 23040\n"
"s_nop 0\n"
"buffer_load_dwordx4 v0, s[12:15], s51 offen sc1 lds\n"
"s_add_u32 m0, s96, 27136\n"
"s_nop 0\n"
"buffer_load_dwordx4 v0, s[12:15], s52 offen sc1 lds\n"
"s_waitcnt vmcnt(0)\n"
"ds_read_b128 v[112:115], v12 offset:18944\n"
"ds_read_b128 v[116:119], v12 offset:23040\n"
"ds_read_b128 v[120:123], v12 offset:27136\n"
"s_waitcnt lgkmcnt(0)\n"
"v_max3_u32 v124, v112, v113, v114\n"
"v_max3_u32 v125, v115, v116, v117\n"
"v_max3_u32 v124, v124, v118, v119\n"
"v_max3_u32 v125, v125, v120, v121\n"
"v_max3_u32 v124, v124, v122, v123\n"
"v_max_u32 v124, v124, v125\n"
"v_cmp_eq_u32 vcc, -1, v124\n"
"s_cbranch_vccz X_mslow_g5_%=\n"
"s_add_u32 s25, s25, 1\n"
"s_cmp_lt_u32 s25, 200000\n"
"s_cbranch_scc1 X_mslow_l5_%=\n"
"X_mslow_g5_%=:\n"
"s_branch X_mslow_ret5_%=\n"
"X_mslow6_%=:\n"
"X_mslow_l6_%=:\n"
"s_add_u32 m0, s96, 18944\n"
"s_nop 0\n"
"buffer_load_dwordx4 v0, s[12:15], s50 offen sc1 lds\n"
"s_add_u32 m0, s96, 23040\n"
"s_nop 0\n"
"buffer_load_dwordx4 v0, s[12:15], s51 offen sc1 lds\n"
"s_add_u32 m0, s96, 27136\n"
"s_nop 0\n"
"buffer_load_dwordx4 v0, s[12:15], s52 offen sc1 lds\n"
"s_waitcnt vmcnt(0)\n"
"ds_read_b128 v[112:115], v12 offset:18944\n"
"ds_read_b128 v[116:119], v12 offset:23040\n"
"ds_read_b128 v[120:123], v12 offset:27136\n"
"s_waitcnt lgkmcnt(0)\n"
"v_max3_u32 v124, v112, v113, v114\n"
"v_max3_u32 v125, v115, v116, v117\n"
"v_max3_u32 v124, v124, v118, v119\n"
"v_max3_u32 v125, v125, v120, v121\n"
"v_max3_u32 v124, v124, v122, v123\n"
"v_max_u32 v124, v124, v125\n"
"v_cmp_eq_u32 vcc, -1, v124\n"
"s_cbranch_vccz X_mslow_g6_%=\n"
"s_add_u32 s25, s25, 1\n"
"s_cmp_lt_u32 s25, 200000\n"
"s_cbranch_scc1 X_mslow_l6_%=\n"
"X_mslow_g6_%=:\n"
"s_branch X_mslow_ret6_%=\n"
"X_mslow7_%=:\n"
"X_mslow_l7_%=:\n"
"s_add_u32 m0, s96, 18944\n"
"s_nop 0\n"
"buffer_load_dwordx4 v0, s[12:15], s50 offen sc1 lds\n"
"s_add_u32 m0, s96, 23040\n"
"s_nop 0\n"
"buffer_load_dwordx4 v0, s[12:15], s51 offen sc1 lds\n"
"s_add_u32 m0, s96, 27136\n"
"s_nop 0\n"
"buffer_load_dwordx4 v0, s[12:15], s52 offen sc1 lds\n"
"s_waitcnt vmcnt(0)\n"
"ds_read_b128 v[112:115], v12 offset:18944\n"
"ds_read_b128 v[116:119], v12 offset:23040\n"
"ds_read_b128 v[120:123], v12 offset:27136\n"
"s_waitcnt lgkmcnt(0)\n"
"v_max3_u32 v124, v112, v113, v114\n"
"v_max3_u32 v125, v115, v116, v117\n"
"v_max3_u32 v124, v124, v118, v119\n"
"v_max3_u32 v125, v125, v120, v121\n"
"v_max3_u32 v124, v124, v122, v123\n"
"v_max_u32 v124, v124, v125\n"
"v_cmp_eq_u32 vcc, -1, v124\n"
"s_cbranch_vccz X_mslow_g7_%=\n"
"s_add_u32 s25, s25, 1\n"
"s_cmp_lt_u32 s25, 200000\n"
"s_cbranch_scc1 X_mslow_l7_%=\n"
"X_mslow_g7_%=:\n"
"s_branch X_mslow_ret7_%=\n"
"X_kvslow1_%=:\n"
"s_waitcnt vmcnt(0)\n"
"X_kvslow_l1_%=:\n"
"buffer_load_dword v18, v5, s[12:15], s69 offen sc1\n"
"s_waitcnt vmcnt(0)\n"
"v_cmp_gt_u32 vcc, s70, v18\n"
"s_nop 0\n"
"s_and_b32 s72, vcc_lo, 0xff\n"
"s_cbranch_scc0 X_kvslow_g1_%=\n"
"s_sleep 2\n"
"s_add_u32 s25, s25, 1\n"
"s_cmp_lt_u32 s25, 200000\n"
"s_cbranch_scc1 X_kvslow_l1_%=\n"
"X_kvslow_g1_%=:\n"
"s_mov_b32 s49, 0\n"
"s_or_b32 s76, s63, s49\n"
"s_add_u32 s77, s76, 4096\n"
"s_add_u32 s78, s76, s38\n"
"s_add_u32 m0, s96, 0\n"
"s_nop 0\n"
"buffer_load_dwordx4 v0, s[12:15], s76 offen sc1 lds\n"
"s_add_u32 m0, s96, 4096\n"
"s_nop 0\n"
"buffer_load_dwordx4 v0, s[12:15], s77 offen sc1 lds\n"
"s_add_u32 m0, s96, 8192\n"
"s_nop 0\n"
"buffer_load_dwordx4 v0, s[12:15], s78 offen sc1 lds\n"
"s_waitcnt vmcnt(0)\n"
"s_branch X_kvslow_ret1_%=\n"
"X_kvslow2_%=:\n"
"s_waitcnt vmcnt(0)\n"
"X_kvslow_l2_%=:\n"
"buffer_load_dword v18, v5, s[12:15], s69 offen sc1\n"
"s_waitcnt vmcnt(0)\n"
"v_cmp_gt_u32 vcc, s70, v18\n"
"s_nop 0\n"
"s_and_b32 s72, vcc_lo, 0xff\n"
"s_cbranch_scc0 X_kvslow_g2_%=\n"
"s_sleep 2\n"
"s_add_u32 s25, s25, 1\n"
"s_cmp_lt_u32 s25, 200000\n"
"s_cbranch_scc1 X_kvslow_l2_%=\n"
"X_kvslow_g2_%=:\n"
"s_mov_b32 s49, 0\n"
"s_or_b32 s76, s63, s49\n"
"s_add_u32 s77, s76, 4096\n"
"s_add_u32 s78, s76, s38\n"
"s_add_u32 m0, s96, 0\n"
"s_nop 0\n"
"buffer_load_dwordx4 v0, s[12:15], s76 offen sc1 lds\n"
"s_add_u32 m0, s96, 4096\n"
"s_nop 0\n"
"buffer_load_dwordx4 v0, s[12:15], s77 offen sc1 lds\n"
"s_add_u32 m0, s96, 8192\n"
"s_nop 0\n"
"buffer_load_dwordx4 v0, s[12:15], s78 offen sc1 lds\n"
"s_waitcnt vmcnt(0)\n"
"s_branch X_kvslow_ret2_%=\n"
"X_kvslow3_%=:\n"
"s_waitcnt vmcnt(0)\n"
"X_kvslow_l3_%=:\n"
"buffer_load_dword v18, v5, s[12:15], s69 offen sc1\n"
"s_waitcnt vmcnt(0)\n"
"v_cmp_gt_u32 vcc, s70, v18\n"
"s_nop 0\n"
"s_and_b32 s72, vcc_lo, 0xff\n"
"s_cbranch_scc0 X_kvslow_g3_%=\n"
"s_sleep 2\n"
"s_add_u32 s25, s25, 1\n"
"s_cmp_lt_u32 s25, 200000\n"
"s_cbranch_scc1 X_kvslow_l3_%=\n"
"X_kvslow_g3_%=:\n"
"s_mov_b32 s49, 0\n"
"s_or_b32 s76, s63, s49\n"
"s_add_u32 s77, s76, 4096\n"
"s_add_u32 s78, s76, s38\n"
"s_add_u32 m0, s96, 0\n"
"s_nop 0\n"
"buffer_load_dwordx4 v0, s[12:15], s76 offen sc1 lds\n"
"s_add_u32 m0, s96, 4096\n"
"s_nop 0\n"
"buffer_load_dwordx4 v0, s[12:15], s77 offen sc1 lds\n"
"s_add_u32 m0, s96, 8192\n"
"s_nop 0\n"
"buffer_load_dwordx4 v0, s[12:15], s78 offen sc1 lds\n"
"s_waitcnt vmcnt(0)\n"
"s_branch X_kvslow_ret3_%=\n"
"X_kvslow4_%=:\n"
"s_waitcnt vmcnt(0)\n"
"X_kvslow_l4_%=:\n"
"buffer_load_dword v18, v5, s[12:15], s69 offen sc1\n"
"s_waitcnt vmcnt(0)\n"
"v_cmp_gt_u32 vcc, s70, v18\n"
"s_nop 0\n"
"s_and_b32 s72, vcc_lo, 0xff\n"
"s_cbranch_scc0 X_kvslow_g4_%=\n"
"s_sleep 2\n"
"s_add_u32 s25, s25, 1\n"
"s_cmp_lt_u32 s25, 200000\n"
"s_cbranch_scc1 X_kvslow_l4_%=\n"
"X_kvslow_g4_%=:\n"
"s_mov_b32 s49, 0\n"
"s_or_b32 s76, s63, s49\n"
"s_add_u32 s77, s76, 4096\n"
"s_add_u32 s78, s76, s38\n"
"s_add_u32 m0, s96, 0\n"
"s_nop 0\n"
"buffer_load_dwordx4 v0, s[12:15], s76 offen sc1 lds\n"
"s_add_u32 m0, s96, 4096\n"
"s_nop 0\n"
"buffer_load_dwordx4 v0, s[12:15], s77 offen sc1 lds\n"
"s_add_u32 m0, s96, 8192\n"
"s_nop 0\n"
"buffer_load_dwordx4 v0, s[12:15], s78 offen sc1 lds\n"
"s_waitcnt vmcnt(0)\n"
"s_branch X_kvslow_ret4_%=\n"
"X_kvslow5_%=:\n"
"s_waitcnt vmcnt(0)\n"
"X_kvslow_l5_%=:\n"
"buffer_load_dword v18, v5, s[12:15], s69 offen sc1\n"
"s_waitcnt vmcnt(0)\n"
"v_cmp_gt_u32 vcc, s70, v18\n"
"s_nop 0\n"
"s_and_b32 s72, vcc_lo, 0xff\n"
"s_cbranch_scc0 X_kvslow_g5_%=\n"
"s_sleep 2\n"
"s_add_u32 s25, s25, 1\n"
"s_cmp_lt_u32 s25, 200000\n"
"s_cbranch_scc1 X_kvslow_l5_%=\n"
"X_kvslow_g5_%=:\n"
"s_mov_b32 s49, 0\n"
"s_or_b32 s76, s63, s49\n"
"s_add_u32 s77, s76, 4096\n"
"s_add_u32 s78, s76, s38\n"
"s_add_u32 m0, s96, 0\n"
"s_nop 0\n"
"buffer_load_dwordx4 v0, s[12:15], s76 offen sc1 lds\n"
"s_add_u32 m0, s96, 4096\n"
"s_nop 0\n"
"buffer_load_dwordx4 v0, s[12:15], s77 offen sc1 lds\n"
"s_add_u32 m0, s96, 8192\n"
"s_nop 0\n"
"buffer_load_dwordx4 v0, s[12:15], s78 offen sc1 lds\n"
"s_waitcnt vmcnt(0)\n"
"s_branch X_kvslow_ret5_%=\n"
"X_kvslow6_%=:\n"
"s_waitcnt vmcnt(0)\n"
"X_kvslow_l6_%=:\n"
"buffer_load_dword v18, v5, s[12:15], s69 offen sc1\n"
"s_waitcnt vmcnt(0)\n"
"v_cmp_gt_u32 vcc, s70, v18\n"
"s_nop 0\n"
"s_and_b32 s72, vcc_lo, 0xff\n"
"s_cbranch_scc0 X_kvslow_g6_%=\n"
"s_sleep 2\n"
"s_add_u32 s25, s25, 1\n"
"s_cmp_lt_u32 s25, 200000\n"
"s_cbranch_scc1 X_kvslow_l6_%=\n"
"X_kvslow_g6_%=:\n"
"s_mov_b32 s49, 0\n"
"s_or_b32 s76, s63, s49\n"
"s_add_u32 s77, s76, 4096\n"
"s_add_u32 s78, s76, s38\n"
"s_add_u32 m0, s96, 0\n"
"s_nop 0\n"
"buffer_load_dwordx4 v0, s[12:15], s76 offen sc1 lds\n"
"s_add_u32 m0, s96, 4096\n"
"s_nop 0\n"
"buffer_load_dwordx4 v0, s[12:15], s77 offen sc1 lds\n"
"s_add_u32 m0, s96, 8192\n"
"s_nop 0\n"
"buffer_load_dwordx4 v0, s[12:15], s78 offen sc1 lds\n"
"s_waitcnt vmcnt(0)\n"
"s_branch X_kvslow_ret6_%=\n"
"X_kvslow7_%=:\n"
"s_waitcnt vmcnt(0)\n"
"X_kvslow_l7_%=:\n"
"buffer_load_dword v18, v5, s[12:15], s69 offen sc1\n"
"s_waitcnt vmcnt(0)\n"
"v_cmp_gt_u32 vcc, s70, v18\n"
"s_nop 0\n"
"s_and_b32 s72, vcc_lo, 0xff\n"
"s_cbranch_scc0 X_kvslow_g7_%=\n"
"s_sleep 2\n"
"s_add_u32 s25, s25, 1\n"
"s_cmp_lt_u32 s25, 200000\n"
"s_cbranch_scc1 X_kvslow_l7_%=\n"
"X_kvslow_g7_%=:\n"
"s_mov_b32 s49, 0\n"
"s_or_b32 s76, s63, s49\n"
"s_add_u32 s77, s76, 4096\n"
"s_add_u32 s78, s76, s38\n"
"s_add_u32 m0, s96, 0\n"
"s_nop 0\n"
"buffer_load_dwordx4 v0, s[12:15], s76 offen sc1 lds\n"
"s_add_u32 m0, s96, 4096\n"
"s_nop 0\n"
"buffer_load_dwordx4 v0, s[12:15], s77 offen sc1 lds\n"
"s_add_u32 m0, s96, 8192\n"
"s_nop 0\n"
"buffer_load_dwordx4 v0, s[12:15], s78 offen sc1 lds\n"
"s_waitcnt vmcnt(0)\n"
"s_branch X_kvslow_ret7_%=\n"
"X_end_%=:\n"
"s_waitcnt vmcnt(0) lgkmcnt(0)\n"
"s_endpgm\n"
"ROLE1_%=:\n"
"s_load_dwordx8 s[52:59], %0, 16\n"
"s_load_dwordx4 s[60:63], %0, 48\n"
"s_load_dwordx2 s[12:13], %0, 72\n"
"s_mov_b32 s21, %1\n"
"s_mov_b32 s20, %2\n"
"s_mov_b32 s22, 0x40000000\n"
"s_mov_b32 s23, 0\n"
"v_mbcnt_lo_u32_b32 v10, -1, 0\n"
"v_mbcnt_hi_u32_b32 v10, -1, v10\n"
"v_lshlrev_b32 v0, 4, v10\n"
"v_and_b32 v11, 15, v10\n"
"v_lshrrev_b32 v12, 4, v10\n"
"s_waitcnt lgkmcnt(0)\n"
"s_and_b32 s13, s13, 0xffff\n"
"s_mov_b32 s14, 149946368\n"
"s_mov_b32 s15, 0x20000\n"
"s_lshr_b32 s64, s21, 5\n"
"s_and_b32 s65, s21, 31\n"
"s_cmp_eq_u32 s64, 1\n"
"s_cselect_b32 s44, s54, s52\n"
"s_cselect_b32 s45, s55, s53\n"
"s_cmp_eq_u32 s64, 2\n"
"s_cselect_b32 s44, s56, s44\n"
"s_cselect_b32 s45, s57, s45\n"
"s_cmp_lg_u32 s64, 0\n"
"s_cbranch_scc1 Q_nohold_%=\n"
"s_sleep 90\n"
"Q_nohold_%=:\n"
"s_mov_b32 s16, s44\n"
"s_and_b32 s17, s45, 0xffff\n"
"s_mov_b32 s18, 5242880\n"
"s_mov_b32 s19, 0x20000\n"
"s_lshl_b32 s46, s65, 8\n"
"s_lshr_b32 s40, s20, 1\n"
"s_lshl_b32 s40, s40, 1\n"
"v_lshl_or_b32 v13, v12, 2, s40\n"
"v_xor_b32 v13, v13, v11\n"
"v_lshlrev_b32 v13, 4, v13\n"
"v_mul_u32_u24 v14, 8192, v12\n"
"v_add_u32 v13, v13, v14\n"
"s_mul_i32 s40, s20, 32768\n"
"s_add_u32 s40, s40, s46\n"
"s_lshl_b32 s41, s20, 10\n"
"s_add_u32 m0, s41, 0\n"
"s_add_u32 s42, s40, 0\n"
"buffer_load_dwordx4 v13, s[16:19], s42 offen lds\n"
"s_add_u32 m0, s41, 4096\n"
"s_add_u32 s42, s40, 131072\n"
"buffer_load_dwordx4 v13, s[16:19], s42 offen lds\n"
"s_add_u32 m0, s41, 8192\n"
"s_add_u32 s42, s40, 262144\n"
"buffer_load_dwordx4 v13, s[16:19], s42 offen lds\n"
"s_add_u32 m0, s41, 12288\n"
"s_add_u32 s42, s40, 393216\n"
"buffer_load_dwordx4 v13, s[16:19], s42 offen lds\n"
"s_add_u32 m0, s41, 16384\n"
"s_add_u32 s42, s40, 524288\n"
"buffer_load_dwordx4 v13, s[16:19], s42 offen lds\n"
"s_add_u32 m0, s41, 20480\n"
"s_add_u32 s42, s40, 655360\n"
"buffer_load_dwordx4 v13, s[16:19], s42 offen lds\n"
"s_add_u32 m0, s41, 24576\n"
"s_add_u32 s42, s40, 786432\n"
"buffer_load_dwordx4 v13, s[16:19], s42 offen lds\n"
"s_add_u32 m0, s41, 28672\n"
"s_add_u32 s42, s40, 917504\n"
"buffer_load_dwordx4 v13, s[16:19], s42 offen lds\n"
"s_add_u32 m0, s41, 32768\n"
"s_add_u32 s42, s40, 1048576\n"
"buffer_load_dwordx4 v13, s[16:19], s42 offen lds\n"
"s_add_u32 m0, s41, 36864\n"
"s_add_u32 s42, s40, 1179648\n"
"buffer_load_dwordx4 v13, s[16:19], s42 offen lds\n"
"s_add_u32 m0, s41, 40960\n"
"s_add_u32 s42, s40, 1310720\n"
"buffer_load_dwordx4 v13, s[16:19], s42 offen lds\n"
"s_add_u32 m0, s41, 45056\n"
"s_add_u32 s42, s40, 1441792\n"
"buffer_load_dwordx4 v13, s[16:19], s42 offen lds\n"
"s_add_u32 m0, s41, 49152\n"
"s_add_u32 s42, s40, 1572864\n"
"buffer_load_dwordx4 v13, s[16:19], s42 offen lds\n"
"s_add_u32 m0, s41, 53248\n"
"s_add_u32 s42, s40, 1703936\n"
"buffer_load_dwordx4 v13, s[16:19], s42 offen lds\n"
"s_add_u32 m0, s41, 57344\n"
"s_add_u32 s42, s40, 1835008\n"
"buffer_load_dwordx4 v13, s[16:19], s42 offen lds\n"
"s_add_u32 m0, s41, 61440\n"
"s_add_u32 s42, s40, 1966080\n"
"buffer_load_dwordx4 v13, s[16:19], s42 offen lds\n"
"s_add_u32 m0, s41, 65536\n"
"s_add_u32 s42, s40, 2097152\n"
"buffer_load_dwordx4 v13, s[16:19], s42 offen lds\n"
"s_add_u32 m0, s41, 69632\n"
"s_add_u32 s42, s40, 2228224\n"
"buffer_load_dwordx4 v13, s[16:19], s42 offen lds\n"
"s_add_u32 m0, s41, 73728\n"
"s_add_u32 s42, s40, 2359296\n"
"buffer_load_dwordx4 v13, s[16:19], s42 offen lds\n"
"s_add_u32 m0, s41, 77824\n"
"s_add_u32 s42, s40, 2490368\n"
"buffer_load_dwordx4 v13, s[16:19], s42 offen lds\n"
"s_add_u32 m0, s41, 81920\n"
"s_add_u32 s42, s40, 2621440\n"
"buffer_load_dwordx4 v13, s[16:19], s42 offen lds\n"
"s_add_u32 m0, s41, 86016\n"
"s_add_u32 s42, s40, 2752512\n"
"buffer_load_dwordx4 v13, s[16:19], s42 offen lds\n"
"s_add_u32 m0, s41, 90112\n"
"s_add_u32 s42, s40, 2883584\n"
"buffer_load_dwordx4 v13, s[16:19], s42 offen lds\n"
"s_add_u32 m0, s41, 94208\n"
"s_add_u32 s42, s40, 3014656\n"
"buffer_load_dwordx4 v13, s[16:19], s42 offen lds\n"
"s_add_u32 m0, s41, 98304\n"
"s_add_u32 s42, s40, 3145728\n"
"buffer_load_dwordx4 v13, s[16:19], s42 offen lds\n"
"s_add_u32 m0, s41, 102400\n"
"s_add_u32 s42, s40, 3276800\n"
"buffer_load_dwordx4 v13, s[16:19], s42 offen lds\n"
"s_add_u32 m0, s41, 106496\n"
"s_add_u32 s42, s40, 3407872\n"
"buffer_load_dwordx4 v13, s[16:19], s42 offen lds\n"
"s_add_u32 m0, s41, 110592\n"
"s_add_u32 s42, s40, 3538944\n"
"buffer_load_dwordx4 v13, s[16:19], s42 offen lds\n"
"s_add_u32 m0, s41, 114688\n"
"s_add_u32 s42, s40, 3670016\n"
"buffer_load_dwordx4 v13, s[16:19], s42 offen lds\n"
"s_add_u32 m0, s41, 118784\n"
"s_add_u32 s42, s40, 3801088\n"
"buffer_load_dwordx4 v13, s[16:19], s42 offen lds\n"
"s_add_u32 m0, s41, 122880\n"
"s_add_u32 s42, s40, 3932160\n"
"buffer_load_dwordx4 v13, s[16:19], s42 offen lds\n"
"s_add_u32 m0, s41, 126976\n"
"s_add_u32 s42, s40, 4063232\n"
"buffer_load_dwordx4 v13, s[16:19], s42 offen lds\n"
"s_add_u32 m0, s41, 131072\n"
"s_add_u32 s42, s40, 4194304\n"
"buffer_load_dwordx4 v13, s[16:19], s42 offen lds\n"
"s_add_u32 m0, s41, 135168\n"
"s_add_u32 s42, s40, 4325376\n"
"buffer_load_dwordx4 v13, s[16:19], s42 offen lds\n"
"s_add_u32 m0, s41, 139264\n"
"s_add_u32 s42, s40, 4456448\n"
"buffer_load_dwordx4 v13, s[16:19], s42 offen lds\n"
"s_add_u32 m0, s41, 143360\n"
"s_add_u32 s42, s40, 4587520\n"
"buffer_load_dwordx4 v13, s[16:19], s42 offen lds\n"
"s_add_u32 m0, s41, 147456\n"
"s_add_u32 s42, s40, 4718592\n"
"buffer_load_dwordx4 v13, s[16:19], s42 offen lds\n"
"s_add_u32 m0, s41, 151552\n"
"s_add_u32 s42, s40, 4849664\n"
"buffer_load_dwordx4 v13, s[16:19], s42 offen lds\n"
"s_add_u32 m0, s41, 155648\n"
"s_add_u32 s42, s40, 4980736\n"
"buffer_load_dwordx4 v13, s[16:19], s42 offen lds\n"
"s_add_u32 m0, s41, 159744\n"
"s_add_u32 s42, s40, 5111808\n"
"buffer_load_dwordx4 v13, s[16:19], s42 offen lds\n"
"s_waitcnt vmcnt(0)\n"
"s_barrier\n"
"v_and_b32 v18, 3, v11\n"
"s_lshl_b32 s40, s20, 2\n"
"s_cmp_eq_u32 s64, 2\n"
"s_cselect_b64 vcc, -1, 0\n"
"v_lshrrev_b32 v19, 1, v18\n"
"v_add_u32 v19, 0, v19\n"
"v_cndmask_b32 v15, v18, v19, vcc\n"
"v_add_u32 v15, s40, v15\n"
"v_and_b32 v19, 1, v18\n"
"v_lshlrev_b32 v19, 3, v19\n"
"v_mov_b32 v20, 0\n"
"v_cndmask_b32 v19, v20, v19, vcc\n"
"v_lshrrev_b32 v16, 2, v11\n"
"v_and_b32 v17, 1, v12\n"
"v_lshlrev_b32 v17, 1, v17\n"
"v_lshl_or_b32 v17, v16, 2, v17\n"
"v_xor_b32 v17, v17, v15\n"
"v_lshlrev_b32 v17, 4, v17\n"
"v_add_u32 v17, v17, v19\n"
"v_lshl_add_u32 v16, v12, 3, v16\n"
"v_lshl_add_u32 v21, v16, 8, v17\n"
"v_lshrrev_b32 v19, 1, v18\n"
"v_add_u32 v19, 2, v19\n"
"v_cndmask_b32 v15, v18, v19, vcc\n"
"v_add_u32 v15, s40, v15\n"
"v_and_b32 v19, 1, v18\n"
"v_lshlrev_b32 v19, 3, v19\n"
"v_mov_b32 v20, 8\n"
"v_cndmask_b32 v19, v20, v19, vcc\n"
"v_lshrrev_b32 v16, 2, v11\n"
"v_and_b32 v17, 1, v12\n"
"v_lshlrev_b32 v17, 1, v17\n"
"v_lshl_or_b32 v17, v16, 2, v17\n"
"v_xor_b32 v17, v17, v15\n"
"v_lshlrev_b32 v17, 4, v17\n"
"v_add_u32 v17, v17, v19\n"
"v_lshl_add_u32 v16, v12, 3, v16\n"
"v_lshl_add_u32 v22, v16, 8, v17\n"
"ds_read_b64_tr_b16 a[0:1], v21 offset:0\n"
"ds_read_b64_tr_b16 a[2:3], v21 offset:1024\n"
"ds_read_b64_tr_b16 a[8:9], v21 offset:8192\n"
"ds_read_b64_tr_b16 a[10:11], v21 offset:9216\n"
"ds_read_b64_tr_b16 a[16:17], v21 offset:16384\n"
"ds_read_b64_tr_b16 a[18:19], v21 offset:17408\n"
"ds_read_b64_tr_b16 a[24:25], v21 offset:24576\n"
"ds_read_b64_tr_b16 a[26:27], v21 offset:25600\n"
"ds_read_b64_tr_b16 a[32:33], v21 offset:32768\n"
"ds_read_b64_tr_b16 a[34:35], v21 offset:33792\n"
"ds_read_b64_tr_b16 a[40:41], v21 offset:40960\n"
"ds_read_b64_tr_b16 a[42:43], v21 offset:41984\n"
"s_waitcnt lgkmcnt(0)\n"
"ds_read_b64_tr_b16 a[48:49], v21 offset:49152\n"
"ds_read_b64_tr_b16 a[50:51], v21 offset:50176\n"
"ds_read_b64_tr_b16 a[56:57], v21 offset:57344\n"
"ds_read_b64_tr_b16 a[58:59], v21 offset:58368\n"
"v_add_u32 v21, 65536, v21\n"
"ds_read_b64_tr_b16 a[64:65], v21 offset:0\n"
"ds_read_b64_tr_b16 a[66:67], v21 offset:1024\n"
"ds_read_b64_tr_b16 a[72:73], v21 offset:8192\n"
"ds_read_b64_tr_b16 a[74:75], v21 offset:9216\n"
"ds_read_b64_tr_b16 a[80:81], v21 offset:16384\n"
"ds_read_b64_tr_b16 a[82:83], v21 offset:17408\n"
"ds_read_b64_tr_b16 a[88:89], v21 offset:24576\n"
"ds_read_b64_tr_b16 a[90:91], v21 offset:25600\n"
"s_waitcnt lgkmcnt(0)\n"
"ds_read_b64_tr_b16 a[96:97], v21 offset:32768\n"
"ds_read_b64_tr_b16 a[98:99], v21 offset:33792\n"
"ds_read_b64_tr_b16 a[104:105], v21 offset:40960\n"
"ds_read_b64_tr_b16 a[106:107], v21 offset:41984\n"
"ds_read_b64_tr_b16 a[112:113], v21 offset:49152\n"
"ds_read_b64_tr_b16 a[114:115], v21 offset:50176\n"
"ds_read_b64_tr_b16 a[120:121], v21 offset:57344\n"
"ds_read_b64_tr_b16 a[122:123], v21 offset:58368\n"
"v_add_u32 v21, 65536, v21\n"
"ds_read_b64_tr_b16 a[128:129], v21 offset:0\n"
"ds_read_b64_tr_b16 a[130:131], v21 offset:1024\n"
"ds_read_b64_tr_b16 a[136:137], v21 offset:8192\n"
"ds_read_b64_tr_b16 a[138:139], v21 offset:9216\n"
"s_waitcnt lgkmcnt(0)\n"
"ds_read_b64_tr_b16 a[144:145], v21 offset:16384\n"
"ds_read_b64_tr_b16 a[146:147], v21 offset:17408\n"
"ds_read_b64_tr_b16 a[152:153], v21 offset:24576\n"
"ds_read_b64_tr_b16 a[154:155], v21 offset:25600\n"
"s_waitcnt lgkmcnt(0)\n"
"ds_read_b64_tr_b16 a[4:5], v22 offset:0\n"
"ds_read_b64_tr_b16 a[6:7], v22 offset:1024\n"
"ds_read_b64_tr_b16 a[12:13], v22 offset:8192\n"
"ds_read_b64_tr_b16 a[14:15], v22 offset:9216\n"
"ds_read_b64_tr_b16 a[20:21], v22 offset:16384\n"
"ds_read_b64_tr_b16 a[22:23], v22 offset:17408\n"
"ds_read_b64_tr_b16 a[28:29], v22 offset:24576\n"
"ds_read_b64_tr_b16 a[30:31], v22 offset:25600\n"
"ds_read_b64_tr_b16 a[36:37], v22 offset:32768\n"
"ds_read_b64_tr_b16 a[38:39], v22 offset:33792\n"
"ds_read_b64_tr_b16 a[44:45], v22 offset:40960\n"
"ds_read_b64_tr_b16 a[46:47], v22 offset:41984\n"
"s_waitcnt lgkmcnt(0)\n"
"ds_read_b64_tr_b16 a[52:53], v22 offset:49152\n"
"ds_read_b64_tr_b16 a[54:55], v22 offset:50176\n"
"ds_read_b64_tr_b16 a[60:61], v22 offset:57344\n"
"ds_read_b64_tr_b16 a[62:63], v22 offset:58368\n"
"v_add_u32 v22, 65536, v22\n"
"ds_read_b64_tr_b16 a[68:69], v22 offset:0\n"
"ds_read_b64_tr_b16 a[70:71], v22 offset:1024\n"
"ds_read_b64_tr_b16 a[76:77], v22 offset:8192\n"
"ds_read_b64_tr_b16 a[78:79], v22 offset:9216\n"
"ds_read_b64_tr_b16 a[84:85], v22 offset:16384\n"
"ds_read_b64_tr_b16 a[86:87], v22 offset:17408\n"
"ds_read_b64_tr_b16 a[92:93], v22 offset:24576\n"
"ds_read_b64_tr_b16 a[94:95], v22 offset:25600\n"
"s_waitcnt lgkmcnt(0)\n"
"ds_read_b64_tr_b16 a[100:101], v22 offset:32768\n"
"ds_read_b64_tr_b16 a[102:103], v22 offset:33792\n"
"ds_read_b64_tr_b16 a[108:109], v22 offset:40960\n"
"ds_read_b64_tr_b16 a[110:111], v22 offset:41984\n"
"ds_read_b64_tr_b16 a[116:117], v22 offset:49152\n"
"ds_read_b64_tr_b16 a[118:119], v22 offset:50176\n"
"ds_read_b64_tr_b16 a[124:125], v22 offset:57344\n"
"ds_read_b64_tr_b16 a[126:127], v22 offset:58368\n"
"v_add_u32 v22, 65536, v22\n"
"ds_read_b64_tr_b16 a[132:133], v22 offset:0\n"
"ds_read_b64_tr_b16 a[134:135], v22 offset:1024\n"
"ds_read_b64_tr_b16 a[140:141], v22 offset:8192\n"
"ds_read_b64_tr_b16 a[142:143], v22 offset:9216\n"
"s_waitcnt lgkmcnt(0)\n"
"ds_read_b64_tr_b16 a[148:149], v22 offset:16384\n"
"ds_read_b64_tr_b16 a[150:151], v22 offset:17408\n"
"ds_read_b64_tr_b16 a[156:157], v22 offset:24576\n"
"ds_read_b64_tr_b16 a[158:159], v22 offset:25600\n"
"s_waitcnt lgkmcnt(0)\n"
"s_barrier\n"
"v_lshlrev_b32 v13, 9, v12\n"
"v_lshl_add_u32 v7, v11, 4, v13\n"
"v_lshlrev_b32 v1, 7, v11\n"
"v_add_u32 v1, 6144, v1\n"
"Q_pol_%=:\n"
"buffer_load_dword v13, off, s[12:15], 0 sc1\n"
"s_waitcnt vmcnt(0)\n"
"v_readfirstlane_b32 s40, v13\n"
"s_add_u32 s23, s23, 1\n"
"s_cmp_ge_u32 s23, 400000\n"
"s_cselect_b32 s40, 16, s40\n"
"s_cmp_eq_u32 s40, 0\n"
"s_cbranch_scc1 Q_pol_%=\n"
"s_bitcmp1_b32 s40, 5\n"
"s_cselect_b32 s74, 8192, 0\n"
"s_xor_b32 s75, s74, 8192\n"
"s_cmp_eq_u32 s64, 2\n"
"s_cselect_b32 s40, 64, 0\n"
"s_add_u32 s40, s40, s21\n"
"s_add_u32 s40, s40, 128\n"
"s_lshl_b32 s40, s40, 7\n"
"s_add_u32 s40, s40, s75\n"
"s_lshl2_add_u32 s37, s20, s40\n"
"s_add_u32 s69, s74, 47104\n"
"s_cmp_eq_u32 s64, 2\n"
"s_cselect_b32 s40, 32, 0\n"
"s_cmp_eq_u32 s64, 0\n"
"s_cselect_b32 s40, 96, s40\n"
"s_cselect_b32 s41, s74, s74\n"
"s_add_u32 s40, s40, s65\n"
"s_lshl_b32 s66, s40, 14\n"
"s_add_u32 s66, s66, s41\n"
"s_lshl_b32 s40, s20, 11\n"
"s_add_u32 s66, s66, s40\n"
"s_add_u32 s66, s66, 10485760\n"
"s_add_u32 s68, s75, 12500992\n"
"s_cmp_eq_u32 s64, 2\n"
"s_cbranch_scc1 QV_entry_%=\n"
"s_mov_b32 s24, 0\n"
"s_mov_b32 s25, 0\n"
"s_mov_b32 s35, s22\n"
"s_mov_b32 s36, 0\n"
"v_cmp_gt_u32 vcc, 16, v10\n"
"v_mov_b32 v13, s22\n"
"s_nop 0\n"
"v_cndmask_b32 v1, v13, v1, vcc\n"
"s_lshl_b32 s34, s20, 10\n"
"s_sub_u32 s40, s24, s25\n"
"s_cmp_ge_u32 s40, 9\n"
"s_cselect_b32 s29, 1, 0\n"
"s_cmp_ge_u32 s24, 64\n"
"s_cselect_b32 s29, 1, s29\n"
"s_cmp_eq_u32 s29, 0\n"
"s_cselect_b32 s30, s24, s25\n"
"s_cselect_b32 s40, 1, 0\n"
"s_add_u32 s24, s24, s40\n"
"s_add_u32 s25, s25, s29\n"
"s_cmp_lt_u32 s30, 64\n"
"s_cselect_b32 s30, s30, -1\n"
"s_mul_i32 s38, s29, s69\n"
"s_mul_i32 s40, s29, 15\n"
"s_lshl1_add_u32 s40, s30, s40\n"
"s_and_b32 s40, s40, 31\n"
"s_lshl_b32 s40, s40, 22\n"
"s_add_u32 s40, s40, s68\n"
"s_mul_i32 s41, s30, 81920\n"
"s_add_u32 s41, s41, 144703488\n"
"s_cmp_eq_u32 s29, 0\n"
"s_cselect_b32 s40, s41, s40\n"
"s_lshl_b32 s41, s20, 10\n"
"s_add_u32 s31, s40, s41\n"
"Q_cold_%=:\n"
"s_waitcnt vmcnt(0)\n"
"v_mov_b32 v6, s36\n"
"buffer_store_dword v6, off, s[12:15], s35 sc1\n"
"s_mov_b32 s35, s22\n"
"s_cmp_lt_i32 s30, 0\n"
"s_cbranch_scc1 Q_end_%=\n"
"Q_spin_%=:\n"
"buffer_load_dword v2, v1, s[12:15], s38 offen sc1\n"
"s_waitcnt vmcnt(0)\n"
"v_cmp_ge_u32 vcc, s30, v2\n"
"s_nop 0\n"
"s_and_b32 s40, vcc_lo, 65535\n"
"s_cbranch_scc0 Q_go_%=\n"
"s_sleep 4\n"
"s_add_u32 s23, s23, 1\n"
"s_cmp_lt_u32 s23, 400000\n"
"s_cbranch_scc1 Q_spin_%=\n"
"Q_go_%=:\n"
"s_add_u32 m0, s34, 0\n"
"s_add_u32 s40, s31, 0\n"
"buffer_load_dwordx4 v0, s[12:15], s40 offen sc1 lds\n"
"s_add_u32 m0, s34, 4096\n"
"s_add_u32 s40, s31, 4096\n"
"buffer_load_dwordx4 v0, s[12:15], s40 offen sc1 lds\n"
"s_add_u32 m0, s34, 8192\n"
"s_add_u32 s40, s31, 16384\n"
"buffer_load_dwordx4 v0, s[12:15], s40 offen sc1 lds\n"
"s_add_u32 m0, s34, 12288\n"
"s_add_u32 s40, s31, 20480\n"
"buffer_load_dwordx4 v0, s[12:15], s40 offen sc1 lds\n"
"s_add_u32 m0, s34, 16384\n"
"s_add_u32 s40, s31, 32768\n"
"buffer_load_dwordx4 v0, s[12:15], s40 offen sc1 lds\n"
"s_add_u32 m0, s34, 20480\n"
"s_add_u32 s40, s31, 36864\n"
"buffer_load_dwordx4 v0, s[12:15], s40 offen sc1 lds\n"
"s_add_u32 m0, s34, 24576\n"
"s_add_u32 s40, s31, 49152\n"
"buffer_load_dwordx4 v0, s[12:15], s40 offen sc1 lds\n"
"s_add_u32 m0, s34, 28672\n"
"s_add_u32 s40, s31, 53248\n"
"buffer_load_dwordx4 v0, s[12:15], s40 offen sc1 lds\n"
"s_add_u32 m0, s34, 32768\n"
"s_add_u32 s40, s31, 65536\n"
"buffer_load_dwordx4 v0, s[12:15], s40 offen sc1 lds\n"
"s_add_u32 m0, s34, 36864\n"
"s_add_u32 s40, s31, 69632\n"
"buffer_load_dwordx4 v0, s[12:15], s40 offen sc1 lds\n"
"buffer_store_dword v6, off, s[12:15], s22\n"
"buffer_store_dword v6, off, s[12:15], s22\n"
"Q_top_%=:\n"
"s_mov_b32 s27, s29\n"
"s_mov_b32 s28, s30\n"
"s_mul_i32 s40, s27, 15\n"
"s_lshl1_add_u32 s40, s28, s40\n"
"s_and_b32 s40, s40, 31\n"
"s_lshl_b32 s40, s40, 22\n"
"s_add_u32 s33, s40, s66\n"
"s_cmp_ge_u32 s34, 40960\n"
"s_cselect_b32 s40, 40960, 0\n"
"v_add_u32 v3, s40, v7\n"
"s_lshl_b32 s40, s40, 1\n"
"s_add_u32 s34, s34, 40960\n"
"s_sub_u32 s34, s34, s40\n"
"s_sub_u32 s40, s24, s25\n"
"s_cmp_ge_u32 s40, 9\n"
"s_cselect_b32 s29, 1, 0\n"
"s_cmp_ge_u32 s24, 64\n"
"s_cselect_b32 s29, 1, s29\n"
"s_cmp_eq_u32 s29, 0\n"
"s_cselect_b32 s30, s24, s25\n"
"s_cselect_b32 s40, 1, 0\n"
"s_add_u32 s24, s24, s40\n"
"s_add_u32 s25, s25, s29\n"
"s_cmp_lt_u32 s30, 64\n"
"s_cselect_b32 s30, s30, -1\n"
"s_mul_i32 s38, s29, s69\n"
"s_mul_i32 s40, s29, 15\n"
"s_lshl1_add_u32 s40, s30, s40\n"
"s_and_b32 s40, s40, 31\n"
"s_lshl_b32 s40, s40, 22\n"
"s_add_u32 s40, s40, s68\n"
"s_mul_i32 s41, s30, 81920\n"
"s_add_u32 s41, s41, 144703488\n"
"s_cmp_eq_u32 s29, 0\n"
"s_cselect_b32 s40, s41, s40\n"
"s_lshl_b32 s41, s20, 10\n"
"s_add_u32 s31, s40, s41\n"
"buffer_load_dword v2, v1, s[12:15], s38 offen sc1\n"
"s_waitcnt vmcnt(3)\n"
"s_barrier\n"
"ds_read_b128 v[32:35], v3 offset:0\n"
"ds_read_b128 v[36:39], v3 offset:256\n"
"ds_read_b128 v[40:43], v3 offset:2048\n"
"ds_read_b128 v[44:47], v3 offset:2304\n"
"ds_read_b128 v[48:51], v3 offset:4096\n"
"ds_read_b128 v[52:55], v3 offset:4352\n"
"ds_read_b128 v[56:59], v3 offset:6144\n"
"ds_read_b128 v[60:63], v3 offset:6400\n"
"s_waitcnt lgkmcnt(6)\n"
"v_mfma_f32_16x16x32_bf16 v[16:19], a[0:3], v[32:35], 0\n"
"v_mfma_f32_16x16x32_bf16 v[20:23], a[4:7], v[32:35], 0\n"
"v_mfma_f32_16x16x32_bf16 v[24:27], a[0:3], v[36:39], 0\n"
"v_mfma_f32_16x16x32_bf16 v[28:31], a[4:7], v[36:39], 0\n"
"ds_read_b128 v[32:35], v3 offset:8192\n"
"ds_read_b128 v[36:39], v3 offset:8448\n"
"s_waitcnt lgkmcnt(6)\n"
"v_mfma_f32_16x16x32_bf16 v[16:19], a[8:11], v[40:43], v[16:19]\n"
"v_mfma_f32_16x16x32_bf16 v[20:23], a[12:15], v[40:43], v[20:23]\n"
"v_mfma_f32_16x16x32_bf16 v[24:27], a[8:11], v[44:47], v[24:27]\n"
"v_mfma_f32_16x16x32_bf16 v[28:31], a[12:15], v[44:47], v[28:31]\n"
"ds_read_b128 v[40:43], v3 offset:10240\n"
"ds_read_b128 v[44:47], v3 offset:10496\n"
"s_waitcnt lgkmcnt(6)\n"
"v_mfma_f32_16x16x32_bf16 v[16:19], a[16:19], v[48:51], v[16:19]\n"
"v_mfma_f32_16x16x32_bf16 v[20:23], a[20:23], v[48:51], v[20:23]\n"
"v_mfma_f32_16x16x32_bf16 v[24:27], a[16:19], v[52:55], v[24:27]\n"
"v_mfma_f32_16x16x32_bf16 v[28:31], a[20:23], v[52:55], v[28:31]\n"
"ds_read_b128 v[48:51], v3 offset:12288\n"
"ds_read_b128 v[52:55], v3 offset:12544\n"
"s_waitcnt lgkmcnt(6)\n"
"v_mfma_f32_16x16x32_bf16 v[16:19], a[24:27], v[56:59], v[16:19]\n"
"v_mfma_f32_16x16x32_bf16 v[20:23], a[28:31], v[56:59], v[20:23]\n"
"v_mfma_f32_16x16x32_bf16 v[24:27], a[24:27], v[60:63], v[24:27]\n"
"v_mfma_f32_16x16x32_bf16 v[28:31], a[28:31], v[60:63], v[28:31]\n"
"ds_read_b128 v[56:59], v3 offset:14336\n"
"ds_read_b128 v[60:63], v3 offset:14592\n"
"s_waitcnt lgkmcnt(6)\n"
"v_mfma_f32_16x16x32_bf16 v[16:19], a[32:35], v[32:35], v[16:19]\n"
"v_mfma_f32_16x16x32_bf16 v[20:23], a[36:39], v[32:35], v[20:23]\n"
"v_mfma_f32_16x16x32_bf16 v[24:27], a[32:35], v[36:39], v[24:27]\n"
"v_mfma_f32_16x16x32_bf16 v[28:31], a[36:39], v[36:39], v[28:31]\n"
"ds_read_b128 v[32:35], v3 offset:16384\n"
"ds_read_b128 v[36:39], v3 offset:16640\n"
"s_waitcnt lgkmcnt(6)\n"
"v_mfma_f32_16x16x32_bf16 v[16:19], a[40:43], v[40:43], v[16:19]\n"
"v_mfma_f32_16x16x32_bf16 v[20:23], a[44:47], v[40:43], v[20:23]\n"
"v_mfma_f32_16x16x32_bf16 v[24:27], a[40:43], v[44:47], v[24:27]\n"
"v_mfma_f32_16x16x32_bf16 v[28:31], a[44:47], v[44:47], v[28:31]\n"
"ds_read_b128 v[40:43], v3 offset:18432\n"
"ds_read_b128 v[44:47], v3 offset:18688\n"
"s_waitcnt lgkmcnt(6)\n"
"v_mfma_f32_16x16x32_bf16 v[16:19], a[48:51], v[48:51], v[16:19]\n"
"v_mfma_f32_16x16x32_bf16 v[20:23], a[52:55], v[48:51], v[20:23]\n"
"v_mfma_f32_16x16x32_bf16 v[24:27], a[48:51], v[52:55], v[24:27]\n"
"v_mfma_f32_16x16x32_bf16 v[28:31], a[52:55], v[52:55], v[28:31]\n"
"ds_read_b128 v[48:51], v3 offset:20480\n"
"ds_read_b128 v[52:55], v3 offset:20736\n"
"s_waitcnt lgkmcnt(6)\n"
"v_mfma_f32_16x16x32_bf16 v[16:19], a[56:59], v[56:59], v[16:19]\n"
"v_mfma_f32_16x16x32_bf16 v[20:23], a[60:63], v[56:59], v[20:23]\n"
"v_mfma_f32_16x16x32_bf16 v[24:27], a[56:59], v[60:63], v[24:27]\n"
"v_mfma_f32_16x16x32_bf16 v[28:31], a[60:63], v[60:63], v[28:31]\n"
"ds_read_b128 v[56:59], v3 offset:22528\n"
"ds_read_b128 v[60:63], v3 offset:22784\n"
"s_waitcnt lgkmcnt(6)\n"
"v_mfma_f32_16x16x32_bf16 v[16:19], a[64:67], v[32:35], v[16:19]\n"
"v_mfma_f32_16x16x32_bf16 v[20:23], a[68:71], v[32:35], v[20:23]\n"
"v_mfma_f32_16x16x32_bf16 v[24:27], a[64:67], v[36:39], v[24:27]\n"
"v_mfma_f32_16x16x32_bf16 v[28:31], a[68:71], v[36:39], v[28:31]\n"
"ds_read_b128 v[32:35], v3 offset:24576\n"
"ds_read_b128 v[36:39], v3 offset:24832\n"
"s_waitcnt lgkmcnt(6)\n"
"v_mfma_f32_16x16x32_bf16 v[16:19], a[72:75], v[40:43], v[16:19]\n"
"v_mfma_f32_16x16x32_bf16 v[20:23], a[76:79], v[40:43], v[20:23]\n"
"v_mfma_f32_16x16x32_bf16 v[24:27], a[72:75], v[44:47], v[24:27]\n"
"v_mfma_f32_16x16x32_bf16 v[28:31], a[76:79], v[44:47], v[28:31]\n"
"s_waitcnt vmcnt(0)\n"
"v_mov_b32 v6, s36\n"
"buffer_store_dword v6, off, s[12:15], s35 sc1\n"
"v_cmp_ge_u32 vcc, s30, v2\n"
"s_nop 0\n"
"s_and_b32 s40, vcc_lo, 65535\n"
"s_cselect_b32 s26, 0, 1\n"
"s_cselect_b32 s39, s22, s31\n"
"ds_read_b128 v[40:43], v3 offset:26624\n"
"ds_read_b128 v[44:47], v3 offset:26880\n"
"s_waitcnt lgkmcnt(6)\n"
"v_mfma_f32_16x16x32_bf16 v[16:19], a[80:83], v[48:51], v[16:19]\n"
"s_add_u32 m0, s34, 0\n"
"v_mfma_f32_16x16x32_bf16 v[20:23], a[84:87], v[48:51], v[20:23]\n"
"s_add_u32 s40, s39, 0\n"
"v_mfma_f32_16x16x32_bf16 v[24:27], a[80:83], v[52:55], v[24:27]\n"
"buffer_load_dwordx4 v0, s[12:15], s40 offen sc1 lds\n"
"v_mfma_f32_16x16x32_bf16 v[28:31], a[84:87], v[52:55], v[28:31]\n"
"ds_read_b128 v[48:51], v3 offset:28672\n"
"ds_read_b128 v[52:55], v3 offset:28928\n"
"s_waitcnt lgkmcnt(6)\n"
"v_mfma_f32_16x16x32_bf16 v[16:19], a[88:91], v[56:59], v[16:19]\n"
"s_add_u32 m0, s34, 4096\n"
"v_mfma_f32_16x16x32_bf16 v[20:23], a[92:95], v[56:59], v[20:23]\n"
"s_add_u32 s40, s39, 4096\n"
"v_mfma_f32_16x16x32_bf16 v[24:27], a[88:91], v[60:63], v[24:27]\n"
"buffer_load_dwordx4 v0, s[12:15], s40 offen sc1 lds\n"
"v_mfma_f32_16x16x32_bf16 v[28:31], a[92:95], v[60:63], v[28:31]\n"
"ds_read_b128 v[56:59], v3 offset:30720\n"
"ds_read_b128 v[60:63], v3 offset:30976\n"
"s_waitcnt lgkmcnt(6)\n"
"v_mfma_f32_16x16x32_bf16 v[16:19], a[96:99], v[32:35], v[16:19]\n"
"s_add_u32 m0, s34, 8192\n"
"v_mfma_f32_16x16x32_bf16 v[20:23], a[100:103], v[32:35], v[20:23]\n"
"s_add_u32 s40, s39, 16384\n"
"v_mfma_f32_16x16x32_bf16 v[24:27], a[96:99], v[36:39], v[24:27]\n"
"buffer_load_dwordx4 v0, s[12:15], s40 offen sc1 lds\n"
"v_mfma_f32_16x16x32_bf16 v[28:31], a[100:103], v[36:39], v[28:31]\n"
"ds_read_b128 v[32:35], v3 offset:32768\n"
"ds_read_b128 v[36:39], v3 offset:33024\n"
"s_waitcnt lgkmcnt(6)\n"
"v_mfma_f32_16x16x32_bf16 v[16:19], a[104:107], v[40:43], v[16:19]\n"
"s_add_u32 m0, s34, 12288\n"
"v_mfma_f32_16x16x32_bf16 v[20:23], a[108:111], v[40:43], v[20:23]\n"
"s_add_u32 s40, s39, 20480\n"
"v_mfma_f32_16x16x32_bf16 v[24:27], a[104:107], v[44:47], v[24:27]\n"
"buffer_load_dwordx4 v0, s[12:15], s40 offen sc1 lds\n"
"v_mfma_f32_16x16x32_bf16 v[28:31], a[108:111], v[44:47], v[28:31]\n"
"ds_read_b128 v[40:43], v3 offset:34816\n"
"ds_read_b128 v[44:47], v3 offset:35072\n"
"s_waitcnt lgkmcnt(6)\n"
"v_mfma_f32_16x16x32_bf16 v[16:19], a[112:115], v[48:51], v[16:19]\n"
"s_add_u32 m0, s34, 16384\n"
"v_mfma_f32_16x16x32_bf16 v[20:23], a[116:119], v[48:51], v[20:23]\n"
"s_add_u32 s40, s39, 32768\n"
"v_mfma_f32_16x16x32_bf16 v[24:27], a[112:115], v[52:55], v[24:27]\n"
"buffer_load_dwordx4 v0, s[12:15], s40 offen sc1 lds\n"
"v_mfma_f32_16x16x32_bf16 v[28:31], a[116:119], v[52:55], v[28:31]\n"
"ds_read_b128 v[48:51], v3 offset:36864\n"
"ds_read_b128 v[52:55], v3 offset:37120\n"
"s_waitcnt lgkmcnt(6)\n"
"v_mfma_f32_16x16x32_bf16 v[16:19], a[120:123], v[56:59], v[16:19]\n"
"s_add_u32 m0, s34, 20480\n"
"v_mfma_f32_16x16x32_bf16 v[20:23], a[124:127], v[56:59], v[20:23]\n"
"s_add_u32 s40, s39, 36864\n"
"v_mfma_f32_16x16x32_bf16 v[24:27], a[120:123], v[60:63], v[24:27]\n"
"buffer_load_dwordx4 v0, s[12:15], s40 offen sc1 lds\n"
"v_mfma_f32_16x16x32_bf16 v[28:31], a[124:127], v[60:63], v[28:31]\n"
"ds_read_b128 v[56:59], v3 offset:38912\n"
"ds_read_b128 v[60:63], v3 offset:39168\n"
"s_waitcnt lgkmcnt(6)\n"
"v_mfma_f32_16x16x32_bf16 v[16:19], a[128:131], v[32:35], v[16:19]\n"
"s_add_u32 m0, s34, 24576\n"
"v_mfma_f32_16x16x32_bf16 v[20:23], a[132:135], v[32:35], v[20:23]\n"
"s_add_u32 s40, s39, 49152\n"
"v_mfma_f32_16x16x32_bf16 v[24:27], a[128:131], v[36:39], v[24:27]\n"
"buffer_load_dwordx4 v0, s[12:15], s40 offen sc1 lds\n"
"v_mfma_f32_16x16x32_bf16 v[28:31], a[132:135], v[36:39], v[28:31]\n"
"s_waitcnt lgkmcnt(4)\n"
"v_mfma_f32_16x16x32_bf16 v[16:19], a[136:139], v[40:43], v[16:19]\n"
"s_add_u32 m0, s34, 28672\n"
"v_mfma_f32_16x16x32_bf16 v[20:23], a[140:143], v[40:43], v[20:23]\n"
"s_add_u32 s40, s39, 53248\n"
"v_mfma_f32_16x16x32_bf16 v[24:27], a[136:139], v[44:47], v[24:27]\n"
"buffer_load_dwordx4 v0, s[12:15], s40 offen sc1 lds\n"
"v_mfma_f32_16x16x32_bf16 v[28:31], a[140:143], v[44:47], v[28:31]\n"
"s_waitcnt lgkmcnt(2)\n"
"v_mfma_f32_16x16x32_bf16 v[16:19], a[144:147], v[48:51], v[16:19]\n"
"s_add_u32 m0, s34, 32768\n"
"v_mfma_f32_16x16x32_bf16 v[20:23], a[148:151], v[48:51], v[20:23]\n"
"s_add_u32 s40, s39, 65536\n"
"v_mfma_f32_16x16x32_bf16 v[24:27], a[144:147], v[52:55], v[24:27]\n"
"buffer_load_dwordx4 v0, s[12:15], s40 offen sc1 lds\n"
"v_mfma_f32_16x16x32_bf16 v[28:31], a[148:151], v[52:55], v[28:31]\n"
"s_waitcnt lgkmcnt(0)\n"
"v_mfma_f32_16x16x32_bf16 v[16:19], a[152:155], v[56:59], v[16:19]\n"
"s_add_u32 m0, s34, 36864\n"
"v_mfma_f32_16x16x32_bf16 v[20:23], a[156:159], v[56:59], v[20:23]\n"
"s_add_u32 s40, s39, 69632\n"
"v_mfma_f32_16x16x32_bf16 v[24:27], a[152:155], v[60:63], v[24:27]\n"
"buffer_load_dwordx4 v0, s[12:15], s40 offen sc1 lds\n"
"v_mfma_f32_16x16x32_bf16 v[28:31], a[156:159], v[60:63], v[28:31]\n"
"s_nop 1\n"
"v_cvt_pk_bf16_f32 v64, v16, v17\n"
"v_cvt_pk_bf16_f32 v65, v18, v19\n"
"v_cvt_pk_bf16_f32 v66, v20, v21\n"
"v_cvt_pk_bf16_f32 v67, v22, v23\n"
"buffer_store_dwordx4 v[64:67], v0, s[12:15], s33 offen offset:0 sc1\n"
"v_cvt_pk_bf16_f32 v68, v24, v25\n"
"v_cvt_pk_bf16_f32 v69, v26, v27\n"
"v_cvt_pk_bf16_f32 v70, v28, v29\n"
"v_cvt_pk_bf16_f32 v71, v30, v31\n"
"buffer_store_dwordx4 v[68:71], v0, s[12:15], s33 offen offset:1024 sc1\n"
"s_lshl_b32 s40, s27, 4\n"
"s_add_u32 s35, s37, s40\n"
"s_add_u32 s36, s28, 1\n"
"s_cmp_lg_u32 s26, 0\n"
"s_cbranch_scc1 Q_top_%=\n"
"s_branch Q_cold_%=\n"
"Q_end_%=:\n"
"s_waitcnt vmcnt(0)\n"
"s_endpgm\n"
"QV_entry_%=:\n"
"s_mov_b32 s24, 0\n"
"s_mov_b32 s25, 0\n"
"s_mov_b32 s35, s22\n"
"s_mov_b32 s36, 0\n"
"v_cmp_gt_u32 vcc, 16, v10\n"
"v_mov_b32 v13, s22\n"
"s_nop 0\n"
"v_cndmask_b32 v1, v13, v1, vcc\n"
"s_lshl_b32 s34, s20, 10\n"
"s_sub_u32 s40, s24, s25\n"
"s_cmp_ge_u32 s40, 9\n"
"s_cselect_b32 s29, 1, 0\n"
"s_cmp_ge_u32 s24, 64\n"
"s_cselect_b32 s29, 1, s29\n"
"s_cmp_eq_u32 s29, 0\n"
"s_cselect_b32 s30, s24, s25\n"
"s_cselect_b32 s40, 1, 0\n"
"s_add_u32 s24, s24, s40\n"
"s_add_u32 s25, s25, s29\n"
"s_cmp_lt_u32 s30, 64\n"
"s_cselect_b32 s30, s30, -1\n"
"s_mul_i32 s38, s29, s69\n"
"s_mul_i32 s40, s29, 15\n"
"s_lshl1_add_u32 s40, s30, s40\n"
"s_and_b32 s40, s40, 31\n"
"s_lshl_b32 s40, s40, 22\n"
"s_add_u32 s40, s40, s68\n"
"s_mul_i32 s41, s30, 81920\n"
"s_add_u32 s41, s41, 144703488\n"
"s_cmp_eq_u32 s29, 0\n"
"s_cselect_b32 s40, s41, s40\n"
"s_lshl_b32 s41, s20, 10\n"
"s_add_u32 s31, s40, s41\n"
"QV_cold_%=:\n"
"s_waitcnt vmcnt(0)\n"
"v_mov_b32 v6, s36\n"
"buffer_store_dword v6, off, s[12:15], s35 sc1\n"
"s_mov_b32 s35, s22\n"
"s_cmp_lt_i32 s30, 0\n"
"s_cbranch_scc1 QV_end_%=\n"
"QV_spin_%=:\n"
"buffer_load_dword v2, v1, s[12:15], s38 offen sc1\n"
"s_waitcnt vmcnt(0)\n"
"v_cmp_ge_u32 vcc, s30, v2\n"
"s_nop 0\n"
"s_and_b32 s40, vcc_lo, 65535\n"
"s_cbranch_scc0 QV_go_%=\n"
"s_sleep 4\n"
"s_add_u32 s23, s23, 1\n"
"s_cmp_lt_u32 s23, 400000\n"
"s_cbranch_scc1 QV_spin_%=\n"
"QV_go_%=:\n"
"s_add_u32 m0, s34, 0\n"
"s_add_u32 s40, s31, 0\n"
"buffer_load_dwordx4 v0, s[12:15], s40 offen sc1 lds\n"
"s_add_u32 m0, s34, 4096\n"
"s_add_u32 s40, s31, 4096\n"
"buffer_load_dwordx4 v0, s[12:15], s40 offen sc1 lds\n"
"s_add_u32 m0, s34, 8192\n"
"s_add_u32 s40, s31, 16384\n"
"buffer_load_dwordx4 v0, s[12:15], s40 offen sc1 lds\n"
"s_add_u32 m0, s34, 12288\n"
"s_add_u32 s40, s31, 20480\n"
"buffer_load_dwordx4 v0, s[12:15], s40 offen sc1 lds\n"
"s_add_u32 m0, s34, 16384\n"
"s_add_u32 s40, s31, 32768\n"
"buffer_load_dwordx4 v0, s[12:15], s40 offen sc1 lds\n"
"s_add_u32 m0, s34, 20480\n"
"s_add_u32 s40, s31, 36864\n"
"buffer_load_dwordx4 v0, s[12:15], s40 offen sc1 lds\n"
"s_add_u32 m0, s34, 24576\n"
"s_add_u32 s40, s31, 49152\n"
"buffer_load_dwordx4 v0, s[12:15], s40 offen sc1 lds\n"
"s_add_u32 m0, s34, 28672\n"
"s_add_u32 s40, s31, 53248\n"
"buffer_load_dwordx4 v0, s[12:15], s40 offen sc1 lds\n"
"s_add_u32 m0, s34, 32768\n"
"s_add_u32 s40, s31, 65536\n"
"buffer_load_dwordx4 v0, s[12:15], s40 offen sc1 lds\n"
"s_add_u32 m0, s34, 36864\n"
"s_add_u32 s40, s31, 69632\n"
"buffer_load_dwordx4 v0, s[12:15], s40 offen sc1 lds\n"
"buffer_store_dword v6, off, s[12:15], s22\n"
"buffer_store_dword v6, off, s[12:15], s22\n"
"QV_top_%=:\n"
"s_mov_b32 s27, s29\n"
"s_mov_b32 s28, s30\n"
"s_mul_i32 s40, s27, 15\n"
"s_lshl1_add_u32 s40, s28, s40\n"
"s_and_b32 s40, s40, 31\n"
"s_lshl_b32 s40, s40, 22\n"
"s_add_u32 s33, s40, s66\n"
"s_cmp_ge_u32 s34, 40960\n"
"s_cselect_b32 s40, 40960, 0\n"
"v_add_u32 v3, s40, v7\n"
"s_lshl_b32 s40, s40, 1\n"
"s_add_u32 s34, s34, 40960\n"
"s_sub_u32 s34, s34, s40\n"
"s_sub_u32 s40, s24, s25\n"
"s_cmp_ge_u32 s40, 9\n"
"s_cselect_b32 s29, 1, 0\n"
"s_cmp_ge_u32 s24, 64\n"
"s_cselect_b32 s29, 1, s29\n"
"s_cmp_eq_u32 s29, 0\n"
"s_cselect_b32 s30, s24, s25\n"
"s_cselect_b32 s40, 1, 0\n"
"s_add_u32 s24, s24, s40\n"
"s_add_u32 s25, s25, s29\n"
"s_cmp_lt_u32 s30, 64\n"
"s_cselect_b32 s30, s30, -1\n"
"s_mul_i32 s38, s29, s69\n"
"s_mul_i32 s40, s29, 15\n"
"s_lshl1_add_u32 s40, s30, s40\n"
"s_and_b32 s40, s40, 31\n"
"s_lshl_b32 s40, s40, 22\n"
"s_add_u32 s40, s40, s68\n"
"s_mul_i32 s41, s30, 81920\n"
"s_add_u32 s41, s41, 144703488\n"
"s_cmp_eq_u32 s29, 0\n"
"s_cselect_b32 s40, s41, s40\n"
"s_lshl_b32 s41, s20, 10\n"
"s_add_u32 s31, s40, s41\n"
"buffer_load_dword v2, v1, s[12:15], s38 offen sc1\n"
"s_waitcnt vmcnt(3)\n"
"s_barrier\n"
"ds_read_b128 v[32:35], v3 offset:0\n"
"ds_read_b128 v[36:39], v3 offset:256\n"
"ds_read_b128 v[40:43], v3 offset:2048\n"
"ds_read_b128 v[44:47], v3 offset:2304\n"
"ds_read_b128 v[48:51], v3 offset:4096\n"
"ds_read_b128 v[52:55], v3 offset:4352\n"
"ds_read_b128 v[56:59], v3 offset:6144\n"
"ds_read_b128 v[60:63], v3 offset:6400\n"
"s_waitcnt lgkmcnt(6)\n"
"v_mfma_f32_16x16x32_bf16 v[16:19], v[32:35], a[0:3], 0\n"
"v_mfma_f32_16x16x32_bf16 v[20:23], v[32:35], a[4:7], 0\n"
"v_mfma_f32_16x16x32_bf16 v[24:27], v[36:39], a[0:3], 0\n"
"v_mfma_f32_16x16x32_bf16 v[28:31], v[36:39], a[4:7], 0\n"
"ds_read_b128 v[32:35], v3 offset:8192\n"
"ds_read_b128 v[36:39], v3 offset:8448\n"
"s_waitcnt lgkmcnt(6)\n"
"v_mfma_f32_16x16x32_bf16 v[16:19], v[40:43], a[8:11], v[16:19]\n"
"v_mfma_f32_16x16x32_bf16 v[20:23], v[40:43], a[12:15], v[20:23]\n"
"v_mfma_f32_16x16x32_bf16 v[24:27], v[44:47], a[8:11], v[24:27]\n"
"v_mfma_f32_16x16x32_bf16 v[28:31], v[44:47], a[12:15], v[28:31]\n"
"ds_read_b128 v[40:43], v3 offset:10240\n"
"ds_read_b128 v[44:47], v3 offset:10496\n"
"s_waitcnt lgkmcnt(6)\n"
"v_mfma_f32_16x16x32_bf16 v[16:19], v[48:51], a[16:19], v[16:19]\n"
"v_mfma_f32_16x16x32_bf16 v[20:23], v[48:51], a[20:23], v[20:23]\n"
"v_mfma_f32_16x16x32_bf16 v[24:27], v[52:55], a[16:19], v[24:27]\n"
"v_mfma_f32_16x16x32_bf16 v[28:31], v[52:55], a[20:23], v[28:31]\n"
"ds_read_b128 v[48:51], v3 offset:12288\n"
"ds_read_b128 v[52:55], v3 offset:12544\n"
"s_waitcnt lgkmcnt(6)\n"
"v_mfma_f32_16x16x32_bf16 v[16:19], v[56:59], a[24:27], v[16:19]\n"
"v_mfma_f32_16x16x32_bf16 v[20:23], v[56:59], a[28:31], v[20:23]\n"
"v_mfma_f32_16x16x32_bf16 v[24:27], v[60:63], a[24:27], v[24:27]\n"
"v_mfma_f32_16x16x32_bf16 v[28:31], v[60:63], a[28:31], v[28:31]\n"
"ds_read_b128 v[56:59], v3 offset:14336\n"
"ds_read_b128 v[60:63], v3 offset:14592\n"
"s_waitcnt lgkmcnt(6)\n"
"v_mfma_f32_16x16x32_bf16 v[16:19], v[32:35], a[32:35], v[16:19]\n"
"v_mfma_f32_16x16x32_bf16 v[20:23], v[32:35], a[36:39], v[20:23]\n"
"v_mfma_f32_16x16x32_bf16 v[24:27], v[36:39], a[32:35], v[24:27]\n"
"v_mfma_f32_16x16x32_bf16 v[28:31], v[36:39], a[36:39], v[28:31]\n"
"ds_read_b128 v[32:35], v3 offset:16384\n"
"ds_read_b128 v[36:39], v3 offset:16640\n"
"s_waitcnt lgkmcnt(6)\n"
"v_mfma_f32_16x16x32_bf16 v[16:19], v[40:43], a[40:43], v[16:19]\n"
"v_mfma_f32_16x16x32_bf16 v[20:23], v[40:43], a[44:47], v[20:23]\n"
"v_mfma_f32_16x16x32_bf16 v[24:27], v[44:47], a[40:43], v[24:27]\n"
"v_mfma_f32_16x16x32_bf16 v[28:31], v[44:47], a[44:47], v[28:31]\n"
"ds_read_b128 v[40:43], v3 offset:18432\n"
"ds_read_b128 v[44:47], v3 offset:18688\n"
"s_waitcnt lgkmcnt(6)\n"
"v_mfma_f32_16x16x32_bf16 v[16:19], v[48:51], a[48:51], v[16:19]\n"
"v_mfma_f32_16x16x32_bf16 v[20:23], v[48:51], a[52:55], v[20:23]\n"
"v_mfma_f32_16x16x32_bf16 v[24:27], v[52:55], a[48:51], v[24:27]\n"
"v_mfma_f32_16x16x32_bf16 v[28:31], v[52:55], a[52:55], v[28:31]\n"
"ds_read_b128 v[48:51], v3 offset:20480\n"
"ds_read_b128 v[52:55], v3 offset:20736\n"
"s_waitcnt lgkmcnt(6)\n"
"v_mfma_f32_16x16x32_bf16 v[16:19], v[56:59], a[56:59], v[16:19]\n"
"v_mfma_f32_16x16x32_bf16 v[20:23], v[56:59], a[60:63], v[20:23]\n"
"v_mfma_f32_16x16x32_bf16 v[24:27], v[60:63], a[56:59], v[24:27]\n"
"v_mfma_f32_16x16x32_bf16 v[28:31], v[60:63], a[60:63], v[28:31]\n"
"ds_read_b128 v[56:59], v3 offset:22528\n"
"ds_read_b128 v[60:63], v3 offset:22784\n"
"s_waitcnt lgkmcnt(6)\n"
"v_mfma_f32_16x16x32_bf16 v[16:19], v[32:35], a[64:67], v[16:19]\n"
"v_mfma_f32_16x16x32_bf16 v[20:23], v[32:35], a[68:71], v[20:23]\n"
"v_mfma_f32_16x16x32_bf16 v[24:27], v[36:39], a[64:67], v[24:27]\n"
"v_mfma_f32_16x16x32_bf16 v[28:31], v[36:39], a[68:71], v[28:31]\n"
"ds_read_b128 v[32:35], v3 offset:24576\n"
"ds_read_b128 v[36:39], v3 offset:24832\n"
"s_waitcnt lgkmcnt(6)\n"
"v_mfma_f32_16x16x32_bf16 v[16:19], v[40:43], a[72:75], v[16:19]\n"
"v_mfma_f32_16x16x32_bf16 v[20:23], v[40:43], a[76:79], v[20:23]\n"
"v_mfma_f32_16x16x32_bf16 v[24:27], v[44:47], a[72:75], v[24:27]\n"
"v_mfma_f32_16x16x32_bf16 v[28:31], v[44:47], a[76:79], v[28:31]\n"
"s_waitcnt vmcnt(0)\n"
"v_mov_b32 v6, s36\n"
"buffer_store_dword v6, off, s[12:15], s35 sc1\n"
"v_cmp_ge_u32 vcc, s30, v2\n"
"s_nop 0\n"
"s_and_b32 s40, vcc_lo, 65535\n"
"s_cselect_b32 s26, 0, 1\n"
"s_cselect_b32 s39, s22, s31\n"
"ds_read_b128 v[40:43], v3 offset:26624\n"
"ds_read_b128 v[44:47], v3 offset:26880\n"
"s_waitcnt lgkmcnt(6)\n"
"v_mfma_f32_16x16x32_bf16 v[16:19], v[48:51], a[80:83], v[16:19]\n"
"s_add_u32 m0, s34, 0\n"
"v_mfma_f32_16x16x32_bf16 v[20:23], v[48:51], a[84:87], v[20:23]\n"
"s_add_u32 s40, s39, 0\n"
"v_mfma_f32_16x16x32_bf16 v[24:27], v[52:55], a[80:83], v[24:27]\n"
"buffer_load_dwordx4 v0, s[12:15], s40 offen sc1 lds\n"
"v_mfma_f32_16x16x32_bf16 v[28:31], v[52:55], a[84:87], v[28:31]\n"
"ds_read_b128 v[48:51], v3 offset:28672\n"
"ds_read_b128 v[52:55], v3 offset:28928\n"
"s_waitcnt lgkmcnt(6)\n"
"v_mfma_f32_16x16x32_bf16 v[16:19], v[56:59], a[88:91], v[16:19]\n"
"s_add_u32 m0, s34, 4096\n"
"v_mfma_f32_16x16x32_bf16 v[20:23], v[56:59], a[92:95], v[20:23]\n"
"s_add_u32 s40, s39, 4096\n"
"v_mfma_f32_16x16x32_bf16 v[24:27], v[60:63], a[88:91], v[24:27]\n"
"buffer_load_dwordx4 v0, s[12:15], s40 offen sc1 lds\n"
"v_mfma_f32_16x16x32_bf16 v[28:31], v[60:63], a[92:95], v[28:31]\n"
"ds_read_b128 v[56:59], v3 offset:30720\n"
"ds_read_b128 v[60:63], v3 offset:30976\n"
"s_waitcnt lgkmcnt(6)\n"
"v_mfma_f32_16x16x32_bf16 v[16:19], v[32:35], a[96:99], v[16:19]\n"
"s_add_u32 m0, s34, 8192\n"
"v_mfma_f32_16x16x32_bf16 v[20:23], v[32:35], a[100:103], v[20:23]\n"
"s_add_u32 s40, s39, 16384\n"
"v_mfma_f32_16x16x32_bf16 v[24:27], v[36:39], a[96:99], v[24:27]\n"
"buffer_load_dwordx4 v0, s[12:15], s40 offen sc1 lds\n"
"v_mfma_f32_16x16x32_bf16 v[28:31], v[36:39], a[100:103], v[28:31]\n"
"ds_read_b128 v[32:35], v3 offset:32768\n"
"ds_read_b128 v[36:39], v3 offset:33024\n"
"s_waitcnt lgkmcnt(6)\n"
"v_mfma_f32_16x16x32_bf16 v[16:19], v[40:43], a[104:107], v[16:19]\n"
"s_add_u32 m0, s34, 12288\n"
"v_mfma_f32_16x16x32_bf16 v[20:23], v[40:43], a[108:111], v[20:23]\n"
"s_add_u32 s40, s39, 20480\n"
"v_mfma_f32_16x16x32_bf16 v[24:27], v[44:47], a[104:107], v[24:27]\n"
"buffer_load_dwordx4 v0, s[12:15], s40 offen sc1 lds\n"
"v_mfma_f32_16x16x32_bf16 v[28:31], v[44:47], a[108:111], v[28:31]\n"
"ds_read_b128 v[40:43], v3 offset:34816\n"
"ds_read_b128 v[44:47], v3 offset:35072\n"
"s_waitcnt lgkmcnt(6)\n"
"v_mfma_f32_16x16x32_bf16 v[16:19], v[48:51], a[112:115], v[16:19]\n"
"s_add_u32 m0, s34, 16384\n"
"v_mfma_f32_16x16x32_bf16 v[20:23], v[48:51], a[116:119], v[20:23]\n"
"s_add_u32 s40, s39, 32768\n"
"v_mfma_f32_16x16x32_bf16 v[24:27], v[52:55], a[112:115], v[24:27]\n"
"buffer_load_dwordx4 v0, s[12:15], s40 offen sc1 lds\n"
"v_mfma_f32_16x16x32_bf16 v[28:31], v[52:55], a[116:119], v[28:31]\n"
"ds_read_b128 v[48:51], v3 offset:36864\n"
"ds_read_b128 v[52:55], v3 offset:37120\n"
"s_waitcnt lgkmcnt(6)\n"
"v_mfma_f32_16x16x32_bf16 v[16:19], v[56:59], a[120:123], v[16:19]\n"
"s_add_u32 m0, s34, 20480\n"
"v_mfma_f32_16x16x32_bf16 v[20:23], v[56:59], a[124:127], v[20:23]\n"
"s_add_u32 s40, s39, 36864\n"
"v_mfma_f32_16x16x32_bf16 v[24:27], v[60:63], a[120:123], v[24:27]\n"
"buffer_load_dwordx4 v0, s[12:15], s40 offen sc1 lds\n"
"v_mfma_f32_16x16x32_bf16 v[28:31], v[60:63], a[124:127], v[28:31]\n"
"ds_read_b128 v[56:59], v3 offset:38912\n"
"ds_read_b128 v[60:63], v3 offset:39168\n"
"s_waitcnt lgkmcnt(6)\n"
"v_mfma_f32_16x16x32_bf16 v[16:19], v[32:35], a[128:131], v[16:19]\n"
"s_add_u32 m0, s34, 24576\n"
"v_mfma_f32_16x16x32_bf16 v[20:23], v[32:35], a[132:135], v[20:23]\n"
"s_add_u32 s40, s39, 49152\n"
"v_mfma_f32_16x16x32_bf16 v[24:27], v[36:39], a[128:131], v[24:27]\n"
"buffer_load_dwordx4 v0, s[12:15], s40 offen sc1 lds\n"
"v_mfma_f32_16x16x32_bf16 v[28:31], v[36:39], a[132:135], v[28:31]\n"
"s_waitcnt lgkmcnt(4)\n"
"v_mfma_f32_16x16x32_bf16 v[16:19], v[40:43], a[136:139], v[16:19]\n"
"s_add_u32 m0, s34, 28672\n"
"v_mfma_f32_16x16x32_bf16 v[20:23], v[40:43], a[140:143], v[20:23]\n"
"s_add_u32 s40, s39, 53248\n"
"v_mfma_f32_16x16x32_bf16 v[24:27], v[44:47], a[136:139], v[24:27]\n"
"buffer_load_dwordx4 v0, s[12:15], s40 offen sc1 lds\n"
"v_mfma_f32_16x16x32_bf16 v[28:31], v[44:47], a[140:143], v[28:31]\n"
"s_waitcnt lgkmcnt(2)\n"
"v_mfma_f32_16x16x32_bf16 v[16:19], v[48:51], a[144:147], v[16:19]\n"
"s_add_u32 m0, s34, 32768\n"
"v_mfma_f32_16x16x32_bf16 v[20:23], v[48:51], a[148:151], v[20:23]\n"
"s_add_u32 s40, s39, 65536\n"
"v_mfma_f32_16x16x32_bf16 v[24:27], v[52:55], a[144:147], v[24:27]\n"
"buffer_load_dwordx4 v0, s[12:15], s40 offen sc1 lds\n"
"v_mfma_f32_16x16x32_bf16 v[28:31], v[52:55], a[148:151], v[28:31]\n"
"s_waitcnt lgkmcnt(0)\n"
"v_mfma_f32_16x16x32_bf16 v[16:19], v[56:59], a[152:155], v[16:19]\n"
"s_add_u32 m0, s34, 36864\n"
"v_mfma_f32_16x16x32_bf16 v[20:23], v[56:59], a[156:159], v[20:23]\n"
"s_add_u32 s40, s39, 69632\n"
"v_mfma_f32_16x16x32_bf16 v[24:27], v[60:63], a[152:155], v[24:27]\n"
"buffer_load_dwordx4 v0, s[12:15], s40 offen sc1 lds\n"
"v_mfma_f32_16x16x32_bf16 v[28:31], v[60:63], a[156:159], v[28:31]\n"
"s_nop 1\n"
"v_cvt_pk_bf16_f32 v64, v16, v17\n"
"v_cvt_pk_bf16_f32 v65, v18, v19\n"
"s_nop 1\n"
"v_cvt_pk_bf16_f32 v66, v24, v25\n"
"v_cvt_pk_bf16_f32 v67, v26, v27\n"
"buffer_store_dwordx4 v[64:67], v0, s[12:15], s33 offen offset:0 sc1\n"
"v_cvt_pk_bf16_f32 v68, v20, v21\n"
"v_cvt_pk_bf16_f32 v69, v22, v23\n"
"v_cvt_pk_bf16_f32 v70, v28, v29\n"
"v_cvt_pk_bf16_f32 v71, v30, v31\n"
"buffer_store_dwordx4 v[68:71], v0, s[12:15], s33 offen offset:1024 sc1\n"
"s_lshl_b32 s40, s27, 4\n"
"s_add_u32 s35, s37, s40\n"
"s_add_u32 s36, s28, 1\n"
"s_cmp_lg_u32 s26, 0\n"
"s_cbranch_scc1 QV_top_%=\n"
"s_branch QV_cold_%=\n"
"QV_end_%=:\n"
"s_waitcnt vmcnt(0)\n"
"s_endpgm\n"
"ROLE2_%=:\n"
"s_load_dwordx8 s[52:59], %0, 16\n"
"s_load_dwordx4 s[60:63], %0, 48\n"
"s_load_dwordx2 s[12:13], %0, 72\n"
"s_mov_b32 s21, %1\n"
"s_mov_b32 s20, %2\n"
"s_mov_b32 s22, 0x40000000\n"
"s_mov_b32 s23, 0\n"
"v_mbcnt_lo_u32_b32 v10, -1, 0\n"
"v_mbcnt_hi_u32_b32 v10, -1, v10\n"
"v_lshlrev_b32 v0, 4, v10\n"
"v_and_b32 v11, 15, v10\n"
"v_lshrrev_b32 v12, 4, v10\n"
"s_waitcnt lgkmcnt(0)\n"
"s_and_b32 s13, s13, 0xffff\n"
"s_mov_b32 s14, 149946368\n"
"s_mov_b32 s15, 0x20000\n"
"s_lshr_b32 s64, s21, 2\n"
"s_and_b32 s65, s21, 3\n"
"s_sleep 100\n"
"s_mov_b32 s16, s58\n"
"s_and_b32 s17, s59, 0xffff\n"
"s_mov_b32 s18, 5242880\n"
"s_mov_b32 s19, 0x20000\n"
"v_add_u32 v13, 0, v10\n"
"v_mul_u32_u24 v14, 3277, v13\n"
"v_lshrrev_b32 v14, 16, v14\n"
"v_mul_u32_u24 v15, 20, v14\n"
"v_sub_u32 v15, v13, v15\n"
"v_mul_u32_u24 v14, 1280, v14\n"
"v_lshl_add_u32 v24, v15, 4, v14\n"
"v_add_u32 v13, 64, v10\n"
"v_mul_u32_u24 v14, 3277, v13\n"
"v_lshrrev_b32 v14, 16, v14\n"
"v_mul_u32_u24 v15, 20, v14\n"
"v_sub_u32 v15, v13, v15\n"
"v_mul_u32_u24 v14, 1280, v14\n"
"v_lshl_add_u32 v25, v15, 4, v14\n"
"v_add_u32 v13, 128, v10\n"
"v_mul_u32_u24 v14, 3277, v13\n"
"v_lshrrev_b32 v14, 16, v14\n"
"v_mul_u32_u24 v15, 20, v14\n"
"v_sub_u32 v15, v13, v15\n"
"v_mul_u32_u24 v14, 1280, v14\n"
"v_lshl_add_u32 v26, v15, 4, v14\n"
"v_add_u32 v13, 192, v10\n"
"v_mul_u32_u24 v14, 3277, v13\n"
"v_lshrrev_b32 v14, 16, v14\n"
"v_mul_u32_u24 v15, 20, v14\n"
"v_sub_u32 v15, v13, v15\n"
"v_mul_u32_u24 v14, 1280, v14\n"
"v_lshl_add_u32 v27, v15, 4, v14\n"
"v_add_u32 v13, 256, v10\n"
"v_mul_u32_u24 v14, 3277, v13\n"
"v_lshrrev_b32 v14, 16, v14\n"
"v_mul_u32_u24 v15, 20, v14\n"
"v_sub_u32 v15, v13, v15\n"
"v_mul_u32_u24 v14, 1280, v14\n"
"v_lshl_add_u32 v28, v15, 4, v14\n"
"s_lshl_b32 s40, s64, 9\n"
"s_lshl4_add_u32 s40, s20, s40\n"
"s_mul_i32 s40, s40, 1280\n"
"s_mul_i32 s41, s65, 320\n"
"s_add_u32 s40, s40, s41\n"
"s_mul_i32 s41, s20, 5120\n"
"s_add_u32 s42, s40, 0\n"
"s_add_u32 m0, s41, 0\n"
"s_nop 0\n"
"buffer_load_dwordx4 v24, s[16:19], s42 offen lds\n"
"s_add_u32 m0, s41, 1024\n"
"s_nop 0\n"
"buffer_load_dwordx4 v25, s[16:19], s42 offen lds\n"
"s_add_u32 m0, s41, 2048\n"
"s_nop 0\n"
"buffer_load_dwordx4 v26, s[16:19], s42 offen lds\n"
"s_add_u32 m0, s41, 3072\n"
"s_nop 0\n"
"buffer_load_dwordx4 v27, s[16:19], s42 offen lds\n"
"s_add_u32 m0, s41, 4096\n"
"s_nop 0\n"
"buffer_load_dwordx4 v28, s[16:19], s42 offen lds\n"
"s_add_u32 s42, s40, 81920\n"
"s_add_u32 m0, s41, 20480\n"
"s_nop 0\n"
"buffer_load_dwordx4 v24, s[16:19], s42 offen lds\n"
"s_add_u32 m0, s41, 21504\n"
"s_nop 0\n"
"buffer_load_dwordx4 v25, s[16:19], s42 offen lds\n"
"s_add_u32 m0, s41, 22528\n"
"s_nop 0\n"
"buffer_load_dwordx4 v26, s[16:19], s42 offen lds\n"
"s_add_u32 m0, s41, 23552\n"
"s_nop 0\n"
"buffer_load_dwordx4 v27, s[16:19], s42 offen lds\n"
"s_add_u32 m0, s41, 24576\n"
"s_nop 0\n"
"buffer_load_dwordx4 v28, s[16:19], s42 offen lds\n"
"s_add_u32 s42, s40, 163840\n"
"s_add_u32 m0, s41, 40960\n"
"s_nop 0\n"
"buffer_load_dwordx4 v24, s[16:19], s42 offen lds\n"
"s_add_u32 m0, s41, 41984\n"
"s_nop 0\n"
"buffer_load_dwordx4 v25, s[16:19], s42 offen lds\n"
"s_add_u32 m0, s41, 43008\n"
"s_nop 0\n"
"buffer_load_dwordx4 v26, s[16:19], s42 offen lds\n"
"s_add_u32 m0, s41, 44032\n"
"s_nop 0\n"
"buffer_load_dwordx4 v27, s[16:19], s42 offen lds\n"
"s_add_u32 m0, s41, 45056\n"
"s_nop 0\n"
"buffer_load_dwordx4 v28, s[16:19], s42 offen lds\n"
"s_add_u32 s42, s40, 245760\n"
"s_add_u32 m0, s41, 61440\n"
"s_nop 0\n"
"buffer_load_dwordx4 v24, s[16:19], s42 offen lds\n"
"s_add_u32 m0, s41, 62464\n"
"s_nop 0\n"
"buffer_load_dwordx4 v25, s[16:19], s42 offen lds\n"
"s_add_u32 m0, s41, 63488\n"
"s_nop 0\n"
"buffer_load_dwordx4 v26, s[16:19], s42 offen lds\n"
"s_add_u32 m0, s41, 64512\n"
"s_nop 0\n"
"buffer_load_dwordx4 v27, s[16:19], s42 offen lds\n"
"s_add_u32 m0, s41, 65536\n"
"s_nop 0\n"
"buffer_load_dwordx4 v28, s[16:19], s42 offen lds\n"
"s_add_u32 s42, s40, 327680\n"
"s_add_u32 m0, s41, 81920\n"
"s_nop 0\n"
"buffer_load_dwordx4 v24, s[16:19], s42 offen lds\n"
"s_add_u32 m0, s41, 82944\n"
"s_nop 0\n"
"buffer_load_dwordx4 v25, s[16:19], s42 offen lds\n"
"s_add_u32 m0, s41, 83968\n"
"s_nop 0\n"
"buffer_load_dwordx4 v26, s[16:19], s42 offen lds\n"
"s_add_u32 m0, s41, 84992\n"
"s_nop 0\n"
"buffer_load_dwordx4 v27, s[16:19], s42 offen lds\n"
"s_add_u32 m0, s41, 86016\n"
"s_nop 0\n"
"buffer_load_dwordx4 v28, s[16:19], s42 offen lds\n"
"s_add_u32 s42, s40, 409600\n"
"s_add_u32 m0, s41, 102400\n"
"s_nop 0\n"
"buffer_load_dwordx4 v24, s[16:19], s42 offen lds\n"
"s_add_u32 m0, s41, 103424\n"
"s_nop 0\n"
"buffer_load_dwordx4 v25, s[16:19], s42 offen lds\n"
"s_add_u32 m0, s41, 104448\n"
"s_nop 0\n"
"buffer_load_dwordx4 v26, s[16:19], s42 offen lds\n"
"s_add_u32 m0, s41, 105472\n"
"s_nop 0\n"
"buffer_load_dwordx4 v27, s[16:19], s42 offen lds\n"
"s_add_u32 m0, s41, 106496\n"
"s_nop 0\n"
"buffer_load_dwordx4 v28, s[16:19], s42 offen lds\n"
"s_add_u32 s42, s40, 491520\n"
"s_add_u32 m0, s41, 122880\n"
"s_nop 0\n"
"buffer_load_dwordx4 v24, s[16:19], s42 offen lds\n"
"s_add_u32 m0, s41, 123904\n"
"s_nop 0\n"
"buffer_load_dwordx4 v25, s[16:19], s42 offen lds\n"
"s_add_u32 m0, s41, 124928\n"
"s_nop 0\n"
"buffer_load_dwordx4 v26, s[16:19], s42 offen lds\n"
"s_add_u32 m0, s41, 125952\n"
"s_nop 0\n"
"buffer_load_dwordx4 v27, s[16:19], s42 offen lds\n"
"s_add_u32 m0, s41, 126976\n"
"s_nop 0\n"
"buffer_load_dwordx4 v28, s[16:19], s42 offen lds\n"
"s_add_u32 s42, s40, 573440\n"
"s_add_u32 m0, s41, 143360\n"
"s_nop 0\n"
"buffer_load_dwordx4 v24, s[16:19], s42 offen lds\n"
"s_add_u32 m0, s41, 144384\n"
"s_nop 0\n"
"buffer_load_dwordx4 v25, s[16:19], s42 offen lds\n"
"s_add_u32 m0, s41, 145408\n"
"s_nop 0\n"
"buffer_load_dwordx4 v26, s[16:19], s42 offen lds\n"
"s_add_u32 m0, s41, 146432\n"
"s_nop 0\n"
"buffer_load_dwordx4 v27, s[16:19], s42 offen lds\n"
"s_add_u32 m0, s41, 147456\n"
"s_nop 0\n"
"buffer_load_dwordx4 v28, s[16:19], s42 offen lds\n"
"s_waitcnt vmcnt(0)\n"
"s_barrier\n"
"v_and_b32 v18, 3, v11\n"
"v_lshrrev_b32 v16, 2, v11\n"
"v_lshl_add_u32 v16, v12, 2, v16\n"
"v_mul_u32_u24 v16, 320, v16\n"
"s_lshr_b32 s66, s20, 1\n"
"s_mul_i32 s40, s66, 192\n"
"v_lshl_add_u32 v17, v18, 4, v16\n"
"v_add_u32 v21, s40, v17\n"
"s_mul_i32 s40, s66, 32\n"
"s_add_u32 s40, s40, 128\n"
"v_lshl_add_u32 v17, v18, 3, v16\n"
"v_add_u32 v22, s40, v17\n"
"ds_read_b64_tr_b16 a[0:1], v21 offset:0\n"
"ds_read_b64_tr_b16 a[2:3], v21 offset:5120\n"
"ds_read_b64_tr_b16 a[4:5], v21 offset:8\n"
"ds_read_b64_tr_b16 a[6:7], v21 offset:5128\n"
"ds_read_b64_tr_b16 a[8:9], v21 offset:64\n"
"ds_read_b64_tr_b16 a[10:11], v21 offset:5184\n"
"ds_read_b64_tr_b16 a[12:13], v21 offset:72\n"
"ds_read_b64_tr_b16 a[14:15], v21 offset:5192\n"
"ds_read_b64_tr_b16 a[16:17], v22 offset:0\n"
"ds_read_b64_tr_b16 a[18:19], v22 offset:5120\n"
"s_waitcnt lgkmcnt(0)\n"
"ds_read_b64_tr_b16 a[20:21], v21 offset:10240\n"
"ds_read_b64_tr_b16 a[22:23], v21 offset:15360\n"
"ds_read_b64_tr_b16 a[24:25], v21 offset:10248\n"
"ds_read_b64_tr_b16 a[26:27], v21 offset:15368\n"
"ds_read_b64_tr_b16 a[28:29], v21 offset:10304\n"
"ds_read_b64_tr_b16 a[30:31], v21 offset:15424\n"
"ds_read_b64_tr_b16 a[32:33], v21 offset:10312\n"
"ds_read_b64_tr_b16 a[34:35], v21 offset:15432\n"
"ds_read_b64_tr_b16 a[36:37], v22 offset:10240\n"
"ds_read_b64_tr_b16 a[38:39], v22 offset:15360\n"
"s_waitcnt lgkmcnt(0)\n"
"ds_read_b64_tr_b16 a[40:41], v21 offset:20480\n"
"ds_read_b64_tr_b16 a[42:43], v21 offset:25600\n"
"ds_read_b64_tr_b16 a[44:45], v21 offset:20488\n"
"ds_read_b64_tr_b16 a[46:47], v21 offset:25608\n"
"ds_read_b64_tr_b16 a[48:49], v21 offset:20544\n"
"ds_read_b64_tr_b16 a[50:51], v21 offset:25664\n"
"ds_read_b64_tr_b16 a[52:53], v21 offset:20552\n"
"ds_read_b64_tr_b16 a[54:55], v21 offset:25672\n"
"ds_read_b64_tr_b16 a[56:57], v22 offset:20480\n"
"ds_read_b64_tr_b16 a[58:59], v22 offset:25600\n"
"s_waitcnt lgkmcnt(0)\n"
"ds_read_b64_tr_b16 a[60:61], v21 offset:30720\n"
"ds_read_b64_tr_b16 a[62:63], v21 offset:35840\n"
"ds_read_b64_tr_b16 a[64:65], v21 offset:30728\n"
"ds_read_b64_tr_b16 a[66:67], v21 offset:35848\n"
"ds_read_b64_tr_b16 a[68:69], v21 offset:30784\n"
"ds_read_b64_tr_b16 a[70:71], v21 offset:35904\n"
"ds_read_b64_tr_b16 a[72:73], v21 offset:30792\n"
"ds_read_b64_tr_b16 a[74:75], v21 offset:35912\n"
"ds_read_b64_tr_b16 a[76:77], v22 offset:30720\n"
"ds_read_b64_tr_b16 a[78:79], v22 offset:35840\n"
"s_waitcnt lgkmcnt(0)\n"
"v_add_u32 v21, 40960, v21\n"
"v_add_u32 v22, 40960, v22\n"
"ds_read_b64_tr_b16 a[80:81], v21 offset:0\n"
"ds_read_b64_tr_b16 a[82:83], v21 offset:5120\n"
"ds_read_b64_tr_b16 a[84:85], v21 offset:8\n"
"ds_read_b64_tr_b16 a[86:87], v21 offset:5128\n"
"ds_read_b64_tr_b16 a[88:89], v21 offset:64\n"
"ds_read_b64_tr_b16 a[90:91], v21 offset:5184\n"
"ds_read_b64_tr_b16 a[92:93], v21 offset:72\n"
"ds_read_b64_tr_b16 a[94:95], v21 offset:5192\n"
"ds_read_b64_tr_b16 a[96:97], v22 offset:0\n"
"ds_read_b64_tr_b16 a[98:99], v22 offset:5120\n"
"s_waitcnt lgkmcnt(0)\n"
"ds_read_b64_tr_b16 a[100:101], v21 offset:10240\n"
"ds_read_b64_tr_b16 a[102:103], v21 offset:15360\n"
"ds_read_b64_tr_b16 a[104:105], v21 offset:10248\n"
"ds_read_b64_tr_b16 a[106:107], v21 offset:15368\n"
"ds_read_b64_tr_b16 a[108:109], v21 offset:10304\n"
"ds_read_b64_tr_b16 a[110:111], v21 offset:15424\n"
"ds_read_b64_tr_b16 a[112:113], v21 offset:10312\n"
"ds_read_b64_tr_b16 a[114:115], v21 offset:15432\n"
"ds_read_b64_tr_b16 a[116:117], v22 offset:10240\n"
"ds_read_b64_tr_b16 a[118:119], v22 offset:15360\n"
"s_waitcnt lgkmcnt(0)\n"
"ds_read_b64_tr_b16 a[120:121], v21 offset:20480\n"
"ds_read_b64_tr_b16 a[122:123], v21 offset:25600\n"
"ds_read_b64_tr_b16 a[124:125], v21 offset:20488\n"
"ds_read_b64_tr_b16 a[126:127], v21 offset:25608\n"
"ds_read_b64_tr_b16 a[128:129], v21 offset:20544\n"
"ds_read_b64_tr_b16 a[130:131], v21 offset:25664\n"
"ds_read_b64_tr_b16 a[132:133], v21 offset:20552\n"
"ds_read_b64_tr_b16 a[134:135], v21 offset:25672\n"
"ds_read_b64_tr_b16 a[136:137], v22 offset:20480\n"
"ds_read_b64_tr_b16 a[138:139], v22 offset:25600\n"
"s_waitcnt lgkmcnt(0)\n"
"ds_read_b64_tr_b16 a[140:141], v21 offset:30720\n"
"ds_read_b64_tr_b16 a[142:143], v21 offset:35840\n"
"ds_read_b64_tr_b16 a[144:145], v21 offset:30728\n"
"ds_read_b64_tr_b16 a[146:147], v21 offset:35848\n"
"ds_read_b64_tr_b16 a[148:149], v21 offset:30784\n"
"ds_read_b64_tr_b16 a[150:151], v21 offset:35904\n"
"ds_read_b64_tr_b16 a[152:153], v21 offset:30792\n"
"ds_read_b64_tr_b16 a[154:155], v21 offset:35912\n"
"ds_read_b64_tr_b16 a[156:157], v22 offset:30720\n"
"ds_read_b64_tr_b16 a[158:159], v22 offset:35840\n"
"s_waitcnt lgkmcnt(0)\n"
"v_add_u32 v21, 40960, v21\n"
"v_add_u32 v22, 40960, v22\n"
"ds_read_b64_tr_b16 a[160:161], v21 offset:0\n"
"ds_read_b64_tr_b16 a[162:163], v21 offset:5120\n"
"ds_read_b64_tr_b16 a[164:165], v21 offset:8\n"
"ds_read_b64_tr_b16 a[166:167], v21 offset:5128\n"
"ds_read_b64_tr_b16 a[168:169], v21 offset:64\n"
"ds_read_b64_tr_b16 a[170:171], v21 offset:5184\n"
"ds_read_b64_tr_b16 a[172:173], v21 offset:72\n"
"ds_read_b64_tr_b16 a[174:175], v21 offset:5192\n"
"ds_read_b64_tr_b16 a[176:177], v22 offset:0\n"
"ds_read_b64_tr_b16 a[178:179], v22 offset:5120\n"
"s_waitcnt lgkmcnt(0)\n"
"ds_read_b64_tr_b16 a[180:181], v21 offset:10240\n"
"ds_read_b64_tr_b16 a[182:183], v21 offset:15360\n"
"ds_read_b64_tr_b16 a[184:185], v21 offset:10248\n"
"ds_read_b64_tr_b16 a[186:187], v21 offset:15368\n"
"ds_read_b64_tr_b16 a[188:189], v21 offset:10304\n"
"ds_read_b64_tr_b16 a[190:191], v21 offset:15424\n"
"ds_read_b64_tr_b16 a[192:193], v21 offset:10312\n"
"ds_read_b64_tr_b16 a[194:195], v21 offset:15432\n"
"ds_read_b64_tr_b16 a[196:197], v22 offset:10240\n"
"ds_read_b64_tr_b16 a[198:199], v22 offset:15360\n"
"s_waitcnt lgkmcnt(0)\n"
"ds_read_b64_tr_b16 a[200:201], v21 offset:20480\n"
"ds_read_b64_tr_b16 a[202:203], v21 offset:25600\n"
"ds_read_b64_tr_b16 a[204:205], v21 offset:20488\n"
"ds_read_b64_tr_b16 a[206:207], v21 offset:25608\n"
"ds_read_b64_tr_b16 a[208:209], v21 offset:20544\n"
"ds_read_b64_tr_b16 a[210:211], v21 offset:25664\n"
"ds_read_b64_tr_b16 a[212:213], v21 offset:20552\n"
"ds_read_b64_tr_b16 a[214:215], v21 offset:25672\n"
"ds_read_b64_tr_b16 a[216:217], v22 offset:20480\n"
"ds_read_b64_tr_b16 a[218:219], v22 offset:25600\n"
"s_waitcnt lgkmcnt(0)\n"
"ds_read_b64_tr_b16 a[220:221], v21 offset:30720\n"
"ds_read_b64_tr_b16 a[222:223], v21 offset:35840\n"
"ds_read_b64_tr_b16 a[224:225], v21 offset:30728\n"
"ds_read_b64_tr_b16 a[226:227], v21 offset:35848\n"
"ds_read_b64_tr_b16 a[228:229], v21 offset:30784\n"
"ds_read_b64_tr_b16 a[230:231], v21 offset:35904\n"
"ds_read_b64_tr_b16 a[232:233], v21 offset:30792\n"
"ds_read_b64_tr_b16 a[234:235], v21 offset:35912\n"
"ds_read_b64_tr_b16 a[236:237], v22 offset:30720\n"
"ds_read_b64_tr_b16 a[238:239], v22 offset:35840\n"
"s_waitcnt lgkmcnt(0)\n"
"v_add_u32 v21, 40960, v21\n"
"v_add_u32 v22, 40960, v22\n"
"ds_read_b64_tr_b16 a[240:241], v21 offset:0\n"
"ds_read_b64_tr_b16 a[242:243], v21 offset:5120\n"
"ds_read_b64_tr_b16 a[244:245], v21 offset:8\n"
"ds_read_b64_tr_b16 a[246:247], v21 offset:5128\n"
"ds_read_b64_tr_b16 a[248:249], v21 offset:64\n"
"ds_read_b64_tr_b16 a[250:251], v21 offset:5184\n"
"ds_read_b64_tr_b16 a[252:253], v21 offset:72\n"
"ds_read_b64_tr_b16 a[254:255], v21 offset:5192\n"
"ds_read_b64_tr_b16 v[192:193], v22 offset:0\n"
"ds_read_b64_tr_b16 v[194:195], v22 offset:5120\n"
"s_waitcnt lgkmcnt(0)\n"
"ds_read_b64_tr_b16 v[196:197], v21 offset:10240\n"
"ds_read_b64_tr_b16 v[198:199], v21 offset:15360\n"
"ds_read_b64_tr_b16 v[200:201], v21 offset:10248\n"
"ds_read_b64_tr_b16 v[202:203], v21 offset:15368\n"
"ds_read_b64_tr_b16 v[204:205], v21 offset:10304\n"
"ds_read_b64_tr_b16 v[206:207], v21 offset:15424\n"
"ds_read_b64_tr_b16 v[208:209], v21 offset:10312\n"
"ds_read_b64_tr_b16 v[210:211], v21 offset:15432\n"
"ds_read_b64_tr_b16 v[212:213], v22 offset:10240\n"
"ds_read_b64_tr_b16 v[214:215], v22 offset:15360\n"
"s_waitcnt lgkmcnt(0)\n"
"ds_read_b64_tr_b16 v[216:217], v21 offset:20480\n"
"ds_read_b64_tr_b16 v[218:219], v21 offset:25600\n"
"ds_read_b64_tr_b16 v[220:221], v21 offset:20488\n"
"ds_read_b64_tr_b16 v[222:223], v21 offset:25608\n"
"ds_read_b64_tr_b16 v[224:225], v21 offset:20544\n"
"ds_read_b64_tr_b16 v[226:227], v21 offset:25664\n"
"ds_read_b64_tr_b16 v[228:229], v21 offset:20552\n"
"ds_read_b64_tr_b16 v[230:231], v21 offset:25672\n"
"ds_read_b64_tr_b16 v[232:233], v22 offset:20480\n"
"ds_read_b64_tr_b16 v[234:235], v22 offset:25600\n"
"s_waitcnt lgkmcnt(0)\n"
"ds_read_b64_tr_b16 v[236:237], v21 offset:30720\n"
"ds_read_b64_tr_b16 v[238:239], v21 offset:35840\n"
"ds_read_b64_tr_b16 v[240:241], v21 offset:30728\n"
"ds_read_b64_tr_b16 v[242:243], v21 offset:35848\n"
"ds_read_b64_tr_b16 v[244:245], v21 offset:30784\n"
"ds_read_b64_tr_b16 v[246:247], v21 offset:35904\n"
"ds_read_b64_tr_b16 v[248:249], v21 offset:30792\n"
"ds_read_b64_tr_b16 v[250:251], v21 offset:35912\n"
"ds_read_b64_tr_b16 v[252:253], v22 offset:30720\n"
"ds_read_b64_tr_b16 v[254:255], v22 offset:35840\n"
"s_waitcnt lgkmcnt(0)\n"
"s_waitcnt lgkmcnt(0)\n"
"s_barrier\n"
"s_and_b32 s43, s20, 1\n"
"s_lshl_b32 s40, s43, 10\n"
"v_add_u32 v7, s40, v0\n"
"s_lshl2_add_u32 s40, s64, s20\n"
"s_lshl_b32 s68, s40, 14\n"
"s_add_u32 s40, s40, 128\n"
"s_lshl_b32 s40, s40, 7\n"
"v_mov_b32 v1, s40\n"
"s_add_u32 s40, s21, 256\n"
"s_lshl_b32 s40, s40, 7\n"
"s_lshl2_add_u32 s37, s20, s40\n"
"v_lshrrev_b32 v13, 1, v11\n"
"s_lshl_b32 s40, s43, 3\n"
"v_add_u32 v14, s40, v13\n"
"v_mul_u32_u24 v15, 86, v14\n"
"v_lshrrev_b32 v15, 8, v15\n"
"v_mul_u32_u24 v16, 8704, v15\n"
"v_mul_u32_u24 v13, 2560, v14\n"
"v_add_u32 v13, v13, v16\n"
"v_and_b32 v14, 1, v11\n"
"v_lshl_add_u32 v13, v14, 4, v13\n"
"s_mul_i32 s40, s65, 640\n"
"v_add_u32 v13, s40, v13\n"
"s_mul_i32 s40, s66, 384\n"
"v_lshl_add_u32 v14, v12, 5, v13\n"
"v_add_u32 v4, s40, v14\n"
"s_mul_i32 s40, s66, 64\n"
"s_add_u32 s40, s40, 256\n"
"v_lshrrev_b32 v14, 1, v12\n"
"v_lshl_add_u32 v13, v14, 5, v13\n"
"v_and_b32 v14, 1, v12\n"
"v_lshl_add_u32 v13, v14, 3, v13\n"
"v_add_u32 v5, s40, v13\n"
"W_pol_%=:\n"
"buffer_load_dword v13, off, s[12:15], 0 sc1\n"
"s_waitcnt vmcnt(0)\n"
"v_readfirstlane_b32 s40, v13\n"
"s_add_u32 s23, s23, 1\n"
"s_cmp_ge_u32 s23, 400000\n"
"s_cselect_b32 s40, 16, s40\n"
"s_cmp_eq_u32 s40, 0\n"
"s_cbranch_scc1 W_pol_%=\n"
"s_bitcmp1_b32 s40, 5\n"
"s_cselect_b32 s74, 8192, 0\n"
"s_xor_b32 s75, s74, 8192\n"
"s_add_u32 s37, s37, s74\n"
"v_add_u32 v1, s74, v1\n"
"s_mul_i32 s66, s64, 98304\n"
"s_add_u32 s66, s66, s75\n"
"s_add_u32 s66, s66, 11534336\n"
"s_add_u32 s68, s68, s75\n"
"s_add_u32 s68, s68, 11010048\n"
"s_mov_b32 s24, 0\n"
"s_mov_b32 s25, 0\n"
"s_mov_b32 s35, s22\n"
"s_mov_b32 s36, 0\n"
"v_cmp_gt_u32 vcc, 1, v10\n"
"v_mov_b32 v13, s22\n"
"s_nop 0\n"
"v_cndmask_b32 v1, v13, v1, vcc\n"
"s_lshl_b32 s34, s20, 13\n"
"s_sub_u32 s40, s24, s25\n"
"s_cmp_ge_u32 s40, 9\n"
"s_cselect_b32 s29, 1, 0\n"
"s_cmp_ge_u32 s24, 64\n"
"s_cselect_b32 s29, 1, s29\n"
"s_cmp_eq_u32 s29, 0\n"
"s_cselect_b32 s30, s24, s25\n"
"s_cselect_b32 s40, 1, 0\n"
"s_add_u32 s24, s24, s40\n"
"s_add_u32 s25, s25, s29\n"
"s_cmp_lt_u32 s30, 64\n"
"s_cselect_b32 s30, s30, -1\n"
"s_lshl_b32 s38, s29, 12\n"
"s_mul_i32 s40, s29, 15\n"
"s_lshl1_add_u32 s40, s30, s40\n"
"s_and_b32 s40, s40, 31\n"
"s_lshl_b32 s40, s40, 22\n"
"s_add_u32 s31, s40, s68\n"
"W_cold_%=:\n"
"s_waitcnt vmcnt(0)\n"
"v_mov_b32 v6, s36\n"
"buffer_store_dword v6, off, s[12:15], s35 sc1\n"
"s_mov_b32 s35, s22\n"
"s_cmp_lt_i32 s30, 0\n"
"s_cbranch_scc1 W_end_%=\n"
"W_spin_%=:\n"
"buffer_load_dword v2, v1, s[12:15], s38 offen sc1\n"
"s_waitcnt vmcnt(0)\n"
"v_cmp_ge_u32 vcc, s30, v2\n"
"s_nop 0\n"
"s_and_b32 s40, vcc_lo, 1\n"
"s_cbranch_scc0 W_go_%=\n"
"s_sleep 4\n"
"s_add_u32 s23, s23, 1\n"
"s_cmp_lt_u32 s23, 400000\n"
"s_cbranch_scc1 W_spin_%=\n"
"W_go_%=:\n"
"s_add_u32 m0, s34, 0\n"
"s_add_u32 s40, s31, 0\n"
"buffer_load_dwordx4 v0, s[12:15], s40 offen sc1 lds\n"
"s_add_u32 m0, s34, 1024\n"
"s_add_u32 s40, s31, 1024\n"
"buffer_load_dwordx4 v0, s[12:15], s40 offen sc1 lds\n"
"s_add_u32 m0, s34, 2048\n"
"s_add_u32 s40, s31, 2048\n"
"buffer_load_dwordx4 v0, s[12:15], s40 offen sc1 lds\n"
"s_add_u32 m0, s34, 3072\n"
"s_add_u32 s40, s31, 3072\n"
"buffer_load_dwordx4 v0, s[12:15], s40 offen sc1 lds\n"
"s_add_u32 m0, s34, 4096\n"
"s_add_u32 s40, s31, 4096\n"
"buffer_load_dwordx4 v0, s[12:15], s40 offen sc1 lds\n"
"s_add_u32 m0, s34, 5120\n"
"s_add_u32 s40, s31, 5120\n"
"buffer_load_dwordx4 v0, s[12:15], s40 offen sc1 lds\n"
"s_add_u32 m0, s34, 6144\n"
"s_add_u32 s40, s31, 6144\n"
"buffer_load_dwordx4 v0, s[12:15], s40 offen sc1 lds\n"
"s_add_u32 m0, s34, 7168\n"
"s_add_u32 s40, s31, 7168\n"
"buffer_load_dwordx4 v0, s[12:15], s40 offen sc1 lds\n"
"buffer_store_dword v6, off, s[12:15], s22\n"
"buffer_store_dword v6, off, s[12:15], s22\n"
"buffer_store_dword v6, off, s[12:15], s22\n"
"W_top_%=:\n"
"s_mov_b32 s27, s29\n"
"s_mov_b32 s28, s30\n"
"s_mul_i32 s40, s27, 15\n"
"s_lshl1_add_u32 s40, s28, s40\n"
"s_and_b32 s40, s40, 31\n"
"s_lshl_b32 s40, s40, 22\n"
"s_add_u32 s33, s40, s66\n"
"s_cmp_ge_u32 s34, 32768\n"
"s_cselect_b32 s40, 32768, 0\n"
"v_add_u32 v3, s40, v7\n"
"s_lshl_b32 s40, s40, 1\n"
"s_add_u32 s34, s34, 32768\n"
"s_sub_u32 s34, s34, s40\n"
"s_sub_u32 s40, s24, s25\n"
"s_cmp_ge_u32 s40, 9\n"
"s_cselect_b32 s29, 1, 0\n"
"s_cmp_ge_u32 s24, 64\n"
"s_cselect_b32 s29, 1, s29\n"
"s_cmp_eq_u32 s29, 0\n"
"s_cselect_b32 s30, s24, s25\n"
"s_cselect_b32 s40, 1, 0\n"
"s_add_u32 s24, s24, s40\n"
"s_add_u32 s25, s25, s29\n"
"s_cmp_lt_u32 s30, 64\n"
"s_cselect_b32 s30, s30, -1\n"
"s_lshl_b32 s38, s29, 12\n"
"s_mul_i32 s40, s29, 15\n"
"s_lshl1_add_u32 s40, s30, s40\n"
"s_and_b32 s40, s40, 31\n"
"s_lshl_b32 s40, s40, 22\n"
"s_add_u32 s31, s40, s68\n"
"buffer_load_dword v2, v1, s[12:15], s38 offen sc1\n"
"s_waitcnt vmcnt(4)\n"
"s_barrier\n"
"ds_read_b128 v[40:43], v3 offset:0\n"
"ds_read_b128 v[44:47], v3 offset:2048\n"
"ds_read_b128 v[48:51], v3 offset:4096\n"
"ds_read_b128 v[52:55], v3 offset:6144\n"
"s_waitcnt lgkmcnt(3)\n"
"v_mfma_f32_16x16x32_bf16 v[16:19], a[0:3], v[40:43], 0\n"
"v_mfma_f32_16x16x32_bf16 v[20:23], a[4:7], v[40:43], 0\n"
"v_mfma_f32_16x16x32_bf16 v[24:27], a[8:11], v[40:43], 0\n"
"v_mfma_f32_16x16x32_bf16 v[28:31], a[12:15], v[40:43], 0\n"
"v_mfma_f32_16x16x32_bf16 v[32:35], a[16:19], v[40:43], 0\n"
"ds_read_b128 v[40:43], v3 offset:8192\n"
"s_waitcnt lgkmcnt(3)\n"
"v_mfma_f32_16x16x32_bf16 v[16:19], a[20:23], v[44:47], v[16:19]\n"
"v_mfma_f32_16x16x32_bf16 v[20:23], a[24:27], v[44:47], v[20:23]\n"
"v_mfma_f32_16x16x32_bf16 v[24:27], a[28:31], v[44:47], v[24:27]\n"
"v_mfma_f32_16x16x32_bf16 v[28:31], a[32:35], v[44:47], v[28:31]\n"
"v_mfma_f32_16x16x32_bf16 v[32:35], a[36:39], v[44:47], v[32:35]\n"
"ds_read_b128 v[44:47], v3 offset:10240\n"
"s_waitcnt lgkmcnt(3)\n"
"v_mfma_f32_16x16x32_bf16 v[16:19], a[40:43], v[48:51], v[16:19]\n"
"v_mfma_f32_16x16x32_bf16 v[20:23], a[44:47], v[48:51], v[20:23]\n"
"v_mfma_f32_16x16x32_bf16 v[24:27], a[48:51], v[48:51], v[24:27]\n"
"v_mfma_f32_16x16x32_bf16 v[28:31], a[52:55], v[48:51], v[28:31]\n"
"v_mfma_f32_16x16x32_bf16 v[32:35], a[56:59], v[48:51], v[32:35]\n"
"ds_read_b128 v[48:51], v3 offset:12288\n"
"s_waitcnt lgkmcnt(3)\n"
"v_mfma_f32_16x16x32_bf16 v[16:19], a[60:63], v[52:55], v[16:19]\n"
"v_mfma_f32_16x16x32_bf16 v[20:23], a[64:67], v[52:55], v[20:23]\n"
"v_mfma_f32_16x16x32_bf16 v[24:27], a[68:71], v[52:55], v[24:27]\n"
"v_mfma_f32_16x16x32_bf16 v[28:31], a[72:75], v[52:55], v[28:31]\n"
"v_mfma_f32_16x16x32_bf16 v[32:35], a[76:79], v[52:55], v[32:35]\n"
"ds_read_b128 v[52:55], v3 offset:14336\n"
"s_waitcnt lgkmcnt(3)\n"
"v_mfma_f32_16x16x32_bf16 v[16:19], a[80:83], v[40:43], v[16:19]\n"
"v_mfma_f32_16x16x32_bf16 v[20:23], a[84:87], v[40:43], v[20:23]\n"
"v_mfma_f32_16x16x32_bf16 v[24:27], a[88:91], v[40:43], v[24:27]\n"
"v_mfma_f32_16x16x32_bf16 v[28:31], a[92:95], v[40:43], v[28:31]\n"
"v_mfma_f32_16x16x32_bf16 v[32:35], a[96:99], v[40:43], v[32:35]\n"
"ds_read_b128 v[40:43], v3 offset:16384\n"
"s_waitcnt lgkmcnt(3)\n"
"v_mfma_f32_16x16x32_bf16 v[16:19], a[100:103], v[44:47], v[16:19]\n"
"v_mfma_f32_16x16x32_bf16 v[20:23], a[104:107], v[44:47], v[20:23]\n"
"v_mfma_f32_16x16x32_bf16 v[24:27], a[108:111], v[44:47], v[24:27]\n"
"v_mfma_f32_16x16x32_bf16 v[28:31], a[112:115], v[44:47], v[28:31]\n"
"v_mfma_f32_16x16x32_bf16 v[32:35], a[116:119], v[44:47], v[32:35]\n"
"ds_read_b128 v[44:47], v3 offset:18432\n"
"s_waitcnt lgkmcnt(3)\n"
"v_mfma_f32_16x16x32_bf16 v[16:19], a[120:123], v[48:51], v[16:19]\n"
"v_mfma_f32_16x16x32_bf16 v[20:23], a[124:127], v[48:51], v[20:23]\n"
"v_mfma_f32_16x16x32_bf16 v[24:27], a[128:131], v[48:51], v[24:27]\n"
"v_mfma_f32_16x16x32_bf16 v[28:31], a[132:135], v[48:51], v[28:31]\n"
"v_mfma_f32_16x16x32_bf16 v[32:35], a[136:139], v[48:51], v[32:35]\n"
"ds_read_b128 v[48:51], v3 offset:20480\n"
"s_waitcnt lgkmcnt(3)\n"
"v_mfma_f32_16x16x32_bf16 v[16:19], a[140:143], v[52:55], v[16:19]\n"
"v_mfma_f32_16x16x32_bf16 v[20:23], a[144:147], v[52:55], v[20:23]\n"
"v_mfma_f32_16x16x32_bf16 v[24:27], a[148:151], v[52:55], v[24:27]\n"
"v_mfma_f32_16x16x32_bf16 v[28:31], a[152:155], v[52:55], v[28:31]\n"
"v_mfma_f32_16x16x32_bf16 v[32:35], a[156:159], v[52:55], v[32:35]\n"
"s_waitcnt vmcnt(0)\n"
"v_mov_b32 v6, s36\n"
"buffer_store_dword v6, off, s[12:15], s35 sc1\n"
"v_cmp_ge_u32 vcc, s30, v2\n"
"s_nop 0\n"
"s_and_b32 s40, vcc_lo, 1\n"
"s_cselect_b32 s26, 0, 1\n"
"s_cselect_b32 s39, s22, s31\n"
"ds_read_b128 v[52:55], v3 offset:22528\n"
"s_waitcnt lgkmcnt(3)\n"
"v_mfma_f32_16x16x32_bf16 v[16:19], a[160:163], v[40:43], v[16:19]\n"
"s_add_u32 m0, s34, 0\n"
"v_mfma_f32_16x16x32_bf16 v[20:23], a[164:167], v[40:43], v[20:23]\n"
"s_add_u32 s40, s39, 0\n"
"v_mfma_f32_16x16x32_bf16 v[24:27], a[168:171], v[40:43], v[24:27]\n"
"buffer_load_dwordx4 v0, s[12:15], s40 offen sc1 lds\n"
"v_mfma_f32_16x16x32_bf16 v[28:31], a[172:175], v[40:43], v[28:31]\n"
"v_mfma_f32_16x16x32_bf16 v[32:35], a[176:179], v[40:43], v[32:35]\n"
"ds_read_b128 v[40:43], v3 offset:24576\n"
"s_waitcnt lgkmcnt(3)\n"
"v_mfma_f32_16x16x32_bf16 v[16:19], a[180:183], v[44:47], v[16:19]\n"
"s_add_u32 m0, s34, 1024\n"
"v_mfma_f32_16x16x32_bf16 v[20:23], a[184:187], v[44:47], v[20:23]\n"
"s_add_u32 s40, s39, 1024\n"
"v_mfma_f32_16x16x32_bf16 v[24:27], a[188:191], v[44:47], v[24:27]\n"
"buffer_load_dwordx4 v0, s[12:15], s40 offen sc1 lds\n"
"v_mfma_f32_16x16x32_bf16 v[28:31], a[192:195], v[44:47], v[28:31]\n"
"v_mfma_f32_16x16x32_bf16 v[32:35], a[196:199], v[44:47], v[32:35]\n"
"ds_read_b128 v[44:47], v3 offset:26624\n"
"s_waitcnt lgkmcnt(3)\n"
"v_mfma_f32_16x16x32_bf16 v[16:19], a[200:203], v[48:51], v[16:19]\n"
"s_add_u32 m0, s34, 2048\n"
"v_mfma_f32_16x16x32_bf16 v[20:23], a[204:207], v[48:51], v[20:23]\n"
"s_add_u32 s40, s39, 2048\n"
"v_mfma_f32_16x16x32_bf16 v[24:27], a[208:211], v[48:51], v[24:27]\n"
"buffer_load_dwordx4 v0, s[12:15], s40 offen sc1 lds\n"
"v_mfma_f32_16x16x32_bf16 v[28:31], a[212:215], v[48:51], v[28:31]\n"
"v_mfma_f32_16x16x32_bf16 v[32:35], a[216:219], v[48:51], v[32:35]\n"
"ds_read_b128 v[48:51], v3 offset:28672\n"
"s_waitcnt lgkmcnt(3)\n"
"v_mfma_f32_16x16x32_bf16 v[16:19], a[220:223], v[52:55], v[16:19]\n"
"s_add_u32 m0, s34, 3072\n"
"v_mfma_f32_16x16x32_bf16 v[20:23], a[224:227], v[52:55], v[20:23]\n"
"s_add_u32 s40, s39, 3072\n"
"v_mfma_f32_16x16x32_bf16 v[24:27], a[228:231], v[52:55], v[24:27]\n"
"buffer_load_dwordx4 v0, s[12:15], s40 offen sc1 lds\n"
"v_mfma_f32_16x16x32_bf16 v[28:31], a[232:235], v[52:55], v[28:31]\n"
"v_mfma_f32_16x16x32_bf16 v[32:35], a[236:239], v[52:55], v[32:35]\n"
"ds_read_b128 v[52:55], v3 offset:30720\n"
"s_waitcnt lgkmcnt(3)\n"
"v_mfma_f32_16x16x32_bf16 v[16:19], a[240:243], v[40:43], v[16:19]\n"
"s_add_u32 m0, s34, 4096\n"
"v_mfma_f32_16x16x32_bf16 v[20:23], a[244:247], v[40:43], v[20:23]\n"
"s_add_u32 s40, s39, 4096\n"
"v_mfma_f32_16x16x32_bf16 v[24:27], a[248:251], v[40:43], v[24:27]\n"
"buffer_load_dwordx4 v0, s[12:15], s40 offen sc1 lds\n"
"v_mfma_f32_16x16x32_bf16 v[28:31], a[252:255], v[40:43], v[28:31]\n"
"v_mfma_f32_16x16x32_bf16 v[32:35], v[192:195], v[40:43], v[32:35]\n"
"s_waitcnt lgkmcnt(2)\n"
"v_mfma_f32_16x16x32_bf16 v[16:19], v[196:199], v[44:47], v[16:19]\n"
"s_add_u32 m0, s34, 5120\n"
"v_mfma_f32_16x16x32_bf16 v[20:23], v[200:203], v[44:47], v[20:23]\n"
"s_add_u32 s40, s39, 5120\n"
"v_mfma_f32_16x16x32_bf16 v[24:27], v[204:207], v[44:47], v[24:27]\n"
"buffer_load_dwordx4 v0, s[12:15], s40 offen sc1 lds\n"
"v_mfma_f32_16x16x32_bf16 v[28:31], v[208:211], v[44:47], v[28:31]\n"
"v_mfma_f32_16x16x32_bf16 v[32:35], v[212:215], v[44:47], v[32:35]\n"
"s_waitcnt lgkmcnt(1)\n"
"v_mfma_f32_16x16x32_bf16 v[16:19], v[216:219], v[48:51], v[16:19]\n"
"s_add_u32 m0, s34, 6144\n"
"v_mfma_f32_16x16x32_bf16 v[20:23], v[220:223], v[48:51], v[20:23]\n"
"s_add_u32 s40, s39, 6144\n"
"v_mfma_f32_16x16x32_bf16 v[24:27], v[224:227], v[48:51], v[24:27]\n"
"buffer_load_dwordx4 v0, s[12:15], s40 offen sc1 lds\n"
"v_mfma_f32_16x16x32_bf16 v[28:31], v[228:231], v[48:51], v[28:31]\n"
"v_mfma_f32_16x16x32_bf16 v[32:35], v[232:235], v[48:51], v[32:35]\n"
"s_waitcnt lgkmcnt(0)\n"
"v_mfma_f32_16x16x32_bf16 v[16:19], v[236:239], v[52:55], v[16:19]\n"
"s_add_u32 m0, s34, 7168\n"
"v_mfma_f32_16x16x32_bf16 v[20:23], v[240:243], v[52:55], v[20:23]\n"
"s_add_u32 s40, s39, 7168\n"
"v_mfma_f32_16x16x32_bf16 v[24:27], v[244:247], v[52:55], v[24:27]\n"
"buffer_load_dwordx4 v0, s[12:15], s40 offen sc1 lds\n"
"v_mfma_f32_16x16x32_bf16 v[28:31], v[248:251], v[52:55], v[28:31]\n"
"v_mfma_f32_16x16x32_bf16 v[32:35], v[252:255], v[52:55], v[32:35]\n"
"s_nop 0\n"
"v_cvt_pk_bf16_f32 v64, v16, v17\n"
"v_cvt_pk_bf16_f32 v65, v18, v19\n"
"v_cvt_pk_bf16_f32 v66, v20, v21\n"
"v_cvt_pk_bf16_f32 v67, v22, v23\n"
"buffer_store_dwordx4 v[64:67], v4, s[12:15], s33 offen offset:0 sc1\n"
"v_cvt_pk_bf16_f32 v68, v24, v25\n"
"v_cvt_pk_bf16_f32 v69, v26, v27\n"
"v_cvt_pk_bf16_f32 v70, v28, v29\n"
"v_cvt_pk_bf16_f32 v71, v30, v31\n"
"buffer_store_dwordx4 v[68:71], v4, s[12:15], s33 offen offset:128 sc1\n"
"v_cvt_pk_bf16_f32 v72, v32, v33\n"
"v_cvt_pk_bf16_f32 v73, v34, v35\n"
"buffer_store_dwordx2 v[72:73], v5, s[12:15], s33 offen sc1\n"
"s_lshl_b32 s40, s27, 4\n"
"s_add_u32 s35, s37, s40\n"
"s_add_u32 s36, s28, 1\n"
"s_cmp_lg_u32 s26, 0\n"
"s_cbranch_scc1 W_top_%=\n"
"s_branch W_cold_%=\n"
"W_end_%=:\n"
"s_waitcnt vmcnt(0)\n"
"s_endpgm\n"
"ROLE3_%=:\n"
"s_load_dwordx8 s[52:59], %0, 16\n"
"s_load_dwordx4 s[60:63], %0, 48\n"
"s_load_dwordx2 s[12:13], %0, 72\n"
"s_mov_b32 s21, %1\n"
"s_mov_b32 s20, %2\n"
"s_mov_b32 s22, 0x40000000\n"
"s_mov_b32 s23, 0\n"
"v_mbcnt_lo_u32_b32 v10, -1, 0\n"
"v_mbcnt_hi_u32_b32 v10, -1, v10\n"
"v_lshlrev_b32 v0, 4, v10\n"
"v_and_b32 v11, 15, v10\n"
"v_lshrrev_b32 v12, 4, v10\n"
"s_waitcnt lgkmcnt(0)\n"
"s_and_b32 s13, s13, 0xffff\n"
"s_mov_b32 s14, 149946368\n"
"s_mov_b32 s15, 0x20000\n"
"s_cmp_ge_u32 s21, 14\n"
"s_cselect_b32 s64, 1, 0\n"
"s_mul_i32 s40, s64, 14\n"
"s_sub_u32 s65, s21, s40\n"
"s_sleep 100\n"
"s_mov_b32 s16, s60\n"
"s_and_b32 s17, s61, 0xffff\n"
"s_mov_b32 s18, 2211840\n"
"s_mov_b32 s19, 0x20000\n"
"s_lshl_b32 s46, s65, 8\n"
"s_lshr_b32 s40, s20, 1\n"
"s_lshl_b32 s40, s40, 1\n"
"v_lshl_or_b32 v13, v12, 2, s40\n"
"v_xor_b32 v13, v13, v11\n"
"v_lshlrev_b32 v13, 4, v13\n"
"v_mul_u32_u24 v14, 3456, v12\n"
"v_add_u32 v13, v13, v14\n"
"s_mul_i32 s40, s20, 13824\n"
"s_add_u32 s40, s40, s46\n"
"s_lshl_b32 s41, s20, 10\n"
"s_add_u32 m0, s41, 0\n"
"s_add_u32 s42, s40, 0\n"
"buffer_load_dwordx4 v13, s[16:19], s42 offen lds\n"
"s_add_u32 m0, s41, 4096\n"
"s_add_u32 s42, s40, 55296\n"
"buffer_load_dwordx4 v13, s[16:19], s42 offen lds\n"
"s_add_u32 m0, s41, 8192\n"
"s_add_u32 s42, s40, 110592\n"
"buffer_load_dwordx4 v13, s[16:19], s42 offen lds\n"
"s_add_u32 m0, s41, 12288\n"
"s_add_u32 s42, s40, 165888\n"
"buffer_load_dwordx4 v13, s[16:19], s42 offen lds\n"
"s_add_u32 m0, s41, 16384\n"
"s_add_u32 s42, s40, 221184\n"
"buffer_load_dwordx4 v13, s[16:19], s42 offen lds\n"
"s_add_u32 m0, s41, 20480\n"
"s_add_u32 s42, s40, 276480\n"
"buffer_load_dwordx4 v13, s[16:19], s42 offen lds\n"
"s_add_u32 m0, s41, 24576\n"
"s_add_u32 s42, s40, 331776\n"
"buffer_load_dwordx4 v13, s[16:19], s42 offen lds\n"
"s_add_u32 m0, s41, 28672\n"
"s_add_u32 s42, s40, 387072\n"
"buffer_load_dwordx4 v13, s[16:19], s42 offen lds\n"
"s_add_u32 m0, s41, 32768\n"
"s_add_u32 s42, s40, 442368\n"
"buffer_load_dwordx4 v13, s[16:19], s42 offen lds\n"
"s_add_u32 m0, s41, 36864\n"
"s_add_u32 s42, s40, 497664\n"
"buffer_load_dwordx4 v13, s[16:19], s42 offen lds\n"
"s_add_u32 m0, s41, 40960\n"
"s_add_u32 s42, s40, 552960\n"
"buffer_load_dwordx4 v13, s[16:19], s42 offen lds\n"
"s_add_u32 m0, s41, 45056\n"
"s_add_u32 s42, s40, 608256\n"
"buffer_load_dwordx4 v13, s[16:19], s42 offen lds\n"
"s_add_u32 m0, s41, 49152\n"
"s_add_u32 s42, s40, 663552\n"
"buffer_load_dwordx4 v13, s[16:19], s42 offen lds\n"
"s_add_u32 m0, s41, 53248\n"
"s_add_u32 s42, s40, 718848\n"
"buffer_load_dwordx4 v13, s[16:19], s42 offen lds\n"
"s_add_u32 m0, s41, 57344\n"
"s_add_u32 s42, s40, 774144\n"
"buffer_load_dwordx4 v13, s[16:19], s42 offen lds\n"
"s_add_u32 m0, s41, 61440\n"
"s_add_u32 s42, s40, 829440\n"
"buffer_load_dwordx4 v13, s[16:19], s42 offen lds\n"
"s_add_u32 m0, s41, 65536\n"
"s_add_u32 s42, s40, 884736\n"
"buffer_load_dwordx4 v13, s[16:19], s42 offen lds\n"
"s_add_u32 m0, s41, 69632\n"
"s_add_u32 s42, s40, 940032\n"
"buffer_load_dwordx4 v13, s[16:19], s42 offen lds\n"
"s_add_u32 m0, s41, 73728\n"
"s_add_u32 s42, s40, 995328\n"
"buffer_load_dwordx4 v13, s[16:19], s42 offen lds\n"
"s_add_u32 m0, s41, 77824\n"
"s_add_u32 s42, s40, 1050624\n"
"buffer_load_dwordx4 v13, s[16:19], s42 offen lds\n"
"s_add_u32 m0, s41, 81920\n"
"s_add_u32 s42, s40, 1105920\n"
"buffer_load_dwordx4 v13, s[16:19], s42 offen lds\n"
"s_add_u32 m0, s41, 86016\n"
"s_add_u32 s42, s40, 1161216\n"
"buffer_load_dwordx4 v13, s[16:19], s42 offen lds\n"
"s_add_u32 m0, s41, 90112\n"
"s_add_u32 s42, s40, 1216512\n"
"buffer_load_dwordx4 v13, s[16:19], s42 offen lds\n"
"s_add_u32 m0, s41, 94208\n"
"s_add_u32 s42, s40, 1271808\n"
"buffer_load_dwordx4 v13, s[16:19], s42 offen lds\n"
"s_add_u32 m0, s41, 98304\n"
"s_add_u32 s42, s40, 1327104\n"
"buffer_load_dwordx4 v13, s[16:19], s42 offen lds\n"
"s_add_u32 m0, s41, 102400\n"
"s_add_u32 s42, s40, 1382400\n"
"buffer_load_dwordx4 v13, s[16:19], s42 offen lds\n"
"s_add_u32 m0, s41, 106496\n"
"s_add_u32 s42, s40, 1437696\n"
"buffer_load_dwordx4 v13, s[16:19], s42 offen lds\n"
"s_add_u32 m0, s41, 110592\n"
"s_add_u32 s42, s40, 1492992\n"
"buffer_load_dwordx4 v13, s[16:19], s42 offen lds\n"
"s_add_u32 m0, s41, 114688\n"
"s_add_u32 s42, s40, 1548288\n"
"buffer_load_dwordx4 v13, s[16:19], s42 offen lds\n"
"s_add_u32 m0, s41, 118784\n"
"s_add_u32 s42, s40, 1603584\n"
"buffer_load_dwordx4 v13, s[16:19], s42 offen lds\n"
"s_add_u32 m0, s41, 122880\n"
"s_add_u32 s42, s40, 1658880\n"
"buffer_load_dwordx4 v13, s[16:19], s42 offen lds\n"
"s_add_u32 m0, s41, 126976\n"
"s_add_u32 s42, s40, 1714176\n"
"buffer_load_dwordx4 v13, s[16:19], s42 offen lds\n"
"s_add_u32 m0, s41, 131072\n"
"s_add_u32 s42, s40, 1769472\n"
"buffer_load_dwordx4 v13, s[16:19], s42 offen lds\n"
"s_add_u32 m0, s41, 135168\n"
"s_add_u32 s42, s40, 1824768\n"
"buffer_load_dwordx4 v13, s[16:19], s42 offen lds\n"
"s_add_u32 m0, s41, 139264\n"
"s_add_u32 s42, s40, 1880064\n"
"buffer_load_dwordx4 v13, s[16:19], s42 offen lds\n"
"s_add_u32 m0, s41, 143360\n"
"s_add_u32 s42, s40, 1935360\n"
"buffer_load_dwordx4 v13, s[16:19], s42 offen lds\n"
"s_add_u32 m0, s41, 147456\n"
"s_add_u32 s42, s40, 1990656\n"
"buffer_load_dwordx4 v13, s[16:19], s42 offen lds\n"
"s_add_u32 m0, s41, 151552\n"
"s_add_u32 s42, s40, 2045952\n"
"buffer_load_dwordx4 v13, s[16:19], s42 offen lds\n"
"s_add_u32 m0, s41, 155648\n"
"s_add_u32 s42, s40, 2101248\n"
"buffer_load_dwordx4 v13, s[16:19], s42 offen lds\n"
"s_add_u32 m0, s41, 159744\n"
"s_add_u32 s42, s40, 2156544\n"
"buffer_load_dwordx4 v13, s[16:19], s42 offen lds\n"
"s_waitcnt vmcnt(0)\n"
"s_barrier\n"
"v_and_b32 v18, 3, v11\n"
"s_lshl_b32 s40, s20, 2\n"
"v_lshrrev_b32 v15, 1, v18\n"
"v_add_u32 v15, 0, v15\n"
"v_add_u32 v15, s40, v15\n"
"v_and_b32 v19, 1, v18\n"
"v_lshlrev_b32 v19, 3, v19\n"
"v_lshrrev_b32 v16, 2, v11\n"
"v_and_b32 v17, 1, v12\n"
"v_lshlrev_b32 v17, 1, v17\n"
"v_lshl_or_b32 v17, v16, 2, v17\n"
"v_xor_b32 v17, v17, v15\n"
"v_lshlrev_b32 v17, 4, v17\n"
"v_add_u32 v17, v17, v19\n"
"v_lshl_add_u32 v16, v12, 3, v16\n"
"v_lshl_add_u32 v21, v16, 8, v17\n"
"v_lshrrev_b32 v15, 1, v18\n"
"v_add_u32 v15, 2, v15\n"
"v_add_u32 v15, s40, v15\n"
"v_and_b32 v19, 1, v18\n"
"v_lshlrev_b32 v19, 3, v19\n"
"v_lshrrev_b32 v16, 2, v11\n"
"v_and_b32 v17, 1, v12\n"
"v_lshlrev_b32 v17, 1, v17\n"
"v_lshl_or_b32 v17, v16, 2, v17\n"
"v_xor_b32 v17, v17, v15\n"
"v_lshlrev_b32 v17, 4, v17\n"
"v_add_u32 v17, v17, v19\n"
"v_lshl_add_u32 v16, v12, 3, v16\n"
"v_lshl_add_u32 v22, v16, 8, v17\n"
"ds_read_b64_tr_b16 a[0:1], v21 offset:0\n"
"ds_read_b64_tr_b16 a[2:3], v21 offset:1024\n"
"ds_read_b64_tr_b16 a[8:9], v21 offset:8192\n"
"ds_read_b64_tr_b16 a[10:11], v21 offset:9216\n"
"ds_read_b64_tr_b16 a[16:17], v21 offset:16384\n"
"ds_read_b64_tr_b16 a[18:19], v21 offset:17408\n"
"ds_read_b64_tr_b16 a[24:25], v21 offset:24576\n"
"ds_read_b64_tr_b16 a[26:27], v21 offset:25600\n"
"ds_read_b64_tr_b16 a[32:33], v21 offset:32768\n"
"ds_read_b64_tr_b16 a[34:35], v21 offset:33792\n"
"ds_read_b64_tr_b16 a[40:41], v21 offset:40960\n"
"ds_read_b64_tr_b16 a[42:43], v21 offset:41984\n"
"s_waitcnt lgkmcnt(0)\n"
"ds_read_b64_tr_b16 a[48:49], v21 offset:49152\n"
"ds_read_b64_tr_b16 a[50:51], v21 offset:50176\n"
"ds_read_b64_tr_b16 a[56:57], v21 offset:57344\n"
"ds_read_b64_tr_b16 a[58:59], v21 offset:58368\n"
"v_add_u32 v21, 65536, v21\n"
"ds_read_b64_tr_b16 a[64:65], v21 offset:0\n"
"ds_read_b64_tr_b16 a[66:67], v21 offset:1024\n"
"ds_read_b64_tr_b16 a[72:73], v21 offset:8192\n"
"ds_read_b64_tr_b16 a[74:75], v21 offset:9216\n"
"ds_read_b64_tr_b16 a[80:81], v21 offset:16384\n"
"ds_read_b64_tr_b16 a[82:83], v21 offset:17408\n"
"ds_read_b64_tr_b16 a[88:89], v21 offset:24576\n"
"ds_read_b64_tr_b16 a[90:91], v21 offset:25600\n"
"s_waitcnt lgkmcnt(0)\n"
"ds_read_b64_tr_b16 a[96:97], v21 offset:32768\n"
"ds_read_b64_tr_b16 a[98:99], v21 offset:33792\n"
"ds_read_b64_tr_b16 a[104:105], v21 offset:40960\n"
"ds_read_b64_tr_b16 a[106:107], v21 offset:41984\n"
"ds_read_b64_tr_b16 a[112:113], v21 offset:49152\n"
"ds_read_b64_tr_b16 a[114:115], v21 offset:50176\n"
"ds_read_b64_tr_b16 a[120:121], v21 offset:57344\n"
"ds_read_b64_tr_b16 a[122:123], v21 offset:58368\n"
"v_add_u32 v21, 65536, v21\n"
"ds_read_b64_tr_b16 a[128:129], v21 offset:0\n"
"ds_read_b64_tr_b16 a[130:131], v21 offset:1024\n"
"ds_read_b64_tr_b16 a[136:137], v21 offset:8192\n"
"ds_read_b64_tr_b16 a[138:139], v21 offset:9216\n"
"s_waitcnt lgkmcnt(0)\n"
"ds_read_b64_tr_b16 a[144:145], v21 offset:16384\n"
"ds_read_b64_tr_b16 a[146:147], v21 offset:17408\n"
"ds_read_b64_tr_b16 a[152:153], v21 offset:24576\n"
"ds_read_b64_tr_b16 a[154:155], v21 offset:25600\n"
"s_waitcnt lgkmcnt(0)\n"
"ds_read_b64_tr_b16 a[4:5], v22 offset:0\n"
"ds_read_b64_tr_b16 a[6:7], v22 offset:1024\n"
"ds_read_b64_tr_b16 a[12:13], v22 offset:8192\n"
"ds_read_b64_tr_b16 a[14:15], v22 offset:9216\n"
"ds_read_b64_tr_b16 a[20:21], v22 offset:16384\n"
"ds_read_b64_tr_b16 a[22:23], v22 offset:17408\n"
"ds_read_b64_tr_b16 a[28:29], v22 offset:24576\n"
"ds_read_b64_tr_b16 a[30:31], v22 offset:25600\n"
"ds_read_b64_tr_b16 a[36:37], v22 offset:32768\n"
"ds_read_b64_tr_b16 a[38:39], v22 offset:33792\n"
"ds_read_b64_tr_b16 a[44:45], v22 offset:40960\n"
"ds_read_b64_tr_b16 a[46:47], v22 offset:41984\n"
"s_waitcnt lgkmcnt(0)\n"
"ds_read_b64_tr_b16 a[52:53], v22 offset:49152\n"
"ds_read_b64_tr_b16 a[54:55], v22 offset:50176\n"
"ds_read_b64_tr_b16 a[60:61], v22 offset:57344\n"
"ds_read_b64_tr_b16 a[62:63], v22 offset:58368\n"
"v_add_u32 v22, 65536, v22\n"
"ds_read_b64_tr_b16 a[68:69], v22 offset:0\n"
"ds_read_b64_tr_b16 a[70:71], v22 offset:1024\n"
"ds_read_b64_tr_b16 a[76:77], v22 offset:8192\n"
"ds_read_b64_tr_b16 a[78:79], v22 offset:9216\n"
"ds_read_b64_tr_b16 a[84:85], v22 offset:16384\n"
"ds_read_b64_tr_b16 a[86:87], v22 offset:17408\n"
"ds_read_b64_tr_b16 a[92:93], v22 offset:24576\n"
"ds_read_b64_tr_b16 a[94:95], v22 offset:25600\n"
"s_waitcnt lgkmcnt(0)\n"
"ds_read_b64_tr_b16 a[100:101], v22 offset:32768\n"
"ds_read_b64_tr_b16 a[102:103], v22 offset:33792\n"
"ds_read_b64_tr_b16 a[108:109], v22 offset:40960\n"
"ds_read_b64_tr_b16 a[110:111], v22 offset:41984\n"
"ds_read_b64_tr_b16 a[116:117], v22 offset:49152\n"
"ds_read_b64_tr_b16 a[118:119], v22 offset:50176\n"
"ds_read_b64_tr_b16 a[124:125], v22 offset:57344\n"
"ds_read_b64_tr_b16 a[126:127], v22 offset:58368\n"
"v_add_u32 v22, 65536, v22\n"
"ds_read_b64_tr_b16 a[132:133], v22 offset:0\n"
"ds_read_b64_tr_b16 a[134:135], v22 offset:1024\n"
"ds_read_b64_tr_b16 a[140:141], v22 offset:8192\n"
"ds_read_b64_tr_b16 a[142:143], v22 offset:9216\n"
"s_waitcnt lgkmcnt(0)\n"
"ds_read_b64_tr_b16 a[148:149], v22 offset:16384\n"
"ds_read_b64_tr_b16 a[150:151], v22 offset:17408\n"
"ds_read_b64_tr_b16 a[156:157], v22 offset:24576\n"
"ds_read_b64_tr_b16 a[158:159], v22 offset:25600\n"
"s_waitcnt lgkmcnt(0)\n"
"s_barrier\n"
"s_mov_b32 s16, s62\n"
"s_and_b32 s17, s63, 0xffff\n"
"s_mov_b32 s18, 2211840\n"
"s_mov_b32 s19, 0x20000\n"
"s_mul_i32 s40, s65, 163840\n"
"s_lshl_b32 s41, s20, 10\n"
"s_add_u32 s40, s40, s41\n"
"s_add_u32 m0, s41, 0\n"
"s_add_u32 s42, s40, 0\n"
"buffer_load_dwordx4 v0, s[16:19], s42 offen lds\n"
"s_add_u32 m0, s41, 4096\n"
"s_add_u32 s42, s40, 4096\n"
"buffer_load_dwordx4 v0, s[16:19], s42 offen lds\n"
"s_add_u32 m0, s41, 8192\n"
"s_add_u32 s42, s40, 8192\n"
"buffer_load_dwordx4 v0, s[16:19], s42 offen lds\n"
"s_add_u32 m0, s41, 12288\n"
"s_add_u32 s42, s40, 12288\n"
"buffer_load_dwordx4 v0, s[16:19], s42 offen lds\n"
"s_add_u32 m0, s41, 16384\n"
"s_add_u32 s42, s40, 16384\n"
"buffer_load_dwordx4 v0, s[16:19], s42 offen lds\n"
"s_add_u32 m0, s41, 20480\n"
"s_add_u32 s42, s40, 20480\n"
"buffer_load_dwordx4 v0, s[16:19], s42 offen lds\n"
"s_add_u32 m0, s41, 24576\n"
"s_add_u32 s42, s40, 24576\n"
"buffer_load_dwordx4 v0, s[16:19], s42 offen lds\n"
"s_add_u32 m0, s41, 28672\n"
"s_add_u32 s42, s40, 28672\n"
"buffer_load_dwordx4 v0, s[16:19], s42 offen lds\n"
"s_add_u32 m0, s41, 32768\n"
"s_add_u32 s42, s40, 32768\n"
"buffer_load_dwordx4 v0, s[16:19], s42 offen lds\n"
"s_add_u32 m0, s41, 36864\n"
"s_add_u32 s42, s40, 36864\n"
"buffer_load_dwordx4 v0, s[16:19], s42 offen lds\n"
"s_add_u32 m0, s41, 40960\n"
"s_add_u32 s42, s40, 40960\n"
"buffer_load_dwordx4 v0, s[16:19], s42 offen lds\n"
"s_add_u32 m0, s41, 45056\n"
"s_add_u32 s42, s40, 45056\n"
"buffer_load_dwordx4 v0, s[16:19], s42 offen lds\n"
"s_add_u32 m0, s41, 49152\n"
"s_add_u32 s42, s40, 49152\n"
"buffer_load_dwordx4 v0, s[16:19], s42 offen lds\n"
"s_add_u32 m0, s41, 53248\n"
"s_add_u32 s42, s40, 53248\n"
"buffer_load_dwordx4 v0, s[16:19], s42 offen lds\n"
"s_add_u32 m0, s41, 57344\n"
"s_add_u32 s42, s40, 57344\n"
"buffer_load_dwordx4 v0, s[16:19], s42 offen lds\n"
"s_add_u32 m0, s41, 61440\n"
"s_add_u32 s42, s40, 61440\n"
"buffer_load_dwordx4 v0, s[16:19], s42 offen lds\n"
"s_add_u32 m0, s41, 65536\n"
"s_add_u32 s42, s40, 65536\n"
"buffer_load_dwordx4 v0, s[16:19], s42 offen lds\n"
"s_add_u32 m0, s41, 69632\n"
"s_add_u32 s42, s40, 69632\n"
"buffer_load_dwordx4 v0, s[16:19], s42 offen lds\n"
"s_add_u32 m0, s41, 73728\n"
"s_add_u32 s42, s40, 73728\n"
"buffer_load_dwordx4 v0, s[16:19], s42 offen lds\n"
"s_add_u32 m0, s41, 77824\n"
"s_add_u32 s42, s40, 77824\n"
"buffer_load_dwordx4 v0, s[16:19], s42 offen lds\n"
"s_add_u32 m0, s41, 81920\n"
"s_add_u32 s42, s40, 81920\n"
"buffer_load_dwordx4 v0, s[16:19], s42 offen lds\n"
"s_add_u32 m0, s41, 86016\n"
"s_add_u32 s42, s40, 86016\n"
"buffer_load_dwordx4 v0, s[16:19], s42 offen lds\n"
"s_add_u32 m0, s41, 90112\n"
"s_add_u32 s42, s40, 90112\n"
"buffer_load_dwordx4 v0, s[16:19], s42 offen lds\n"
"s_add_u32 m0, s41, 94208\n"
"s_add_u32 s42, s40, 94208\n"
"buffer_load_dwordx4 v0, s[16:19], s42 offen lds\n"
"s_add_u32 m0, s41, 98304\n"
"s_add_u32 s42, s40, 98304\n"
"buffer_load_dwordx4 v0, s[16:19], s42 offen lds\n"
"s_add_u32 m0, s41, 102400\n"
"s_add_u32 s42, s40, 102400\n"
"buffer_load_dwordx4 v0, s[16:19], s42 offen lds\n"
"s_add_u32 m0, s41, 106496\n"
"s_add_u32 s42, s40, 106496\n"
"buffer_load_dwordx4 v0, s[16:19], s42 offen lds\n"
"s_add_u32 m0, s41, 110592\n"
"s_add_u32 s42, s40, 110592\n"
"buffer_load_dwordx4 v0, s[16:19], s42 offen lds\n"
"s_add_u32 m0, s41, 114688\n"
"s_add_u32 s42, s40, 114688\n"
"buffer_load_dwordx4 v0, s[16:19], s42 offen lds\n"
"s_add_u32 m0, s41, 118784\n"
"s_add_u32 s42, s40, 118784\n"
"buffer_load_dwordx4 v0, s[16:19], s42 offen lds\n"
"s_add_u32 m0, s41, 122880\n"
"s_add_u32 s42, s40, 122880\n"
"buffer_load_dwordx4 v0, s[16:19], s42 offen lds\n"
"s_add_u32 m0, s41, 126976\n"
"s_add_u32 s42, s40, 126976\n"
"buffer_load_dwordx4 v0, s[16:19], s42 offen lds\n"
"s_add_u32 m0, s41, 131072\n"
"s_add_u32 s42, s40, 131072\n"
"buffer_load_dwordx4 v0, s[16:19], s42 offen lds\n"
"s_add_u32 m0, s41, 135168\n"
"s_add_u32 s42, s40, 135168\n"
"buffer_load_dwordx4 v0, s[16:19], s42 offen lds\n"
"s_add_u32 m0, s41, 139264\n"
"s_add_u32 s42, s40, 139264\n"
"buffer_load_dwordx4 v0, s[16:19], s42 offen lds\n"
"s_add_u32 m0, s41, 143360\n"
"s_add_u32 s42, s40, 143360\n"
"buffer_load_dwordx4 v0, s[16:19], s42 offen lds\n"
"s_add_u32 m0, s41, 147456\n"
"s_add_u32 s42, s40, 147456\n"
"buffer_load_dwordx4 v0, s[16:19], s42 offen lds\n"
"s_add_u32 m0, s41, 151552\n"
"s_add_u32 s42, s40, 151552\n"
"buffer_load_dwordx4 v0, s[16:19], s42 offen lds\n"
"s_add_u32 m0, s41, 155648\n"
"s_add_u32 s42, s40, 155648\n"
"buffer_load_dwordx4 v0, s[16:19], s42 offen lds\n"
"s_add_u32 m0, s41, 159744\n"
"s_add_u32 s42, s40, 159744\n"
"buffer_load_dwordx4 v0, s[16:19], s42 offen lds\n"
"s_waitcnt vmcnt(0)\n"
"s_barrier\n"
"v_lshrrev_b32 v16, 2, v11\n"
"v_lshl_add_u32 v16, v12, 2, v16\n"
"v_mul_u32_u24 v16, 1280, v16\n"
"v_lshl_add_u32 v16, v18, 4, v16\n"
"s_mul_i32 s40, s20, 320\n"
"v_add_u32 v21, s40, v16\n"
"ds_read_b64_tr_b16 a[160:161], v21 offset:0\n"
"ds_read_b64_tr_b16 a[162:163], v21 offset:20480\n"
"ds_read_b64_tr_b16 a[164:165], v21 offset:8\n"
"ds_read_b64_tr_b16 a[166:167], v21 offset:20488\n"
"ds_read_b64_tr_b16 a[168:169], v21 offset:64\n"
"ds_read_b64_tr_b16 a[170:171], v21 offset:20544\n"
"ds_read_b64_tr_b16 a[172:173], v21 offset:72\n"
"ds_read_b64_tr_b16 a[174:175], v21 offset:20552\n"
"ds_read_b64_tr_b16 a[176:177], v21 offset:128\n"
"ds_read_b64_tr_b16 a[178:179], v21 offset:20608\n"
"s_waitcnt lgkmcnt(0)\n"
"ds_read_b64_tr_b16 a[180:181], v21 offset:136\n"
"ds_read_b64_tr_b16 a[182:183], v21 offset:20616\n"
"ds_read_b64_tr_b16 a[184:185], v21 offset:192\n"
"ds_read_b64_tr_b16 a[186:187], v21 offset:20672\n"
"ds_read_b64_tr_b16 a[188:189], v21 offset:200\n"
"ds_read_b64_tr_b16 a[190:191], v21 offset:20680\n"
"ds_read_b64_tr_b16 a[192:193], v21 offset:256\n"
"ds_read_b64_tr_b16 a[194:195], v21 offset:20736\n"
"ds_read_b64_tr_b16 a[196:197], v21 offset:264\n"
"ds_read_b64_tr_b16 a[198:199], v21 offset:20744\n"
"s_waitcnt lgkmcnt(0)\n"
"v_add_u32 v21, 40960, v21\n"
"ds_read_b64_tr_b16 a[200:201], v21 offset:0\n"
"ds_read_b64_tr_b16 a[202:203], v21 offset:20480\n"
"ds_read_b64_tr_b16 a[204:205], v21 offset:8\n"
"ds_read_b64_tr_b16 a[206:207], v21 offset:20488\n"
"ds_read_b64_tr_b16 a[208:209], v21 offset:64\n"
"ds_read_b64_tr_b16 a[210:211], v21 offset:20544\n"
"ds_read_b64_tr_b16 a[212:213], v21 offset:72\n"
"ds_read_b64_tr_b16 a[214:215], v21 offset:20552\n"
"ds_read_b64_tr_b16 a[216:217], v21 offset:128\n"
"ds_read_b64_tr_b16 a[218:219], v21 offset:20608\n"
"s_waitcnt lgkmcnt(0)\n"
"ds_read_b64_tr_b16 a[220:221], v21 offset:136\n"
"ds_read_b64_tr_b16 a[222:223], v21 offset:20616\n"
"ds_read_b64_tr_b16 a[224:225], v21 offset:192\n"
"ds_read_b64_tr_b16 a[226:227], v21 offset:20672\n"
"ds_read_b64_tr_b16 a[228:229], v21 offset:200\n"
"ds_read_b64_tr_b16 a[230:231], v21 offset:20680\n"
"ds_read_b64_tr_b16 a[232:233], v21 offset:256\n"
"ds_read_b64_tr_b16 a[234:235], v21 offset:20736\n"
"ds_read_b64_tr_b16 a[236:237], v21 offset:264\n"
"ds_read_b64_tr_b16 a[238:239], v21 offset:20744\n"
"s_waitcnt lgkmcnt(0)\n"
"v_add_u32 v21, 40960, v21\n"
"ds_read_b64_tr_b16 a[240:241], v21 offset:0\n"
"ds_read_b64_tr_b16 a[242:243], v21 offset:20480\n"
"ds_read_b64_tr_b16 a[244:245], v21 offset:8\n"
"ds_read_b64_tr_b16 a[246:247], v21 offset:20488\n"
"ds_read_b64_tr_b16 a[248:249], v21 offset:64\n"
"ds_read_b64_tr_b16 a[250:251], v21 offset:20544\n"
"ds_read_b64_tr_b16 a[252:253], v21 offset:72\n"
"ds_read_b64_tr_b16 a[254:255], v21 offset:20552\n"
"ds_read_b64_tr_b16 v[192:193], v21 offset:128\n"
"ds_read_b64_tr_b16 v[194:195], v21 offset:20608\n"
"s_waitcnt lgkmcnt(0)\n"
"ds_read_b64_tr_b16 v[196:197], v21 offset:136\n"
"ds_read_b64_tr_b16 v[198:199], v21 offset:20616\n"
"ds_read_b64_tr_b16 v[200:201], v21 offset:192\n"
"ds_read_b64_tr_b16 v[202:203], v21 offset:20672\n"
"ds_read_b64_tr_b16 v[204:205], v21 offset:200\n"
"ds_read_b64_tr_b16 v[206:207], v21 offset:20680\n"
"ds_read_b64_tr_b16 v[208:209], v21 offset:256\n"
"ds_read_b64_tr_b16 v[210:211], v21 offset:20736\n"
"ds_read_b64_tr_b16 v[212:213], v21 offset:264\n"
"ds_read_b64_tr_b16 v[214:215], v21 offset:20744\n"
"s_waitcnt lgkmcnt(0)\n"
"v_add_u32 v21, 40960, v21\n"
"ds_read_b64_tr_b16 v[216:217], v21 offset:0\n"
"ds_read_b64_tr_b16 v[218:219], v21 offset:20480\n"
"ds_read_b64_tr_b16 v[220:221], v21 offset:8\n"
"ds_read_b64_tr_b16 v[222:223], v21 offset:20488\n"
"ds_read_b64_tr_b16 v[224:225], v21 offset:64\n"
"ds_read_b64_tr_b16 v[226:227], v21 offset:20544\n"
"ds_read_b64_tr_b16 v[228:229], v21 offset:72\n"
"ds_read_b64_tr_b16 v[230:231], v21 offset:20552\n"
"ds_read_b64_tr_b16 v[232:233], v21 offset:128\n"
"ds_read_b64_tr_b16 v[234:235], v21 offset:20608\n"
"s_waitcnt lgkmcnt(0)\n"
"ds_read_b64_tr_b16 v[236:237], v21 offset:136\n"
"ds_read_b64_tr_b16 v[238:239], v21 offset:20616\n"
"ds_read_b64_tr_b16 v[240:241], v21 offset:192\n"
"ds_read_b64_tr_b16 v[242:243], v21 offset:20672\n"
"ds_read_b64_tr_b16 v[244:245], v21 offset:200\n"
"ds_read_b64_tr_b16 v[246:247], v21 offset:20680\n"
"ds_read_b64_tr_b16 v[248:249], v21 offset:256\n"
"ds_read_b64_tr_b16 v[250:251], v21 offset:20736\n"
"ds_read_b64_tr_b16 v[252:253], v21 offset:264\n"
"ds_read_b64_tr_b16 v[254:255], v21 offset:20744\n"
"s_waitcnt lgkmcnt(0)\n"
"s_waitcnt lgkmcnt(0)\n"
"s_barrier\n"
"v_lshlrev_b32 v13, 9, v12\n"
"v_lshl_add_u32 v7, v11, 4, v13\n"
"s_lshl_b32 s40, s64, 4\n"
"s_add_u32 s40, s40, 288\n"
"v_add_u32 v13, s40, v11\n"
"v_lshlrev_b32 v1, 7, v13\n"
"s_mov_b32 s38, 0\n"
"s_add_u32 s40, s21, 384\n"
"s_lshl_b32 s40, s40, 7\n"
"s_lshl2_add_u32 s37, s20, s40\n"
"s_mul_i32 s40, s20, 640\n"
"v_lshrrev_b32 v14, 1, v11\n"
"v_add_u32 v14, 0, v14\n"
"v_mul_u32_u24 v15, 86, v14\n"
"v_lshrrev_b32 v15, 8, v15\n"
"v_mul_u32_u24 v16, 8704, v15\n"
"v_mul_u32_u24 v13, 2560, v14\n"
"v_add_u32 v13, v13, v16\n"
"v_and_b32 v14, 1, v11\n"
"v_lshl_add_u32 v13, v14, 4, v13\n"
"v_lshl_add_u32 v13, v12, 5, v13\n"
"v_add_u32 v4, s40, v13\n"
"v_lshrrev_b32 v14, 1, v11\n"
"v_add_u32 v14, 8, v14\n"
"v_mul_u32_u24 v15, 86, v14\n"
"v_lshrrev_b32 v15, 8, v15\n"
"v_mul_u32_u24 v16, 8704, v15\n"
"v_mul_u32_u24 v13, 2560, v14\n"
"v_add_u32 v13, v13, v16\n"
"v_and_b32 v14, 1, v11\n"
"v_lshl_add_u32 v13, v14, 4, v13\n"
"v_lshl_add_u32 v13, v12, 5, v13\n"
"v_add_u32 v160, s40, v13\n"
"s_lshl_b32 s40, s20, 11\n"
"s_add_u32 s40, s40, 81920\n"
"v_add_u32 v5, s40, v0\n"
"v_add_u32 v8, 81920, v0\n"
"s_cmp_eq_u32 s65, 13\n"
"s_cselect_b32 s40, s20, 0\n"
"s_cmp_ge_u32 s40, 2\n"
"s_cselect_b32 s71, 0, -1\n"
"F_pol_%=:\n"
"buffer_load_dword v13, off, s[12:15], 0 sc1\n"
"s_waitcnt vmcnt(0)\n"
"v_readfirstlane_b32 s40, v13\n"
"s_add_u32 s23, s23, 1\n"
"s_cmp_ge_u32 s23, 400000\n"
"s_cselect_b32 s40, 16, s40\n"
"s_cmp_eq_u32 s40, 0\n"
"s_cbranch_scc1 F_pol_%=\n"
"s_bitcmp1_b32 s40, 5\n"
"s_cselect_b32 s74, 8192, 0\n"
"s_xor_b32 s75, s74, 8192\n"
"s_add_u32 s37, s37, s74\n"
"v_add_u32 v1, s74, v1\n"
"s_mul_i32 s66, s65, 98304\n"
"s_add_u32 s66, s66, s74\n"
"s_add_u32 s66, s66, 12582912\n"
"s_lshl_b32 s41, s20, 10\n"
"s_add_u32 s40, s75, s41\n"
"s_add_u32 s69, s40, 12419072\n"
"s_mov_b32 s70, 0xbfb8aa3b\n"
"s_mov_b32 s24, 0\n"
"s_mov_b32 s25, 0\n"
"s_mov_b32 s35, s22\n"
"s_mov_b32 s36, 0\n"
"v_cmp_gt_u32 vcc, 16, v10\n"
"v_mov_b32 v13, s22\n"
"s_nop 0\n"
"v_cndmask_b32 v1, v13, v1, vcc\n"
"s_lshl_b32 s34, s20, 10\n"
"s_mov_b32 s30, s24\n"
"s_add_u32 s24, s24, 1\n"
"s_cmp_lt_u32 s30, 64\n"
"s_cselect_b32 s30, s30, -1\n"
"s_mul_i32 s40, s64, 15\n"
"s_lshl1_add_u32 s40, s30, s40\n"
"s_and_b32 s40, s40, 31\n"
"s_lshl_b32 s40, s40, 22\n"
"s_add_u32 s31, s40, s69\n"
"F_cold_%=:\n"
"s_waitcnt vmcnt(0)\n"
"v_mov_b32 v6, s36\n"
"buffer_store_dword v6, off, s[12:15], s35 sc1\n"
"s_mov_b32 s35, s22\n"
"s_cmp_lt_i32 s30, 0\n"
"s_cbranch_scc1 F_end_%=\n"
"F_spin_%=:\n"
"buffer_load_dword v2, v1, s[12:15], s38 offen sc1\n"
"s_waitcnt vmcnt(0)\n"
"v_cmp_ge_u32 vcc, s30, v2\n"
"s_nop 0\n"
"s_and_b32 s40, vcc_lo, 65535\n"
"s_cbranch_scc0 F_go_%=\n"
"s_sleep 4\n"
"s_add_u32 s23, s23, 1\n"
"s_cmp_lt_u32 s23, 400000\n"
"s_cbranch_scc1 F_spin_%=\n"
"F_go_%=:\n"
"s_add_u32 m0, s34, 0\n"
"s_add_u32 s40, s31, 0\n"
"buffer_load_dwordx4 v0, s[12:15], s40 offen sc1 lds\n"
"s_add_u32 m0, s34, 4096\n"
"s_add_u32 s40, s31, 4096\n"
"buffer_load_dwordx4 v0, s[12:15], s40 offen sc1 lds\n"
"s_add_u32 m0, s34, 8192\n"
"s_add_u32 s40, s31, 16384\n"
"buffer_load_dwordx4 v0, s[12:15], s40 offen sc1 lds\n"
"s_add_u32 m0, s34, 12288\n"
"s_add_u32 s40, s31, 20480\n"
"buffer_load_dwordx4 v0, s[12:15], s40 offen sc1 lds\n"
"s_add_u32 m0, s34, 16384\n"
"s_add_u32 s40, s31, 32768\n"
"buffer_load_dwordx4 v0, s[12:15], s40 offen sc1 lds\n"
"s_add_u32 m0, s34, 20480\n"
"s_add_u32 s40, s31, 36864\n"
"buffer_load_dwordx4 v0, s[12:15], s40 offen sc1 lds\n"
"s_add_u32 m0, s34, 24576\n"
"s_add_u32 s40, s31, 49152\n"
"buffer_load_dwordx4 v0, s[12:15], s40 offen sc1 lds\n"
"s_add_u32 m0, s34, 28672\n"
"s_add_u32 s40, s31, 53248\n"
"buffer_load_dwordx4 v0, s[12:15], s40 offen sc1 lds\n"
"s_add_u32 m0, s34, 32768\n"
"s_add_u32 s40, s31, 65536\n"
"buffer_load_dwordx4 v0, s[12:15], s40 offen sc1 lds\n"
"s_add_u32 m0, s34, 36864\n"
"s_add_u32 s40, s31, 69632\n"
"buffer_load_dwordx4 v0, s[12:15], s40 offen sc1 lds\n"
"buffer_store_dword v6, off, s[12:15], s22\n"
"buffer_store_dword v6, off, s[12:15], s22\n"
"buffer_store_dword v6, off, s[12:15], s22\n"
"buffer_store_dword v6, off, s[12:15], s22\n"
"F_top_%=:\n"
"s_mov_b32 s27, s29\n"
"s_mov_b32 s28, s30\n"
"s_mul_i32 s40, s64, 15\n"
"s_lshl1_add_u32 s40, s28, s40\n"
"s_and_b32 s40, s40, 31\n"
"s_lshl_b32 s40, s40, 22\n"
"s_add_u32 s33, s40, s66\n"
"s_cmp_ge_u32 s34, 40960\n"
"s_cselect_b32 s40, 40960, 0\n"
"v_add_u32 v3, s40, v7\n"
"s_lshl_b32 s40, s40, 1\n"
"s_add_u32 s34, s34, 40960\n"
"s_sub_u32 s34, s34, s40\n"
"s_mov_b32 s30, s24\n"
"s_add_u32 s24, s24, 1\n"
"s_cmp_lt_u32 s30, 64\n"
"s_cselect_b32 s30, s30, -1\n"
"s_mul_i32 s40, s64, 15\n"
"s_lshl1_add_u32 s40, s30, s40\n"
"s_and_b32 s40, s40, 31\n"
"s_lshl_b32 s40, s40, 22\n"
"s_add_u32 s31, s40, s69\n"
"s_waitcnt vmcnt(4)\n"
"s_barrier\n"
"ds_read_b128 v[32:35], v3 offset:0\n"
"ds_read_b128 v[36:39], v3 offset:2048\n"
"ds_read_b128 v[40:43], v3 offset:4096\n"
"ds_read_b128 v[44:47], v3 offset:6144\n"
"ds_read_b128 v[48:51], v3 offset:8192\n"
"ds_read_b128 v[52:55], v3 offset:10240\n"
"ds_read_b128 v[56:59], v3 offset:12288\n"
"s_waitcnt lgkmcnt(6)\n"
"v_mfma_f32_16x16x32_bf16 v[16:19], a[0:3], v[32:35], 0\n"
"v_mfma_f32_16x16x32_bf16 v[20:23], a[4:7], v[32:35], 0\n"
"ds_read_b128 v[60:63], v3 offset:14336\n"
"s_waitcnt lgkmcnt(6)\n"
"v_mfma_f32_16x16x32_bf16 v[16:19], a[8:11], v[36:39], v[16:19]\n"
"v_mfma_f32_16x16x32_bf16 v[20:23], a[12:15], v[36:39], v[20:23]\n"
"ds_read_b128 v[32:35], v3 offset:16384\n"
"s_waitcnt lgkmcnt(6)\n"
"v_mfma_f32_16x16x32_bf16 v[16:19], a[16:19], v[40:43], v[16:19]\n"
"v_mfma_f32_16x16x32_bf16 v[20:23], a[20:23], v[40:43], v[20:23]\n"
"ds_read_b128 v[36:39], v3 offset:18432\n"
"s_waitcnt lgkmcnt(6)\n"
"v_mfma_f32_16x16x32_bf16 v[16:19], a[24:27], v[44:47], v[16:19]\n"
"v_mfma_f32_16x16x32_bf16 v[20:23], a[28:31], v[44:47], v[20:23]\n"
"ds_read_b128 v[40:43], v3 offset:20480\n"
"s_waitcnt lgkmcnt(6)\n"
"v_mfma_f32_16x16x32_bf16 v[16:19], a[32:35], v[48:51], v[16:19]\n"
"v_mfma_f32_16x16x32_bf16 v[20:23], a[36:39], v[48:51], v[20:23]\n"
"ds_read_b128 v[44:47], v3 offset:22528\n"
"s_waitcnt lgkmcnt(6)\n"
"v_mfma_f32_16x16x32_bf16 v[16:19], a[40:43], v[52:55], v[16:19]\n"
"v_mfma_f32_16x16x32_bf16 v[20:23], a[44:47], v[52:55], v[20:23]\n"
"ds_read_b128 v[48:51], v3 offset:24576\n"
"s_waitcnt lgkmcnt(6)\n"
"v_mfma_f32_16x16x32_bf16 v[16:19], a[48:51], v[56:59], v[16:19]\n"
"v_mfma_f32_16x16x32_bf16 v[20:23], a[52:55], v[56:59], v[20:23]\n"
"ds_read_b128 v[52:55], v3 offset:26624\n"
"s_waitcnt lgkmcnt(6)\n"
"v_mfma_f32_16x16x32_bf16 v[16:19], a[56:59], v[60:63], v[16:19]\n"
"v_mfma_f32_16x16x32_bf16 v[20:23], a[60:63], v[60:63], v[20:23]\n"
"ds_read_b128 v[56:59], v3 offset:28672\n"
"s_waitcnt lgkmcnt(6)\n"
"v_mfma_f32_16x16x32_bf16 v[16:19], a[64:67], v[32:35], v[16:19]\n"
"v_mfma_f32_16x16x32_bf16 v[20:23], a[68:71], v[32:35], v[20:23]\n"
"ds_read_b128 v[60:63], v3 offset:30720\n"
"s_waitcnt lgkmcnt(6)\n"
"v_mfma_f32_16x16x32_bf16 v[16:19], a[72:75], v[36:39], v[16:19]\n"
"v_mfma_f32_16x16x32_bf16 v[20:23], a[76:79], v[36:39], v[20:23]\n"
"ds_read_b128 v[32:35], v3 offset:32768\n"
"s_waitcnt lgkmcnt(6)\n"
"v_mfma_f32_16x16x32_bf16 v[16:19], a[80:83], v[40:43], v[16:19]\n"
"v_mfma_f32_16x16x32_bf16 v[20:23], a[84:87], v[40:43], v[20:23]\n"
"ds_read_b128 v[36:39], v3 offset:34816\n"
"s_waitcnt lgkmcnt(6)\n"
"v_mfma_f32_16x16x32_bf16 v[16:19], a[88:91], v[44:47], v[16:19]\n"
"v_mfma_f32_16x16x32_bf16 v[20:23], a[92:95], v[44:47], v[20:23]\n"
"ds_read_b128 v[40:43], v3 offset:36864\n"
"s_waitcnt lgkmcnt(6)\n"
"v_mfma_f32_16x16x32_bf16 v[16:19], a[96:99], v[48:51], v[16:19]\n"
"v_mfma_f32_16x16x32_bf16 v[20:23], a[100:103], v[48:51], v[20:23]\n"
"ds_read_b128 v[44:47], v3 offset:38912\n"
"s_waitcnt lgkmcnt(6)\n"
"v_mfma_f32_16x16x32_bf16 v[16:19], a[104:107], v[52:55], v[16:19]\n"
"v_mfma_f32_16x16x32_bf16 v[20:23], a[108:111], v[52:55], v[20:23]\n"
"ds_read_b128 v[48:51], v3 offset:256\n"
"s_waitcnt lgkmcnt(6)\n"
"v_mfma_f32_16x16x32_bf16 v[16:19], a[112:115], v[56:59], v[16:19]\n"
"v_mfma_f32_16x16x32_bf16 v[20:23], a[116:119], v[56:59], v[20:23]\n"
"ds_read_b128 v[52:55], v3 offset:2304\n"
"s_waitcnt lgkmcnt(6)\n"
"v_mfma_f32_16x16x32_bf16 v[16:19], a[120:123], v[60:63], v[16:19]\n"
"v_mfma_f32_16x16x32_bf16 v[20:23], a[124:127], v[60:63], v[20:23]\n"
"s_waitcnt vmcnt(0)\n"
"v_mov_b32 v6, s36\n"
"buffer_store_dword v6, off, s[12:15], s35 sc1\n"
"ds_read_b128 v[56:59], v3 offset:4352\n"
"s_waitcnt lgkmcnt(6)\n"
"v_mfma_f32_16x16x32_bf16 v[16:19], a[128:131], v[32:35], v[16:19]\n"
"v_mfma_f32_16x16x32_bf16 v[20:23], a[132:135], v[32:35], v[20:23]\n"
"ds_read_b128 v[60:63], v3 offset:6400\n"
"s_waitcnt lgkmcnt(6)\n"
"v_mfma_f32_16x16x32_bf16 v[16:19], a[136:139], v[36:39], v[16:19]\n"
"v_mfma_f32_16x16x32_bf16 v[20:23], a[140:143], v[36:39], v[20:23]\n"
"ds_read_b128 v[32:35], v3 offset:8448\n"
"s_waitcnt lgkmcnt(6)\n"
"v_mfma_f32_16x16x32_bf16 v[16:19], a[144:147], v[40:43], v[16:19]\n"
"v_mfma_f32_16x16x32_bf16 v[20:23], a[148:151], v[40:43], v[20:23]\n"
"ds_read_b128 v[36:39], v3 offset:10496\n"
"s_waitcnt lgkmcnt(6)\n"
"v_mfma_f32_16x16x32_bf16 v[16:19], a[152:155], v[44:47], v[16:19]\n"
"v_mfma_f32_16x16x32_bf16 v[20:23], a[156:159], v[44:47], v[20:23]\n"
"ds_read_b128 v[40:43], v3 offset:12544\n"
"s_waitcnt lgkmcnt(6)\n"
"v_mfma_f32_16x16x32_bf16 v[24:27], a[0:3], v[48:51], 0\n"
"v_mfma_f32_16x16x32_bf16 v[28:31], a[4:7], v[48:51], 0\n"
"ds_read_b128 v[44:47], v3 offset:14592\n"
"s_waitcnt lgkmcnt(6)\n"
"v_mfma_f32_16x16x32_bf16 v[24:27], a[8:11], v[52:55], v[24:27]\n"
"v_mfma_f32_16x16x32_bf16 v[28:31], a[12:15], v[52:55], v[28:31]\n"
"ds_read_b128 v[48:51], v3 offset:16640\n"
"s_waitcnt lgkmcnt(6)\n"
"v_mfma_f32_16x16x32_bf16 v[24:27], a[16:19], v[56:59], v[24:27]\n"
"v_mfma_f32_16x16x32_bf16 v[28:31], a[20:23], v[56:59], v[28:31]\n"
"ds_read_b128 v[52:55], v3 offset:18688\n"
"s_waitcnt lgkmcnt(6)\n"
"v_mfma_f32_16x16x32_bf16 v[24:27], a[24:27], v[60:63], v[24:27]\n"
"v_mul_f32 v64, s70, v16\n"
"v_mul_f32 v65, s70, v17\n"
"v_mfma_f32_16x16x32_bf16 v[28:31], a[28:31], v[60:63], v[28:31]\n"
"v_mul_f32 v66, s70, v18\n"
"ds_read_b128 v[56:59], v3 offset:20736\n"
"s_waitcnt lgkmcnt(6)\n"
"v_mfma_f32_16x16x32_bf16 v[24:27], a[32:35], v[32:35], v[24:27]\n"
"v_mul_f32 v67, s70, v19\n"
"v_mul_f32 v68, s70, v20\n"
"v_mfma_f32_16x16x32_bf16 v[28:31], a[36:39], v[32:35], v[28:31]\n"
"v_mul_f32 v69, s70, v21\n"
"ds_read_b128 v[60:63], v3 offset:22784\n"
"s_waitcnt lgkmcnt(6)\n"
"v_mfma_f32_16x16x32_bf16 v[24:27], a[40:43], v[36:39], v[24:27]\n"
"v_mul_f32 v70, s70, v22\n"
"v_mul_f32 v71, s70, v23\n"
"v_mfma_f32_16x16x32_bf16 v[28:31], a[44:47], v[36:39], v[28:31]\n"
"v_exp_f32 v64, v64\n"
"ds_read_b128 v[32:35], v3 offset:24832\n"
"s_waitcnt lgkmcnt(6)\n"
"v_mfma_f32_16x16x32_bf16 v[24:27], a[48:51], v[40:43], v[24:27]\n"
"v_exp_f32 v65, v65\n"
"v_exp_f32 v66, v66\n"
"v_mfma_f32_16x16x32_bf16 v[28:31], a[52:55], v[40:43], v[28:31]\n"
"v_exp_f32 v67, v67\n"
"ds_read_b128 v[36:39], v3 offset:26880\n"
"s_waitcnt lgkmcnt(6)\n"
"v_mfma_f32_16x16x32_bf16 v[24:27], a[56:59], v[44:47], v[24:27]\n"
"v_exp_f32 v68, v68\n"
"v_exp_f32 v69, v69\n"
"v_mfma_f32_16x16x32_bf16 v[28:31], a[60:63], v[44:47], v[28:31]\n"
"v_exp_f32 v70, v70\n"
"ds_read_b128 v[40:43], v3 offset:28928\n"
"s_waitcnt lgkmcnt(6)\n"
"v_mfma_f32_16x16x32_bf16 v[24:27], a[64:67], v[48:51], v[24:27]\n"
"v_exp_f32 v71, v71\n"
"v_add_f32 v64, 1.0, v64\n"
"v_mfma_f32_16x16x32_bf16 v[28:31], a[68:71], v[48:51], v[28:31]\n"
"v_add_f32 v65, 1.0, v65\n"
"ds_read_b128 v[44:47], v3 offset:30976\n"
"s_waitcnt lgkmcnt(6)\n"
"v_mfma_f32_16x16x32_bf16 v[24:27], a[72:75], v[52:55], v[24:27]\n"
"v_add_f32 v66, 1.0, v66\n"
"v_add_f32 v67, 1.0, v67\n"
"v_mfma_f32_16x16x32_bf16 v[28:31], a[76:79], v[52:55], v[28:31]\n"
"v_add_f32 v68, 1.0, v68\n"
"ds_read_b128 v[48:51], v3 offset:33024\n"
"s_waitcnt lgkmcnt(6)\n"
"v_mfma_f32_16x16x32_bf16 v[24:27], a[80:83], v[56:59], v[24:27]\n"
"v_add_f32 v69, 1.0, v69\n"
"v_add_f32 v70, 1.0, v70\n"
"v_mfma_f32_16x16x32_bf16 v[28:31], a[84:87], v[56:59], v[28:31]\n"
"v_add_f32 v71, 1.0, v71\n"
"ds_read_b128 v[52:55], v3 offset:35072\n"
"s_waitcnt lgkmcnt(6)\n"
"v_mfma_f32_16x16x32_bf16 v[24:27], a[88:91], v[60:63], v[24:27]\n"
"v_rcp_f32 v64, v64\n"
"v_rcp_f32 v65, v65\n"
"v_mfma_f32_16x16x32_bf16 v[28:31], a[92:95], v[60:63], v[28:31]\n"
"v_rcp_f32 v66, v66\n"
"ds_read_b128 v[56:59], v3 offset:37120\n"
"s_waitcnt lgkmcnt(6)\n"
"v_mfma_f32_16x16x32_bf16 v[24:27], a[96:99], v[32:35], v[24:27]\n"
"v_rcp_f32 v67, v67\n"
"v_rcp_f32 v68, v68\n"
"v_mfma_f32_16x16x32_bf16 v[28:31], a[100:103], v[32:35], v[28:31]\n"
"v_rcp_f32 v69, v69\n"
"ds_read_b128 v[60:63], v3 offset:39168\n"
"s_waitcnt lgkmcnt(6)\n"
"v_mfma_f32_16x16x32_bf16 v[24:27], a[104:107], v[36:39], v[24:27]\n"
"v_rcp_f32 v70, v70\n"
"v_rcp_f32 v71, v71\n"
"v_mfma_f32_16x16x32_bf16 v[28:31], a[108:111], v[36:39], v[28:31]\n"
"v_pk_mul_f32 v[64:65], v[64:65], v[16:17]\n"
"s_waitcnt lgkmcnt(5)\n"
"v_mfma_f32_16x16x32_bf16 v[24:27], a[112:115], v[40:43], v[24:27]\n"
"v_pk_mul_f32 v[66:67], v[66:67], v[18:19]\n"
"v_pk_mul_f32 v[68:69], v[68:69], v[20:21]\n"
"v_mfma_f32_16x16x32_bf16 v[28:31], a[116:119], v[40:43], v[28:31]\n"
"v_pk_mul_f32 v[70:71], v[70:71], v[22:23]\n"
"s_waitcnt lgkmcnt(4)\n"
"v_mfma_f32_16x16x32_bf16 v[24:27], a[120:123], v[44:47], v[24:27]\n"
"v_cvt_pk_bf16_f32 v144, v64, v65\n"
"v_cvt_pk_bf16_f32 v145, v66, v67\n"
"v_mfma_f32_16x16x32_bf16 v[28:31], a[124:127], v[44:47], v[28:31]\n"
"v_cvt_pk_bf16_f32 v146, v68, v69\n"
"s_waitcnt lgkmcnt(3)\n"
"v_mfma_f32_16x16x32_bf16 v[24:27], a[128:131], v[48:51], v[24:27]\n"
"v_cvt_pk_bf16_f32 v147, v70, v71\n"
"v_and_b32 v144, s71, v144\n"
"v_mfma_f32_16x16x32_bf16 v[28:31], a[132:135], v[48:51], v[28:31]\n"
"v_and_b32 v145, s71, v145\n"
"s_waitcnt lgkmcnt(2)\n"
"v_mfma_f32_16x16x32_bf16 v[24:27], a[136:139], v[52:55], v[24:27]\n"
"v_and_b32 v146, s71, v146\n"
"v_and_b32 v147, s71, v147\n"
"v_mfma_f32_16x16x32_bf16 v[28:31], a[140:143], v[52:55], v[28:31]\n"
"s_waitcnt lgkmcnt(1)\n"
"v_mfma_f32_16x16x32_bf16 v[24:27], a[144:147], v[56:59], v[24:27]\n"
"v_mfma_f32_16x16x32_bf16 v[28:31], a[148:151], v[56:59], v[28:31]\n"
"s_waitcnt lgkmcnt(0)\n"
"v_mfma_f32_16x16x32_bf16 v[24:27], a[152:155], v[60:63], v[24:27]\n"
"v_mfma_f32_16x16x32_bf16 v[28:31], a[156:159], v[60:63], v[28:31]\n"
"ds_write_b128 v5, v[144:147] offset:0\n"
"buffer_load_dword v2, v1, s[12:15], s38 offen sc1\n"
"s_nop 4\n"
"v_mul_f32 v72, s70, v24\n"
"v_mul_f32 v73, s70, v25\n"
"v_mul_f32 v74, s70, v26\n"
"v_mul_f32 v75, s70, v27\n"
"v_mul_f32 v76, s70, v28\n"
"v_mul_f32 v77, s70, v29\n"
"v_mul_f32 v78, s70, v30\n"
"v_mul_f32 v79, s70, v31\n"
"v_exp_f32 v72, v72\n"
"v_exp_f32 v73, v73\n"
"v_exp_f32 v74, v74\n"
"v_exp_f32 v75, v75\n"
"v_exp_f32 v76, v76\n"
"v_exp_f32 v77, v77\n"
"v_exp_f32 v78, v78\n"
"v_exp_f32 v79, v79\n"
"v_add_f32 v72, 1.0, v72\n"
"v_add_f32 v73, 1.0, v73\n"
"v_add_f32 v74, 1.0, v74\n"
"v_add_f32 v75, 1.0, v75\n"
"v_add_f32 v76, 1.0, v76\n"
"v_add_f32 v77, 1.0, v77\n"
"v_add_f32 v78, 1.0, v78\n"
"v_add_f32 v79, 1.0, v79\n"
"v_rcp_f32 v72, v72\n"
"v_rcp_f32 v73, v73\n"
"v_rcp_f32 v74, v74\n"
"v_rcp_f32 v75, v75\n"
"v_rcp_f32 v76, v76\n"
"v_rcp_f32 v77, v77\n"
"v_rcp_f32 v78, v78\n"
"v_rcp_f32 v79, v79\n"
"v_pk_mul_f32 v[72:73], v[72:73], v[24:25]\n"
"v_pk_mul_f32 v[74:75], v[74:75], v[26:27]\n"
"v_pk_mul_f32 v[76:77], v[76:77], v[28:29]\n"
"v_pk_mul_f32 v[78:79], v[78:79], v[30:31]\n"
"v_cvt_pk_bf16_f32 v148, v72, v73\n"
"v_cvt_pk_bf16_f32 v149, v74, v75\n"
"v_cvt_pk_bf16_f32 v150, v76, v77\n"
"v_cvt_pk_bf16_f32 v151, v78, v79\n"
"v_and_b32 v148, s71, v148\n"
"v_and_b32 v149, s71, v149\n"
"v_and_b32 v150, s71, v150\n"
"v_and_b32 v151, s71, v151\n"
"ds_write_b128 v5, v[148:151] offset:1024\n"
"s_waitcnt lgkmcnt(0)\n"
"s_barrier\n"
"ds_read_b128 v[80:83], v8 offset:0\n"
"ds_read_b128 v[84:87], v8 offset:2048\n"
"ds_read_b128 v[88:91], v8 offset:4096\n"
"ds_read_b128 v[92:95], v8 offset:6144\n"
"ds_read_b128 v[96:99], v8 offset:1024\n"
"ds_read_b128 v[100:103], v8 offset:3072\n"
"ds_read_b128 v[104:107], v8 offset:5120\n"
"ds_read_b128 v[108:111], v8 offset:7168\n"
"s_waitcnt lgkmcnt(7)\n"
"v_mfma_f32_16x16x32_bf16 v[112:115], a[160:163], v[80:83], 0\n"
"v_mfma_f32_16x16x32_bf16 v[116:119], a[164:167], v[80:83], 0\n"
"s_waitcnt lgkmcnt(6)\n"
"v_mfma_f32_16x16x32_bf16 v[112:115], a[200:203], v[84:87], v[112:115]\n"
"v_mfma_f32_16x16x32_bf16 v[116:119], a[204:207], v[84:87], v[116:119]\n"
"s_waitcnt lgkmcnt(5)\n"
"v_mfma_f32_16x16x32_bf16 v[112:115], a[240:243], v[88:91], v[112:115]\n"
"v_mfma_f32_16x16x32_bf16 v[116:119], a[244:247], v[88:91], v[116:119]\n"
"s_waitcnt lgkmcnt(4)\n"
"v_mfma_f32_16x16x32_bf16 v[112:115], v[216:219], v[92:95], v[112:115]\n"
"v_mfma_f32_16x16x32_bf16 v[116:119], v[220:223], v[92:95], v[116:119]\n"
"v_mfma_f32_16x16x32_bf16 v[120:123], a[168:171], v[80:83], 0\n"
"v_mfma_f32_16x16x32_bf16 v[124:127], a[172:175], v[80:83], 0\n"
"v_mfma_f32_16x16x32_bf16 v[120:123], a[208:211], v[84:87], v[120:123]\n"
"v_mfma_f32_16x16x32_bf16 v[124:127], a[212:215], v[84:87], v[124:127]\n"
"v_mfma_f32_16x16x32_bf16 v[120:123], a[248:251], v[88:91], v[120:123]\n"
"v_mfma_f32_16x16x32_bf16 v[124:127], a[252:255], v[88:91], v[124:127]\n"
"s_nop 0\n"
"v_cvt_pk_bf16_f32 v144, v112, v113\n"
"v_cvt_pk_bf16_f32 v145, v114, v115\n"
"v_cvt_pk_bf16_f32 v146, v116, v117\n"
"v_cvt_pk_bf16_f32 v147, v118, v119\n"
"buffer_store_dwordx4 v[144:147], v4, s[12:15], s33 offen offset:0 sc1\n"
"v_mfma_f32_16x16x32_bf16 v[120:123], v[224:227], v[92:95], v[120:123]\n"
"v_mfma_f32_16x16x32_bf16 v[124:127], v[228:231], v[92:95], v[124:127]\n"
"s_waitcnt vmcnt(1)\n"
"v_cmp_ge_u32 vcc, s30, v2\n"
"s_nop 0\n"
"s_and_b32 s40, vcc_lo, 65535\n"
"s_cselect_b32 s26, 0, 1\n"
"s_cselect_b32 s39, s22, s31\n"
"v_mfma_f32_16x16x32_bf16 v[128:131], a[176:179], v[80:83], 0\n"
"s_add_u32 m0, s34, 0\n"
"s_add_u32 s40, s39, 0\n"
"v_mfma_f32_16x16x32_bf16 v[132:135], a[180:183], v[80:83], 0\n"
"buffer_load_dwordx4 v0, s[12:15], s40 offen sc1 lds\n"
"v_mfma_f32_16x16x32_bf16 v[128:131], a[216:219], v[84:87], v[128:131]\n"
"v_mfma_f32_16x16x32_bf16 v[132:135], a[220:223], v[84:87], v[132:135]\n"
"v_mfma_f32_16x16x32_bf16 v[128:131], v[192:195], v[88:91], v[128:131]\n"
"v_mfma_f32_16x16x32_bf16 v[132:135], v[196:199], v[88:91], v[132:135]\n"
"v_cvt_pk_bf16_f32 v148, v120, v121\n"
"v_cvt_pk_bf16_f32 v149, v122, v123\n"
"v_cvt_pk_bf16_f32 v150, v124, v125\n"
"v_cvt_pk_bf16_f32 v151, v126, v127\n"
"buffer_store_dwordx4 v[148:151], v4, s[12:15], s33 offen offset:128 sc1\n"
"v_mfma_f32_16x16x32_bf16 v[128:131], v[232:235], v[92:95], v[128:131]\n"
"s_add_u32 m0, s34, 4096\n"
"s_add_u32 s40, s39, 4096\n"
"v_mfma_f32_16x16x32_bf16 v[132:135], v[236:239], v[92:95], v[132:135]\n"
"buffer_load_dwordx4 v0, s[12:15], s40 offen sc1 lds\n"
"v_mfma_f32_16x16x32_bf16 v[112:115], a[184:187], v[80:83], 0\n"
"s_add_u32 m0, s34, 8192\n"
"s_add_u32 s40, s39, 16384\n"
"v_mfma_f32_16x16x32_bf16 v[116:119], a[188:191], v[80:83], 0\n"
"buffer_load_dwordx4 v0, s[12:15], s40 offen sc1 lds\n"
"v_mfma_f32_16x16x32_bf16 v[112:115], a[224:227], v[84:87], v[112:115]\n"
"v_mfma_f32_16x16x32_bf16 v[116:119], a[228:231], v[84:87], v[116:119]\n"
"v_mfma_f32_16x16x32_bf16 v[112:115], v[200:203], v[88:91], v[112:115]\n"
"v_mfma_f32_16x16x32_bf16 v[116:119], v[204:207], v[88:91], v[116:119]\n"
"v_cvt_pk_bf16_f32 v152, v128, v129\n"
"v_cvt_pk_bf16_f32 v153, v130, v131\n"
"v_cvt_pk_bf16_f32 v154, v132, v133\n"
"v_cvt_pk_bf16_f32 v155, v134, v135\n"
"buffer_store_dwordx4 v[152:155], v4, s[12:15], s33 offen offset:256 sc1\n"
"v_mfma_f32_16x16x32_bf16 v[112:115], v[240:243], v[92:95], v[112:115]\n"
"s_add_u32 m0, s34, 12288\n"
"s_add_u32 s40, s39, 20480\n"
"v_mfma_f32_16x16x32_bf16 v[116:119], v[244:247], v[92:95], v[116:119]\n"
"buffer_load_dwordx4 v0, s[12:15], s40 offen sc1 lds\n"
"v_mfma_f32_16x16x32_bf16 v[120:123], a[192:195], v[80:83], 0\n"
"s_add_u32 m0, s34, 16384\n"
"s_add_u32 s40, s39, 32768\n"
"v_mfma_f32_16x16x32_bf16 v[124:127], a[196:199], v[80:83], 0\n"
"buffer_load_dwordx4 v0, s[12:15], s40 offen sc1 lds\n"
"v_mfma_f32_16x16x32_bf16 v[120:123], a[232:235], v[84:87], v[120:123]\n"
"v_mfma_f32_16x16x32_bf16 v[124:127], a[236:239], v[84:87], v[124:127]\n"
"v_mfma_f32_16x16x32_bf16 v[120:123], v[208:211], v[88:91], v[120:123]\n"
"v_mfma_f32_16x16x32_bf16 v[124:127], v[212:215], v[88:91], v[124:127]\n"
"v_cvt_pk_bf16_f32 v144, v112, v113\n"
"v_cvt_pk_bf16_f32 v145, v114, v115\n"
"v_cvt_pk_bf16_f32 v146, v116, v117\n"
"v_cvt_pk_bf16_f32 v147, v118, v119\n"
"buffer_store_dwordx4 v[144:147], v4, s[12:15], s33 offen offset:384 sc1\n"
"v_mfma_f32_16x16x32_bf16 v[120:123], v[248:251], v[92:95], v[120:123]\n"
"s_add_u32 m0, s34, 20480\n"
"s_add_u32 s40, s39, 36864\n"
"v_mfma_f32_16x16x32_bf16 v[124:127], v[252:255], v[92:95], v[124:127]\n"
"buffer_load_dwordx4 v0, s[12:15], s40 offen sc1 lds\n"
"s_waitcnt lgkmcnt(3)\n"
"v_mfma_f32_16x16x32_bf16 v[128:131], a[160:163], v[96:99], 0\n"
"s_add_u32 m0, s34, 24576\n"
"s_add_u32 s40, s39, 49152\n"
"v_mfma_f32_16x16x32_bf16 v[132:135], a[164:167], v[96:99], 0\n"
"buffer_load_dwordx4 v0, s[12:15], s40 offen sc1 lds\n"
"s_waitcnt lgkmcnt(2)\n"
"v_mfma_f32_16x16x32_bf16 v[128:131], a[200:203], v[100:103], v[128:131]\n"
"v_mfma_f32_16x16x32_bf16 v[132:135], a[204:207], v[100:103], v[132:135]\n"
"s_waitcnt lgkmcnt(1)\n"
"v_mfma_f32_16x16x32_bf16 v[128:131], a[240:243], v[104:107], v[128:131]\n"
"v_mfma_f32_16x16x32_bf16 v[132:135], a[244:247], v[104:107], v[132:135]\n"
"v_cvt_pk_bf16_f32 v148, v120, v121\n"
"v_cvt_pk_bf16_f32 v149, v122, v123\n"
"v_cvt_pk_bf16_f32 v150, v124, v125\n"
"v_cvt_pk_bf16_f32 v151, v126, v127\n"
"buffer_store_dwordx4 v[148:151], v4, s[12:15], s33 offen offset:512 sc1\n"
"s_waitcnt lgkmcnt(0)\n"
"v_mfma_f32_16x16x32_bf16 v[128:131], v[216:219], v[108:111], v[128:131]\n"
"s_add_u32 m0, s34, 28672\n"
"s_add_u32 s40, s39, 53248\n"
"v_mfma_f32_16x16x32_bf16 v[132:135], v[220:223], v[108:111], v[132:135]\n"
"buffer_load_dwordx4 v0, s[12:15], s40 offen sc1 lds\n"
"v_mfma_f32_16x16x32_bf16 v[112:115], a[168:171], v[96:99], 0\n"
"s_add_u32 m0, s34, 32768\n"
"s_add_u32 s40, s39, 65536\n"
"v_mfma_f32_16x16x32_bf16 v[116:119], a[172:175], v[96:99], 0\n"
"buffer_load_dwordx4 v0, s[12:15], s40 offen sc1 lds\n"
"v_mfma_f32_16x16x32_bf16 v[112:115], a[208:211], v[100:103], v[112:115]\n"
"v_mfma_f32_16x16x32_bf16 v[116:119], a[212:215], v[100:103], v[116:119]\n"
"v_mfma_f32_16x16x32_bf16 v[112:115], a[248:251], v[104:107], v[112:115]\n"
"v_mfma_f32_16x16x32_bf16 v[116:119], a[252:255], v[104:107], v[116:119]\n"
"v_cvt_pk_bf16_f32 v152, v128, v129\n"
"v_cvt_pk_bf16_f32 v153, v130, v131\n"
"v_cvt_pk_bf16_f32 v154, v132, v133\n"
"v_cvt_pk_bf16_f32 v155, v134, v135\n"
"buffer_store_dwordx4 v[152:155], v160, s[12:15], s33 offen offset:0 sc1\n"
"v_mfma_f32_16x16x32_bf16 v[112:115], v[224:227], v[108:111], v[112:115]\n"
"s_add_u32 m0, s34, 36864\n"
"s_add_u32 s40, s39, 69632\n"
"v_mfma_f32_16x16x32_bf16 v[116:119], v[228:231], v[108:111], v[116:119]\n"
"buffer_load_dwordx4 v0, s[12:15], s40 offen sc1 lds\n"
"v_mfma_f32_16x16x32_bf16 v[120:123], a[176:179], v[96:99], 0\n"
"v_mfma_f32_16x16x32_bf16 v[124:127], a[180:183], v[96:99], 0\n"
"v_mfma_f32_16x16x32_bf16 v[120:123], a[216:219], v[100:103], v[120:123]\n"
"v_mfma_f32_16x16x32_bf16 v[124:127], a[220:223], v[100:103], v[124:127]\n"
"v_mfma_f32_16x16x32_bf16 v[120:123], v[192:195], v[104:107], v[120:123]\n"
"v_mfma_f32_16x16x32_bf16 v[124:127], v[196:199], v[104:107], v[124:127]\n"
"v_cvt_pk_bf16_f32 v144, v112, v113\n"
"v_cvt_pk_bf16_f32 v145, v114, v115\n"
"v_cvt_pk_bf16_f32 v146, v116, v117\n"
"v_cvt_pk_bf16_f32 v147, v118, v119\n"
"buffer_store_dwordx4 v[144:147], v160, s[12:15], s33 offen offset:128 sc1\n"
"v_mfma_f32_16x16x32_bf16 v[120:123], v[232:235], v[108:111], v[120:123]\n"
"v_mfma_f32_16x16x32_bf16 v[124:127], v[236:239], v[108:111], v[124:127]\n"
"v_mfma_f32_16x16x32_bf16 v[128:131], a[184:187], v[96:99], 0\n"
"v_mfma_f32_16x16x32_bf16 v[132:135], a[188:191], v[96:99], 0\n"
"v_mfma_f32_16x16x32_bf16 v[128:131], a[224:227], v[100:103], v[128:131]\n"
"v_mfma_f32_16x16x32_bf16 v[132:135], a[228:231], v[100:103], v[132:135]\n"
"v_mfma_f32_16x16x32_bf16 v[128:131], v[200:203], v[104:107], v[128:131]\n"
"v_mfma_f32_16x16x32_bf16 v[132:135], v[204:207], v[104:107], v[132:135]\n"
"s_nop 0\n"
"v_cvt_pk_bf16_f32 v148, v120, v121\n"
"v_cvt_pk_bf16_f32 v149, v122, v123\n"
"v_cvt_pk_bf16_f32 v150, v124, v125\n"
"v_cvt_pk_bf16_f32 v151, v126, v127\n"
"buffer_store_dwordx4 v[148:151], v160, s[12:15], s33 offen offset:256 sc1\n"
"v_mfma_f32_16x16x32_bf16 v[128:131], v[240:243], v[108:111], v[128:131]\n"
"v_mfma_f32_16x16x32_bf16 v[132:135], v[244:247], v[108:111], v[132:135]\n"
"v_mfma_f32_16x16x32_bf16 v[112:115], a[192:195], v[96:99], 0\n"
"v_mfma_f32_16x16x32_bf16 v[116:119], a[196:199], v[96:99], 0\n"
"v_mfma_f32_16x16x32_bf16 v[112:115], a[232:235], v[100:103], v[112:115]\n"
"v_mfma_f32_16x16x32_bf16 v[116:119], a[236:239], v[100:103], v[116:119]\n"
"v_mfma_f32_16x16x32_bf16 v[112:115], v[208:211], v[104:107], v[112:115]\n"
"v_mfma_f32_16x16x32_bf16 v[116:119], v[212:215], v[104:107], v[116:119]\n"
"s_nop 0\n"
"v_cvt_pk_bf16_f32 v152, v128, v129\n"
"v_cvt_pk_bf16_f32 v153, v130, v131\n"
"v_cvt_pk_bf16_f32 v154, v132, v133\n"
"v_cvt_pk_bf16_f32 v155, v134, v135\n"
"buffer_store_dwordx4 v[152:155], v160, s[12:15], s33 offen offset:384 sc1\n"
"v_mfma_f32_16x16x32_bf16 v[112:115], v[248:251], v[108:111], v[112:115]\n"
"v_mfma_f32_16x16x32_bf16 v[116:119], v[252:255], v[108:111], v[116:119]\n"
"s_nop 6\n"
"v_cvt_pk_bf16_f32 v144, v112, v113\n"
"v_cvt_pk_bf16_f32 v145, v114, v115\n"
"v_cvt_pk_bf16_f32 v146, v116, v117\n"
"v_cvt_pk_bf16_f32 v147, v118, v119\n"
"buffer_store_dwordx4 v[144:147], v160, s[12:15], s33 offen offset:512 sc1\n"
"s_mov_b32 s40, 0\n"
"s_add_u32 s35, s37, s40\n"
"s_add_u32 s36, s28, 1\n"
"s_cmp_lg_u32 s26, 0\n"
"s_cbranch_scc1 F_top_%=\n"
"s_branch F_cold_%=\n"
"F_end_%=:\n"
"s_waitcnt vmcnt(0)\n"
"s_endpgm\n"
"ROLE4_%=:\n"
"s_load_dwordx8 s[52:59], %0, 16\n"
"s_load_dwordx4 s[60:63], %0, 48\n"
"s_load_dwordx2 s[12:13], %0, 72\n"
"s_mov_b32 s21, %1\n"
"s_mov_b32 s20, %2\n"
"s_mov_b32 s22, 0x40000000\n"
"s_mov_b32 s23, 0\n"
"v_mbcnt_lo_u32_b32 v10, -1, 0\n"
"v_mbcnt_hi_u32_b32 v10, -1, v10\n"
"v_lshlrev_b32 v0, 4, v10\n"
"v_and_b32 v11, 15, v10\n"
"v_lshrrev_b32 v12, 4, v10\n"
"s_waitcnt lgkmcnt(0)\n"
"s_and_b32 s13, s13, 0xffff\n"
"s_mov_b32 s14, 149946368\n"
"s_mov_b32 s15, 0x20000\n"
"s_lshr_b32 s25, s20, 1\n"
"s_and_b32 s40, s20, 1\n"
"s_lshl1_add_u32 s65, s21, s40\n"
"v_mul_u32_u24 v13, v10, v10\n"
"v_lshlrev_b32 v13, 12, v13\n"
"s_add_u32 s40, s65, 128\n"
"s_lshl_b32 s40, s40, 7\n"
"s_lshl4_add_u32 s40, s25, s40\n"
"v_add_u32 v1, s40, v13\n"
"v_cmp_gt_u32 vcc, 3, v10\n"
"v_mov_b32 v13, s22\n"
"s_nop 0\n"
"v_cndmask_b32 v1, v13, v1, vcc\n"
"v_lshlrev_b32 v13, 7, v10\n"
"s_lshl_b32 s40, s65, 1\n"
"s_add_u32 s40, s40, 384\n"
"s_lshl_b32 s40, s40, 7\n"
"s_lshl2_add_u32 s40, s25, s40\n"
"v_add_u32 v2, s40, v13\n"
"v_cmp_gt_u32 vcc, 2, v10\n"
"v_mov_b32 v13, s22\n"
"s_nop 0\n"
"v_cndmask_b32 v2, v13, v2, vcc\n"
"s_lshl_b32 s40, s25, 5\n"
"s_add_u32 s64, s40, s65\n"
"s_add_u32 s40, s64, 128\n"
"s_lshl_b32 s27, s40, 7\n"
"v_lshl_add_u32 v13, v12, 2, 0\n"
"v_cmp_gt_u32 s[76:77], v13, v11\n"
"v_lshl_add_u32 v13, v12, 2, 1\n"
"v_cmp_gt_u32 s[78:79], v13, v11\n"
"v_lshl_add_u32 v13, v12, 2, 2\n"
"v_cmp_gt_u32 s[80:81], v13, v11\n"
"v_lshl_add_u32 v13, v12, 2, 3\n"
"v_cmp_gt_u32 s[82:83], v13, v11\n"
"s_mov_b32 s84, 0x3e0293ee\n"
"s_mov_b32 s85, 0x3e0293ee\n"
"v_mov_b32 v3, 0xff800000\n"
"A_pol_%=:\n"
"buffer_load_dword v13, off, s[12:15], 0 sc1\n"
"s_waitcnt vmcnt(0)\n"
"v_readfirstlane_b32 s40, v13\n"
"s_add_u32 s23, s23, 1\n"
"s_cmp_ge_u32 s23, 400000\n"
"s_cselect_b32 s40, 16, s40\n"
"s_cmp_eq_u32 s40, 0\n"
"s_cbranch_scc1 A_pol_%=\n"
"s_bitcmp1_b32 s40, 5\n"
"s_cselect_b32 s74, 8192, 0\n"
"s_xor_b32 s75, s74, 8192\n"
"v_add_u32 v1, s75, v1\n"
"v_add_u32 v2, s75, v2\n"
"s_add_u32 s27, s27, s74\n"
"s_lshl_b32 s40, s65, 14\n"
"s_add_u32 s40, s40, 10485760\n"
"s_add_u32 s74, s74, s40\n"
"s_add_u32 s75, s75, s40\n"
"v_mov_b64 v[226:227], 0\n"
"v_mov_b64 v[232:233], 0\n"
"v_mov_b64 v[236:237], 0\n"
"v_mov_b64 v[8:9], 0\n"
"v_mov_b32 v235, 0\n"
"v_mov_b32 v11, 0\n"
"v_mov_b64 v[12:13], 0\n"
"s_mov_b32 s24, 0\n"
"s_mov_b32 s29, 0\n"
"A_loop0_%=:\n"
"s_mul_i32 s26, s25, 15\n"
"s_lshl1_add_u32 s26, s24, s26\n"
"s_and_b32 s26, s26, 31\n"
"s_lshl_b32 s26, s26, 22\n"
"s_cmp_lg_u32 s29, 0\n"
"s_cbranch_scc1 A_have0_%=\n"
"A_wq0_%=:\n"
"buffer_load_dwordx4 v[4:7], v1, s[12:15], 0 offen sc1\n"
"s_waitcnt vmcnt(0)\n"
"v_min3_u32 v4, v4, v5, v6\n"
"v_min_u32 v4, v4, v7\n"
"v_cmp_ge_u32 vcc, s24, v4\n"
"s_nop 0\n"
"s_and_b32 s40, vcc_lo, 0x7\n"
"s_cbranch_scc0 A_wq0g_%=\n"
"s_sleep 4\n"
"s_add_u32 s23, s23, 1\n"
"s_cmp_lt_u32 s23, 400000\n"
"s_cbranch_scc1 A_wq0_%=\n"
"A_wq0g_%=:\n"
"s_add_u32 s40, s26, s74\n"
"s_add_u32 s40, s40, 1572864\n"
"s_add_u32 s41, s40, 4096\n"
"buffer_load_dwordx4 v[16:19], v0, s[12:15], s40 offen offset:0 sc1\n"
"buffer_load_dwordx4 v[20:23], v0, s[12:15], s40 offen offset:1024 sc1\n"
"buffer_load_dwordx4 v[24:27], v0, s[12:15], s40 offen offset:2048 sc1\n"
"buffer_load_dwordx4 v[28:31], v0, s[12:15], s40 offen offset:3072 sc1\n"
"buffer_load_dwordx4 v[32:35], v0, s[12:15], s41 offen offset:0 sc1\n"
"buffer_load_dwordx4 v[36:39], v0, s[12:15], s41 offen offset:1024 sc1\n"
"buffer_load_dwordx4 v[40:43], v0, s[12:15], s41 offen offset:2048 sc1\n"
"buffer_load_dwordx4 v[44:47], v0, s[12:15], s41 offen offset:3072 sc1\n"
"s_add_u32 s40, s26, s74\n"
"s_add_u32 s40, s40, 0\n"
"s_add_u32 s41, s40, 4096\n"
"buffer_load_dwordx4 v[48:51], v0, s[12:15], s40 offen offset:0 sc1\n"
"buffer_load_dwordx4 v[52:55], v0, s[12:15], s40 offen offset:1024 sc1\n"
"buffer_load_dwordx4 v[56:59], v0, s[12:15], s40 offen offset:2048 sc1\n"
"buffer_load_dwordx4 v[60:63], v0, s[12:15], s40 offen offset:3072 sc1\n"
"buffer_load_dwordx4 v[64:67], v0, s[12:15], s41 offen offset:0 sc1\n"
"buffer_load_dwordx4 v[68:71], v0, s[12:15], s41 offen offset:1024 sc1\n"
"buffer_load_dwordx4 v[72:75], v0, s[12:15], s41 offen offset:2048 sc1\n"
"buffer_load_dwordx4 v[76:79], v0, s[12:15], s41 offen offset:3072 sc1\n"
"s_add_u32 s40, s26, s74\n"
"s_add_u32 s40, s40, 524288\n"
"s_add_u32 s41, s40, 4096\n"
"buffer_load_dwordx4 v[80:83], v0, s[12:15], s40 offen offset:0 sc1\n"
"buffer_load_dwordx4 v[84:87], v0, s[12:15], s40 offen offset:1024 sc1\n"
"buffer_load_dwordx4 v[88:91], v0, s[12:15], s40 offen offset:2048 sc1\n"
"buffer_load_dwordx4 v[92:95], v0, s[12:15], s40 offen offset:3072 sc1\n"
"buffer_load_dwordx4 v[96:99], v0, s[12:15], s41 offen offset:0 sc1\n"
"buffer_load_dwordx4 v[100:103], v0, s[12:15], s41 offen offset:1024 sc1\n"
"buffer_load_dwordx4 v[104:107], v0, s[12:15], s41 offen offset:2048 sc1\n"
"buffer_load_dwordx4 v[108:111], v0, s[12:15], s41 offen offset:3072 sc1\n"
"A_have0_%=:\n"
"buffer_load_dwordx4 v[4:7], v1, s[12:15], 0 offen sc1\n"
"s_waitcnt vmcnt(15)\n"
"v_mfma_f32_16x16x32_bf16 v[144:147], v[48:51], v[16:19], 0\n"
"v_mfma_f32_16x16x32_bf16 v[148:151], v[48:51], v[20:23], 0\n"
"v_mfma_f32_16x16x32_bf16 v[152:155], v[52:55], v[20:23], 0\n"
"s_waitcnt vmcnt(13)\n"
"v_mfma_f32_16x16x32_bf16 v[144:147], v[56:59], v[24:27], v[144:147]\n"
"v_mfma_f32_16x16x32_bf16 v[148:151], v[56:59], v[28:31], v[148:151]\n"
"v_mfma_f32_16x16x32_bf16 v[152:155], v[60:63], v[28:31], v[152:155]\n"
"s_waitcnt vmcnt(11)\n"
"v_mfma_f32_16x16x32_bf16 v[144:147], v[64:67], v[32:35], v[144:147]\n"
"v_mfma_f32_16x16x32_bf16 v[148:151], v[64:67], v[36:39], v[148:151]\n"
"v_mfma_f32_16x16x32_bf16 v[152:155], v[68:71], v[36:39], v[152:155]\n"
"s_waitcnt vmcnt(9)\n"
"v_mfma_f32_16x16x32_bf16 v[144:147], v[72:75], v[40:43], v[144:147]\n"
"v_mfma_f32_16x16x32_bf16 v[148:151], v[72:75], v[44:47], v[148:151]\n"
"v_mfma_f32_16x16x32_bf16 v[152:155], v[76:79], v[44:47], v[152:155]\n"
"s_nop 5\n"
"v_cndmask_b32 v144, v144, v3, s[76:77]\n"
"v_cndmask_b32 v145, v145, v3, s[78:79]\n"
"v_cndmask_b32 v146, v146, v3, s[80:81]\n"
"v_cndmask_b32 v147, v147, v3, s[82:83]\n"
"v_cndmask_b32 v152, v152, v3, s[76:77]\n"
"v_cndmask_b32 v153, v153, v3, s[78:79]\n"
"v_cndmask_b32 v154, v154, v3, s[80:81]\n"
"v_cndmask_b32 v155, v155, v3, s[82:83]\n"
"v_max3_f32 v240, v144, v145, v146\n"
"v_max3_f32 v242, v148, v149, v150\n"
"v_max_f32 v240, v240, v147\n"
"v_max3_f32 v242, v242, v151, v152\n"
"v_max3_f32 v249, v153, v154, v155\n"
"v_max_f32 v242, v242, v249\n"
"v_mov_b32 v248, v240\n"
"v_mov_b32 v249, v242\n"
"s_nop 0\n"
"v_permlane32_swap_b32 v240, v248\n"
"v_permlane32_swap_b32 v242, v249\n"
"v_max_f32 v240, v240, v248\n"
"v_max_f32 v242, v242, v249\n"
"v_mov_b32 v248, v240\n"
"v_mov_b32 v249, v242\n"
"s_nop 0\n"
"v_permlane16_swap_b32 v240, v248\n"
"v_permlane16_swap_b32 v242, v249\n"
"v_max_f32 v240, v240, v248\n"
"v_max_f32 v242, v242, v249\n"
"v_mul_f32 v240, s84, v240\n"
"v_mul_f32 v242, s84, v242\n"
"v_pk_fma_f32 v[144:145], v[144:145], s[84:85], v[240:241] op_sel_hi:[1,1,0] neg_lo:[0,0,1] neg_hi:[0,0,1]\n"
"v_pk_fma_f32 v[146:147], v[146:147], s[84:85], v[240:241] op_sel_hi:[1,1,0] neg_lo:[0,0,1] neg_hi:[0,0,1]\n"
"v_pk_fma_f32 v[148:149], v[148:149], s[84:85], v[242:243] op_sel_hi:[1,1,0] neg_lo:[0,0,1] neg_hi:[0,0,1]\n"
"v_pk_fma_f32 v[150:151], v[150:151], s[84:85], v[242:243] op_sel_hi:[1,1,0] neg_lo:[0,0,1] neg_hi:[0,0,1]\n"
"v_pk_fma_f32 v[152:153], v[152:153], s[84:85], v[242:243] op_sel_hi:[1,1,0] neg_lo:[0,0,1] neg_hi:[0,0,1]\n"
"v_pk_fma_f32 v[154:155], v[154:155], s[84:85], v[242:243] op_sel_hi:[1,1,0] neg_lo:[0,0,1] neg_hi:[0,0,1]\n"
"v_exp_f32 v144, v144\n"
"v_exp_f32 v145, v145\n"
"v_exp_f32 v146, v146\n"
"v_exp_f32 v147, v147\n"
"v_exp_f32 v148, v148\n"
"v_exp_f32 v149, v149\n"
"v_exp_f32 v150, v150\n"
"v_exp_f32 v151, v151\n"
"v_exp_f32 v152, v152\n"
"v_exp_f32 v153, v153\n"
"v_exp_f32 v154, v154\n"
"v_exp_f32 v155, v155\n"
"v_cvt_pk_bf16_f32 v224, v144, v145\n"
"v_cvt_pk_bf16_f32 v225, v146, v147\n"
"v_cvt_pk_bf16_f32 v228, v148, v149\n"
"v_cvt_pk_bf16_f32 v229, v150, v151\n"
"v_cvt_pk_bf16_f32 v230, v152, v153\n"
"v_cvt_pk_bf16_f32 v231, v154, v155\n"
"s_waitcnt vmcnt(8)\n"
"v_mfma_f32_16x16x32_bf16 v[160:163], v[80:83], v[224:227], 0\n"
"v_pk_add_f32 v[144:145], v[144:145], v[146:147]\n"
"v_mfma_f32_16x16x32_bf16 v[192:195], v[80:83], v[228:231], 0\n"
"v_pk_add_f32 v[148:149], v[148:149], v[150:151]\n"
"s_waitcnt vmcnt(7)\n"
"v_mfma_f32_16x16x32_bf16 v[164:167], v[84:87], v[224:227], 0\n"
"v_pk_add_f32 v[152:153], v[152:153], v[154:155]\n"
"v_mfma_f32_16x16x32_bf16 v[196:199], v[84:87], v[228:231], 0\n"
"v_pk_add_f32 v[148:149], v[148:149], v[152:153]\n"
"s_waitcnt vmcnt(6)\n"
"v_mfma_f32_16x16x32_bf16 v[168:171], v[88:91], v[224:227], 0\n"
"v_add_f32 v244, v144, v145\n"
"v_mfma_f32_16x16x32_bf16 v[200:203], v[88:91], v[228:231], 0\n"
"v_add_f32 v246, v148, v149\n"
"s_waitcnt vmcnt(5)\n"
"v_mfma_f32_16x16x32_bf16 v[172:175], v[92:95], v[224:227], 0\n"
"v_mfma_f32_16x16x32_bf16 v[204:207], v[92:95], v[228:231], 0\n"
"s_waitcnt vmcnt(4)\n"
"v_mfma_f32_16x16x32_bf16 v[176:179], v[96:99], v[224:227], 0\n"
"v_mfma_f32_16x16x32_bf16 v[208:211], v[96:99], v[228:231], 0\n"
"s_waitcnt vmcnt(3)\n"
"v_mfma_f32_16x16x32_bf16 v[180:183], v[100:103], v[224:227], 0\n"
"v_mfma_f32_16x16x32_bf16 v[212:215], v[100:103], v[228:231], 0\n"
"s_waitcnt vmcnt(2)\n"
"v_mfma_f32_16x16x32_bf16 v[184:187], v[104:107], v[224:227], 0\n"
"v_mfma_f32_16x16x32_bf16 v[216:219], v[104:107], v[228:231], 0\n"
"s_waitcnt vmcnt(1)\n"
"v_mfma_f32_16x16x32_bf16 v[188:191], v[108:111], v[224:227], 0\n"
"v_mfma_f32_16x16x32_bf16 v[220:223], v[108:111], v[228:231], 0\n"
"s_waitcnt vmcnt(0)\n"
"v_min3_u32 v4, v4, v5, v6\n"
"s_add_u32 s28, s24, 1\n"
"v_min_u32 v4, v4, v7\n"
"v_cmp_ge_u32 vcc, s28, v4\n"
"s_mov_b32 s29, 0\n"
"s_and_b32 s40, vcc_lo, 7\n"
"s_cbranch_scc1 A_nopf0_%=\n"
"s_mul_i32 s29, s25, 15\n"
"s_lshl1_add_u32 s29, s28, s29\n"
"s_and_b32 s29, s29, 31\n"
"s_lshl_b32 s29, s29, 22\n"
"s_add_u32 s40, s29, s74\n"
"s_add_u32 s40, s40, 1572864\n"
"s_add_u32 s41, s40, 4096\n"
"buffer_load_dwordx4 a[16:19], v0, s[12:15], s40 offen offset:0 sc1\n"
"buffer_load_dwordx4 a[20:23], v0, s[12:15], s40 offen offset:1024 sc1\n"
"buffer_load_dwordx4 a[24:27], v0, s[12:15], s40 offen offset:2048 sc1\n"
"buffer_load_dwordx4 a[28:31], v0, s[12:15], s40 offen offset:3072 sc1\n"
"buffer_load_dwordx4 a[32:35], v0, s[12:15], s41 offen offset:0 sc1\n"
"buffer_load_dwordx4 a[36:39], v0, s[12:15], s41 offen offset:1024 sc1\n"
"buffer_load_dwordx4 a[40:43], v0, s[12:15], s41 offen offset:2048 sc1\n"
"buffer_load_dwordx4 a[44:47], v0, s[12:15], s41 offen offset:3072 sc1\n"
"s_add_u32 s40, s29, s74\n"
"s_add_u32 s40, s40, 0\n"
"s_add_u32 s41, s40, 4096\n"
"buffer_load_dwordx4 a[48:51], v0, s[12:15], s40 offen offset:0 sc1\n"
"buffer_load_dwordx4 a[52:55], v0, s[12:15], s40 offen offset:1024 sc1\n"
"buffer_load_dwordx4 a[56:59], v0, s[12:15], s40 offen offset:2048 sc1\n"
"buffer_load_dwordx4 a[60:63], v0, s[12:15], s40 offen offset:3072 sc1\n"
"buffer_load_dwordx4 a[64:67], v0, s[12:15], s41 offen offset:0 sc1\n"
"buffer_load_dwordx4 a[68:71], v0, s[12:15], s41 offen offset:1024 sc1\n"
"buffer_load_dwordx4 a[72:75], v0, s[12:15], s41 offen offset:2048 sc1\n"
"buffer_load_dwordx4 a[76:79], v0, s[12:15], s41 offen offset:3072 sc1\n"
"s_add_u32 s40, s29, s74\n"
"s_add_u32 s40, s40, 524288\n"
"s_add_u32 s41, s40, 4096\n"
"buffer_load_dwordx4 a[80:83], v0, s[12:15], s40 offen offset:0 sc1\n"
"buffer_load_dwordx4 a[84:87], v0, s[12:15], s40 offen offset:1024 sc1\n"
"buffer_load_dwordx4 a[88:91], v0, s[12:15], s40 offen offset:2048 sc1\n"
"buffer_load_dwordx4 a[92:95], v0, s[12:15], s40 offen offset:3072 sc1\n"
"buffer_load_dwordx4 a[96:99], v0, s[12:15], s41 offen offset:0 sc1\n"
"buffer_load_dwordx4 a[100:103], v0, s[12:15], s41 offen offset:1024 sc1\n"
"buffer_load_dwordx4 a[104:107], v0, s[12:15], s41 offen offset:2048 sc1\n"
"buffer_load_dwordx4 a[108:111], v0, s[12:15], s41 offen offset:3072 sc1\n"
"s_mov_b32 s29, 1\n"
"A_nopf0_%=:\n"
"A_wx0_%=:\n"
"buffer_load_dword v4, v2, s[12:15], 0 offen sc1\n"
"s_waitcnt vmcnt(0)\n"
"v_cmp_ge_u32 vcc, s24, v4\n"
"s_nop 0\n"
"s_and_b32 s40, vcc_lo, 0x3\n"
"s_cbranch_scc0 A_wx0g_%=\n"
"s_sleep 4\n"
"s_add_u32 s23, s23, 1\n"
"s_cmp_lt_u32 s23, 400000\n"
"s_cbranch_scc1 A_wx0_%=\n"
"A_wx0g_%=:\n"
"s_add_u32 s40, s26, s74\n"
"s_add_u32 s40, s40, 1048576\n"
"s_add_u32 s41, s40, 4096\n"
"buffer_load_dwordx4 v[112:115], v0, s[12:15], s40 offen offset:0 sc1\n"
"buffer_load_dwordx4 v[116:119], v0, s[12:15], s40 offen offset:1024 sc1\n"
"buffer_load_dwordx4 v[120:123], v0, s[12:15], s40 offen offset:2048 sc1\n"
"buffer_load_dwordx4 v[124:127], v0, s[12:15], s40 offen offset:3072 sc1\n"
"buffer_load_dwordx4 v[128:131], v0, s[12:15], s41 offen offset:0 sc1\n"
"buffer_load_dwordx4 v[132:135], v0, s[12:15], s41 offen offset:1024 sc1\n"
"buffer_load_dwordx4 v[136:139], v0, s[12:15], s41 offen offset:2048 sc1\n"
"buffer_load_dwordx4 v[140:143], v0, s[12:15], s41 offen offset:3072 sc1\n"
"s_waitcnt vmcnt(7)\n"
"v_mfma_f32_16x16x32_bf16 v[144:147], v[112:115], v[16:19], 0\n"
"v_mfma_f32_16x16x32_bf16 v[148:151], v[112:115], v[20:23], 0\n"
"s_waitcnt vmcnt(6)\n"
"v_mfma_f32_16x16x32_bf16 v[144:147], v[116:119], v[24:27], v[144:147]\n"
"v_mfma_f32_16x16x32_bf16 v[148:151], v[116:119], v[28:31], v[148:151]\n"
"s_waitcnt vmcnt(5)\n"
"v_mfma_f32_16x16x32_bf16 v[144:147], v[120:123], v[32:35], v[144:147]\n"
"v_mfma_f32_16x16x32_bf16 v[148:151], v[120:123], v[36:39], v[148:151]\n"
"s_waitcnt vmcnt(4)\n"
"v_mfma_f32_16x16x32_bf16 v[144:147], v[124:127], v[40:43], v[144:147]\n"
"v_mfma_f32_16x16x32_bf16 v[148:151], v[124:127], v[44:47], v[148:151]\n"
"s_add_u32 s42, s26, s75\n"
"s_add_u32 s42, s42, 524288\n"
"s_add_u32 s43, s42, 4096\n"
"s_add_u32 s24, s24, 1\n"
"v_mov_b32 v5, s24\n"
"s_nop 1\n"
"v_max_f32 v152, v144, v145\n"
"v_max_f32 v154, v148, v149\n"
"v_mov_b32 v248, v152\n"
"v_mov_b32 v249, v154\n"
"s_nop 0\n"
"v_permlane32_swap_b32 v152, v248\n"
"v_permlane32_swap_b32 v154, v249\n"
"v_max_f32 v152, v152, v248\n"
"v_max_f32 v154, v154, v249\n"
"v_mov_b32 v248, v152\n"
"v_mov_b32 v249, v154\n"
"s_nop 0\n"
"v_permlane16_swap_b32 v152, v248\n"
"v_permlane16_swap_b32 v154, v249\n"
"v_max_f32 v152, v152, v248\n"
"v_max_f32 v154, v154, v249\n"
"v_mul_f32 v152, s84, v152\n"
"v_mul_f32 v154, s84, v154\n"
"v_max_f32 v152, v152, v240\n"
"v_max_f32 v154, v154, v242\n"
"v_pk_fma_f32 v[144:145], v[144:145], s[84:85], v[152:153] op_sel_hi:[1,1,0] neg_lo:[0,0,1] neg_hi:[0,0,1]\n"
"v_pk_fma_f32 v[148:149], v[148:149], s[84:85], v[154:155] op_sel_hi:[1,1,0] neg_lo:[0,0,1] neg_hi:[0,0,1]\n"
"v_sub_f32 v240, v240, v152\n"
"v_sub_f32 v242, v242, v154\n"
"v_exp_f32 v144, v144\n"
"v_exp_f32 v145, v145\n"
"v_exp_f32 v148, v148\n"
"v_exp_f32 v149, v149\n"
"v_exp_f32 v240, v240\n"
"v_exp_f32 v242, v242\n"
"v_add_f32 v152, v144, v145\n"
"v_add_f32 v154, v148, v149\n"
"v_fmac_f32 v152, v240, v244\n"
"v_fmac_f32 v154, v242, v246\n"
"v_mov_b32 v248, v152\n"
"v_mov_b32 v249, v154\n"
"s_nop 0\n"
"v_permlane32_swap_b32 v152, v248\n"
"v_permlane32_swap_b32 v154, v249\n"
"v_add_f32 v152, v152, v248\n"
"v_add_f32 v154, v154, v249\n"
"v_mov_b32 v248, v152\n"
"v_mov_b32 v249, v154\n"
"s_nop 0\n"
"v_permlane16_swap_b32 v152, v248\n"
"v_permlane16_swap_b32 v154, v249\n"
"v_add_f32 v152, v152, v248\n"
"v_add_f32 v154, v154, v249\n"
"v_rcp_f32 v152, v152\n"
"v_rcp_f32 v154, v154\n"
"v_pk_mul_f32 v[144:145], v[144:145], v[152:153] op_sel_hi:[1,0]\n"
"v_pk_mul_f32 v[148:149], v[148:149], v[154:155] op_sel_hi:[1,0]\n"
"v_mul_f32 v240, v240, v152\n"
"v_mul_f32 v242, v242, v154\n"
"v_cvt_pk_bf16_f32 v234, v144, v145\n"
"v_cvt_pk_bf16_f32 v10, v148, v149\n"
"v_pk_mul_f32 v[160:161], v[160:161], v[240:241] op_sel_hi:[1,0]\n"
"v_pk_mul_f32 v[162:163], v[162:163], v[240:241] op_sel_hi:[1,0]\n"
"v_pk_mul_f32 v[164:165], v[164:165], v[240:241] op_sel_hi:[1,0]\n"
"v_pk_mul_f32 v[166:167], v[166:167], v[240:241] op_sel_hi:[1,0]\n"
"v_pk_mul_f32 v[192:193], v[192:193], v[242:243] op_sel_hi:[1,0]\n"
"v_pk_mul_f32 v[194:195], v[194:195], v[242:243] op_sel_hi:[1,0]\n"
"v_pk_mul_f32 v[196:197], v[196:197], v[242:243] op_sel_hi:[1,0]\n"
"v_pk_mul_f32 v[198:199], v[198:199], v[242:243] op_sel_hi:[1,0]\n"
"s_waitcnt vmcnt(3)\n"
"v_mfma_f32_16x16x32_bf16 v[160:163], v[128:131], v[234:237], v[160:163]\n"
"v_mfma_f32_16x16x32_bf16 v[164:167], v[128:131], v[232:235], v[164:167]\n"
"v_pk_mul_f32 v[168:169], v[168:169], v[240:241] op_sel_hi:[1,0]\n"
"v_pk_mul_f32 v[170:171], v[170:171], v[240:241] op_sel_hi:[1,0]\n"
"v_pk_mul_f32 v[172:173], v[172:173], v[240:241] op_sel_hi:[1,0]\n"
"v_pk_mul_f32 v[174:175], v[174:175], v[240:241] op_sel_hi:[1,0]\n"
"v_mfma_f32_16x16x32_bf16 v[192:195], v[128:131], v[10:13], v[192:195]\n"
"v_mfma_f32_16x16x32_bf16 v[196:199], v[128:131], v[8:11], v[196:199]\n"
"v_pk_mul_f32 v[200:201], v[200:201], v[242:243] op_sel_hi:[1,0]\n"
"v_pk_mul_f32 v[202:203], v[202:203], v[242:243] op_sel_hi:[1,0]\n"
"v_pk_mul_f32 v[204:205], v[204:205], v[242:243] op_sel_hi:[1,0]\n"
"v_pk_mul_f32 v[206:207], v[206:207], v[242:243] op_sel_hi:[1,0]\n"
"s_waitcnt vmcnt(2)\n"
"v_mfma_f32_16x16x32_bf16 v[168:171], v[132:135], v[234:237], v[168:171]\n"
"v_mfma_f32_16x16x32_bf16 v[172:175], v[132:135], v[232:235], v[172:175]\n"
"v_cvt_pk_bf16_f32 v160, v160, v161\n"
"v_cvt_pk_bf16_f32 v161, v162, v163\n"
"v_cvt_pk_bf16_f32 v162, v164, v165\n"
"v_cvt_pk_bf16_f32 v163, v166, v167\n"
"buffer_store_dwordx4 v[160:163], v0, s[12:15], s42 offen offset:0 sc1\n"
"v_pk_mul_f32 v[176:177], v[176:177], v[240:241] op_sel_hi:[1,0]\n"
"v_pk_mul_f32 v[178:179], v[178:179], v[240:241] op_sel_hi:[1,0]\n"
"v_pk_mul_f32 v[180:181], v[180:181], v[240:241] op_sel_hi:[1,0]\n"
"v_pk_mul_f32 v[182:183], v[182:183], v[240:241] op_sel_hi:[1,0]\n"
"v_mfma_f32_16x16x32_bf16 v[200:203], v[132:135], v[10:13], v[200:203]\n"
"v_mfma_f32_16x16x32_bf16 v[204:207], v[132:135], v[8:11], v[204:207]\n"
"v_cvt_pk_bf16_f32 v192, v192, v193\n"
"v_cvt_pk_bf16_f32 v193, v194, v195\n"
"v_cvt_pk_bf16_f32 v194, v196, v197\n"
"v_cvt_pk_bf16_f32 v195, v198, v199\n"
"buffer_store_dwordx4 v[192:195], v0, s[12:15], s42 offen offset:1024 sc1\n"
"v_pk_mul_f32 v[208:209], v[208:209], v[242:243] op_sel_hi:[1,0]\n"
"v_pk_mul_f32 v[210:211], v[210:211], v[242:243] op_sel_hi:[1,0]\n"
"v_pk_mul_f32 v[212:213], v[212:213], v[242:243] op_sel_hi:[1,0]\n"
"v_pk_mul_f32 v[214:215], v[214:215], v[242:243] op_sel_hi:[1,0]\n"
"s_waitcnt vmcnt(3)\n"
"v_mfma_f32_16x16x32_bf16 v[176:179], v[136:139], v[234:237], v[176:179]\n"
"v_mfma_f32_16x16x32_bf16 v[180:183], v[136:139], v[232:235], v[180:183]\n"
"v_cvt_pk_bf16_f32 v168, v168, v169\n"
"v_cvt_pk_bf16_f32 v169, v170, v171\n"
"v_cvt_pk_bf16_f32 v170, v172, v173\n"
"v_cvt_pk_bf16_f32 v171, v174, v175\n"
"buffer_store_dwordx4 v[168:171], v0, s[12:15], s42 offen offset:2048 sc1\n"
"v_pk_mul_f32 v[184:185], v[184:185], v[240:241] op_sel_hi:[1,0]\n"
"v_pk_mul_f32 v[186:187], v[186:187], v[240:241] op_sel_hi:[1,0]\n"
"v_pk_mul_f32 v[188:189], v[188:189], v[240:241] op_sel_hi:[1,0]\n"
"v_pk_mul_f32 v[190:191], v[190:191], v[240:241] op_sel_hi:[1,0]\n"
"v_mfma_f32_16x16x32_bf16 v[208:211], v[136:139], v[10:13], v[208:211]\n"
"v_mfma_f32_16x16x32_bf16 v[212:215], v[136:139], v[8:11], v[212:215]\n"
"v_cvt_pk_bf16_f32 v200, v200, v201\n"
"v_cvt_pk_bf16_f32 v201, v202, v203\n"
"v_cvt_pk_bf16_f32 v202, v204, v205\n"
"v_cvt_pk_bf16_f32 v203, v206, v207\n"
"buffer_store_dwordx4 v[200:203], v0, s[12:15], s42 offen offset:3072 sc1\n"
"v_pk_mul_f32 v[216:217], v[216:217], v[242:243] op_sel_hi:[1,0]\n"
"v_pk_mul_f32 v[218:219], v[218:219], v[242:243] op_sel_hi:[1,0]\n"
"v_pk_mul_f32 v[220:221], v[220:221], v[242:243] op_sel_hi:[1,0]\n"
"v_pk_mul_f32 v[222:223], v[222:223], v[242:243] op_sel_hi:[1,0]\n"
"s_waitcnt vmcnt(4)\n"
"v_mfma_f32_16x16x32_bf16 v[184:187], v[140:143], v[234:237], v[184:187]\n"
"v_mfma_f32_16x16x32_bf16 v[188:191], v[140:143], v[232:235], v[188:191]\n"
"v_cvt_pk_bf16_f32 v176, v176, v177\n"
"v_cvt_pk_bf16_f32 v177, v178, v179\n"
"v_cvt_pk_bf16_f32 v178, v180, v181\n"
"v_cvt_pk_bf16_f32 v179, v182, v183\n"
"buffer_store_dwordx4 v[176:179], v0, s[12:15], s43 offen offset:0 sc1\n"
"v_mfma_f32_16x16x32_bf16 v[216:219], v[140:143], v[10:13], v[216:219]\n"
"v_mfma_f32_16x16x32_bf16 v[220:223], v[140:143], v[8:11], v[220:223]\n"
"v_cvt_pk_bf16_f32 v208, v208, v209\n"
"v_cvt_pk_bf16_f32 v209, v210, v211\n"
"v_cvt_pk_bf16_f32 v210, v212, v213\n"
"v_cvt_pk_bf16_f32 v211, v214, v215\n"
"buffer_store_dwordx4 v[208:211], v0, s[12:15], s43 offen offset:1024 sc1\n"
"v_cvt_pk_bf16_f32 v184, v184, v185\n"
"v_cvt_pk_bf16_f32 v185, v186, v187\n"
"v_cvt_pk_bf16_f32 v186, v188, v189\n"
"v_cvt_pk_bf16_f32 v187, v190, v191\n"
"buffer_store_dwordx4 v[184:187], v0, s[12:15], s43 offen offset:2048 sc1\n"
"v_cvt_pk_bf16_f32 v216, v216, v217\n"
"v_cvt_pk_bf16_f32 v217, v218, v219\n"
"v_cvt_pk_bf16_f32 v218, v220, v221\n"
"v_cvt_pk_bf16_f32 v219, v222, v223\n"
"buffer_store_dwordx4 v[216:219], v0, s[12:15], s43 offen offset:3072 sc1\n"
"s_waitcnt vmcnt(0)\n"
"buffer_store_dword v5, off, s[12:15], s27 sc1\n"
"s_cmp_lt_u32 s24, 64\n"
"A_loop1_%=:\n"
"s_mul_i32 s26, s25, 15\n"
"s_lshl1_add_u32 s26, s24, s26\n"
"s_and_b32 s26, s26, 31\n"
"s_lshl_b32 s26, s26, 22\n"
"s_cmp_lg_u32 s29, 0\n"
"s_cbranch_scc1 A_have1_%=\n"
"A_wq1_%=:\n"
"buffer_load_dwordx4 v[4:7], v1, s[12:15], 0 offen sc1\n"
"s_waitcnt vmcnt(0)\n"
"v_min3_u32 v4, v4, v5, v6\n"
"v_min_u32 v4, v4, v7\n"
"v_cmp_ge_u32 vcc, s24, v4\n"
"s_nop 0\n"
"s_and_b32 s40, vcc_lo, 0x7\n"
"s_cbranch_scc0 A_wq1g_%=\n"
"s_sleep 4\n"
"s_add_u32 s23, s23, 1\n"
"s_cmp_lt_u32 s23, 400000\n"
"s_cbranch_scc1 A_wq1_%=\n"
"A_wq1g_%=:\n"
"s_add_u32 s40, s26, s74\n"
"s_add_u32 s40, s40, 1572864\n"
"s_add_u32 s41, s40, 4096\n"
"buffer_load_dwordx4 a[16:19], v0, s[12:15], s40 offen offset:0 sc1\n"
"buffer_load_dwordx4 a[20:23], v0, s[12:15], s40 offen offset:1024 sc1\n"
"buffer_load_dwordx4 a[24:27], v0, s[12:15], s40 offen offset:2048 sc1\n"
"buffer_load_dwordx4 a[28:31], v0, s[12:15], s40 offen offset:3072 sc1\n"
"buffer_load_dwordx4 a[32:35], v0, s[12:15], s41 offen offset:0 sc1\n"
"buffer_load_dwordx4 a[36:39], v0, s[12:15], s41 offen offset:1024 sc1\n"
"buffer_load_dwordx4 a[40:43], v0, s[12:15], s41 offen offset:2048 sc1\n"
"buffer_load_dwordx4 a[44:47], v0, s[12:15], s41 offen offset:3072 sc1\n"
"s_add_u32 s40, s26, s74\n"
"s_add_u32 s40, s40, 0\n"
"s_add_u32 s41, s40, 4096\n"
"buffer_load_dwordx4 a[48:51], v0, s[12:15], s40 offen offset:0 sc1\n"
"buffer_load_dwordx4 a[52:55], v0, s[12:15], s40 offen offset:1024 sc1\n"
"buffer_load_dwordx4 a[56:59], v0, s[12:15], s40 offen offset:2048 sc1\n"
"buffer_load_dwordx4 a[60:63], v0, s[12:15], s40 offen offset:3072 sc1\n"
"buffer_load_dwordx4 a[64:67], v0, s[12:15], s41 offen offset:0 sc1\n"
"buffer_load_dwordx4 a[68:71], v0, s[12:15], s41 offen offset:1024 sc1\n"
"buffer_load_dwordx4 a[72:75], v0, s[12:15], s41 offen offset:2048 sc1\n"
"buffer_load_dwordx4 a[76:79], v0, s[12:15], s41 offen offset:3072 sc1\n"
"s_add_u32 s40, s26, s74\n"
"s_add_u32 s40, s40, 524288\n"
"s_add_u32 s41, s40, 4096\n"
"buffer_load_dwordx4 a[80:83], v0, s[12:15], s40 offen offset:0 sc1\n"
"buffer_load_dwordx4 a[84:87], v0, s[12:15], s40 offen offset:1024 sc1\n"
"buffer_load_dwordx4 a[88:91], v0, s[12:15], s40 offen offset:2048 sc1\n"
"buffer_load_dwordx4 a[92:95], v0, s[12:15], s40 offen offset:3072 sc1\n"
"buffer_load_dwordx4 a[96:99], v0, s[12:15], s41 offen offset:0 sc1\n"
"buffer_load_dwordx4 a[100:103], v0, s[12:15], s41 offen offset:1024 sc1\n"
"buffer_load_dwordx4 a[104:107], v0, s[12:15], s41 offen offset:2048 sc1\n"
"buffer_load_dwordx4 a[108:111], v0, s[12:15], s41 offen offset:3072 sc1\n"
"A_have1_%=:\n"
"buffer_load_dwordx4 v[4:7], v1, s[12:15], 0 offen sc1\n"
"s_waitcnt vmcnt(15)\n"
"v_mfma_f32_16x16x32_bf16 v[144:147], a[48:51], a[16:19], 0\n"
"v_mfma_f32_16x16x32_bf16 v[148:151], a[48:51], a[20:23], 0\n"
"v_mfma_f32_16x16x32_bf16 v[152:155], a[52:55], a[20:23], 0\n"
"s_waitcnt vmcnt(13)\n"
"v_mfma_f32_16x16x32_bf16 v[144:147], a[56:59], a[24:27], v[144:147]\n"
"v_mfma_f32_16x16x32_bf16 v[148:151], a[56:59], a[28:31], v[148:151]\n"
"v_mfma_f32_16x16x32_bf16 v[152:155], a[60:63], a[28:31], v[152:155]\n"
"s_waitcnt vmcnt(11)\n"
"v_mfma_f32_16x16x32_bf16 v[144:147], a[64:67], a[32:35], v[144:147]\n"
"v_mfma_f32_16x16x32_bf16 v[148:151], a[64:67], a[36:39], v[148:151]\n"
"v_mfma_f32_16x16x32_bf16 v[152:155], a[68:71], a[36:39], v[152:155]\n"
"s_waitcnt vmcnt(9)\n"
"v_mfma_f32_16x16x32_bf16 v[144:147], a[72:75], a[40:43], v[144:147]\n"
"v_mfma_f32_16x16x32_bf16 v[148:151], a[72:75], a[44:47], v[148:151]\n"
"v_mfma_f32_16x16x32_bf16 v[152:155], a[76:79], a[44:47], v[152:155]\n"
"s_nop 5\n"
"v_cndmask_b32 v144, v144, v3, s[76:77]\n"
"v_cndmask_b32 v145, v145, v3, s[78:79]\n"
"v_cndmask_b32 v146, v146, v3, s[80:81]\n"
"v_cndmask_b32 v147, v147, v3, s[82:83]\n"
"v_cndmask_b32 v152, v152, v3, s[76:77]\n"
"v_cndmask_b32 v153, v153, v3, s[78:79]\n"
"v_cndmask_b32 v154, v154, v3, s[80:81]\n"
"v_cndmask_b32 v155, v155, v3, s[82:83]\n"
"v_max3_f32 v240, v144, v145, v146\n"
"v_max3_f32 v242, v148, v149, v150\n"
"v_max_f32 v240, v240, v147\n"
"v_max3_f32 v242, v242, v151, v152\n"
"v_max3_f32 v249, v153, v154, v155\n"
"v_max_f32 v242, v242, v249\n"
"v_mov_b32 v248, v240\n"
"v_mov_b32 v249, v242\n"
"s_nop 0\n"
"v_permlane32_swap_b32 v240, v248\n"
"v_permlane32_swap_b32 v242, v249\n"
"v_max_f32 v240, v240, v248\n"
"v_max_f32 v242, v242, v249\n"
"v_mov_b32 v248, v240\n"
"v_mov_b32 v249, v242\n"
"s_nop 0\n"
"v_permlane16_swap_b32 v240, v248\n"
"v_permlane16_swap_b32 v242, v249\n"
"v_max_f32 v240, v240, v248\n"
"v_max_f32 v242, v242, v249\n"
"v_mul_f32 v240, s84, v240\n"
"v_mul_f32 v242, s84, v242\n"
"v_pk_fma_f32 v[144:145], v[144:145], s[84:85], v[240:241] op_sel_hi:[1,1,0] neg_lo:[0,0,1] neg_hi:[0,0,1]\n"
"v_pk_fma_f32 v[146:147], v[146:147], s[84:85], v[240:241] op_sel_hi:[1,1,0] neg_lo:[0,0,1] neg_hi:[0,0,1]\n"
"v_pk_fma_f32 v[148:149], v[148:149], s[84:85], v[242:243] op_sel_hi:[1,1,0] neg_lo:[0,0,1] neg_hi:[0,0,1]\n"
"v_pk_fma_f32 v[150:151], v[150:151], s[84:85], v[242:243] op_sel_hi:[1,1,0] neg_lo:[0,0,1] neg_hi:[0,0,1]\n"
"v_pk_fma_f32 v[152:153], v[152:153], s[84:85], v[242:243] op_sel_hi:[1,1,0] neg_lo:[0,0,1] neg_hi:[0,0,1]\n"
"v_pk_fma_f32 v[154:155], v[154:155], s[84:85], v[242:243] op_sel_hi:[1,1,0] neg_lo:[0,0,1] neg_hi:[0,0,1]\n"
"v_exp_f32 v144, v144\n"
"v_exp_f32 v145, v145\n"
"v_exp_f32 v146, v146\n"
"v_exp_f32 v147, v147\n"
"v_exp_f32 v148, v148\n"
"v_exp_f32 v149, v149\n"
"v_exp_f32 v150, v150\n"
"v_exp_f32 v151, v151\n"
"v_exp_f32 v152, v152\n"
"v_exp_f32 v153, v153\n"
"v_exp_f32 v154, v154\n"
"v_exp_f32 v155, v155\n"
"v_cvt_pk_bf16_f32 v224, v144, v145\n"
"v_cvt_pk_bf16_f32 v225, v146, v147\n"
"v_cvt_pk_bf16_f32 v228, v148, v149\n"
"v_cvt_pk_bf16_f32 v229, v150, v151\n"
"v_cvt_pk_bf16_f32 v230, v152, v153\n"
"v_cvt_pk_bf16_f32 v231, v154, v155\n"
"s_waitcnt vmcnt(8)\n"
"v_mfma_f32_16x16x32_bf16 v[160:163], a[80:83], v[224:227], 0\n"
"v_pk_add_f32 v[144:145], v[144:145], v[146:147]\n"
"v_mfma_f32_16x16x32_bf16 v[192:195], a[80:83], v[228:231], 0\n"
"v_pk_add_f32 v[148:149], v[148:149], v[150:151]\n"
"s_waitcnt vmcnt(7)\n"
"v_mfma_f32_16x16x32_bf16 v[164:167], a[84:87], v[224:227], 0\n"
"v_pk_add_f32 v[152:153], v[152:153], v[154:155]\n"
"v_mfma_f32_16x16x32_bf16 v[196:199], a[84:87], v[228:231], 0\n"
"v_pk_add_f32 v[148:149], v[148:149], v[152:153]\n"
"s_waitcnt vmcnt(6)\n"
"v_mfma_f32_16x16x32_bf16 v[168:171], a[88:91], v[224:227], 0\n"
"v_add_f32 v244, v144, v145\n"
"v_mfma_f32_16x16x32_bf16 v[200:203], a[88:91], v[228:231], 0\n"
"v_add_f32 v246, v148, v149\n"
"s_waitcnt vmcnt(5)\n"
"v_mfma_f32_16x16x32_bf16 v[172:175], a[92:95], v[224:227], 0\n"
"v_mfma_f32_16x16x32_bf16 v[204:207], a[92:95], v[228:231], 0\n"
"s_waitcnt vmcnt(4)\n"
"v_mfma_f32_16x16x32_bf16 v[176:179], a[96:99], v[224:227], 0\n"
"v_mfma_f32_16x16x32_bf16 v[208:211], a[96:99], v[228:231], 0\n"
"s_waitcnt vmcnt(3)\n"
"v_mfma_f32_16x16x32_bf16 v[180:183], a[100:103], v[224:227], 0\n"
"v_mfma_f32_16x16x32_bf16 v[212:215], a[100:103], v[228:231], 0\n"
"s_waitcnt vmcnt(2)\n"
"v_mfma_f32_16x16x32_bf16 v[184:187], a[104:107], v[224:227], 0\n"
"v_mfma_f32_16x16x32_bf16 v[216:219], a[104:107], v[228:231], 0\n"
"s_waitcnt vmcnt(1)\n"
"v_mfma_f32_16x16x32_bf16 v[188:191], a[108:111], v[224:227], 0\n"
"v_mfma_f32_16x16x32_bf16 v[220:223], a[108:111], v[228:231], 0\n"
"s_waitcnt vmcnt(0)\n"
"v_min3_u32 v4, v4, v5, v6\n"
"s_add_u32 s28, s24, 1\n"
"v_min_u32 v4, v4, v7\n"
"v_cmp_ge_u32 vcc, s28, v4\n"
"s_mov_b32 s29, 0\n"
"s_and_b32 s40, vcc_lo, 7\n"
"s_cbranch_scc1 A_nopf1_%=\n"
"s_mul_i32 s29, s25, 15\n"
"s_lshl1_add_u32 s29, s28, s29\n"
"s_and_b32 s29, s29, 31\n"
"s_lshl_b32 s29, s29, 22\n"
"s_add_u32 s40, s29, s74\n"
"s_add_u32 s40, s40, 1572864\n"
"s_add_u32 s41, s40, 4096\n"
"buffer_load_dwordx4 v[16:19], v0, s[12:15], s40 offen offset:0 sc1\n"
"buffer_load_dwordx4 v[20:23], v0, s[12:15], s40 offen offset:1024 sc1\n"
"buffer_load_dwordx4 v[24:27], v0, s[12:15], s40 offen offset:2048 sc1\n"
"buffer_load_dwordx4 v[28:31], v0, s[12:15], s40 offen offset:3072 sc1\n"
"buffer_load_dwordx4 v[32:35], v0, s[12:15], s41 offen offset:0 sc1\n"
"buffer_load_dwordx4 v[36:39], v0, s[12:15], s41 offen offset:1024 sc1\n"
"buffer_load_dwordx4 v[40:43], v0, s[12:15], s41 offen offset:2048 sc1\n"
"buffer_load_dwordx4 v[44:47], v0, s[12:15], s41 offen offset:3072 sc1\n"
"s_add_u32 s40, s29, s74\n"
"s_add_u32 s40, s40, 0\n"
"s_add_u32 s41, s40, 4096\n"
"buffer_load_dwordx4 v[48:51], v0, s[12:15], s40 offen offset:0 sc1\n"
"buffer_load_dwordx4 v[52:55], v0, s[12:15], s40 offen offset:1024 sc1\n"
"buffer_load_dwordx4 v[56:59], v0, s[12:15], s40 offen offset:2048 sc1\n"
"buffer_load_dwordx4 v[60:63], v0, s[12:15], s40 offen offset:3072 sc1\n"
"buffer_load_dwordx4 v[64:67], v0, s[12:15], s41 offen offset:0 sc1\n"
"buffer_load_dwordx4 v[68:71], v0, s[12:15], s41 offen offset:1024 sc1\n"
"buffer_load_dwordx4 v[72:75], v0, s[12:15], s41 offen offset:2048 sc1\n"
"buffer_load_dwordx4 v[76:79], v0, s[12:15], s41 offen offset:3072 sc1\n"
"s_add_u32 s40, s29, s74\n"
"s_add_u32 s40, s40, 524288\n"
"s_add_u32 s41, s40, 4096\n"
"buffer_load_dwordx4 v[80:83], v0, s[12:15], s40 offen offset:0 sc1\n"
"buffer_load_dwordx4 v[84:87], v0, s[12:15], s40 offen offset:1024 sc1\n"
"buffer_load_dwordx4 v[88:91], v0, s[12:15], s40 offen offset:2048 sc1\n"
"buffer_load_dwordx4 v[92:95], v0, s[12:15], s40 offen offset:3072 sc1\n"
"buffer_load_dwordx4 v[96:99], v0, s[12:15], s41 offen offset:0 sc1\n"
"buffer_load_dwordx4 v[100:103], v0, s[12:15], s41 offen offset:1024 sc1\n"
"buffer_load_dwordx4 v[104:107], v0, s[12:15], s41 offen offset:2048 sc1\n"
"buffer_load_dwordx4 v[108:111], v0, s[12:15], s41 offen offset:3072 sc1\n"
"s_mov_b32 s29, 1\n"
"A_nopf1_%=:\n"
"A_wx1_%=:\n"
"buffer_load_dword v4, v2, s[12:15], 0 offen sc1\n"
"s_waitcnt vmcnt(0)\n"
"v_cmp_ge_u32 vcc, s24, v4\n"
"s_nop 0\n"
"s_and_b32 s40, vcc_lo, 0x3\n"
"s_cbranch_scc0 A_wx1g_%=\n"
"s_sleep 4\n"
"s_add_u32 s23, s23, 1\n"
"s_cmp_lt_u32 s23, 400000\n"
"s_cbranch_scc1 A_wx1_%=\n"
"A_wx1g_%=:\n"
"s_add_u32 s40, s26, s74\n"
"s_add_u32 s40, s40, 1048576\n"
"s_add_u32 s41, s40, 4096\n"
"buffer_load_dwordx4 v[112:115], v0, s[12:15], s40 offen offset:0 sc1\n"
"buffer_load_dwordx4 v[116:119], v0, s[12:15], s40 offen offset:1024 sc1\n"
"buffer_load_dwordx4 v[120:123], v0, s[12:15], s40 offen offset:2048 sc1\n"
"buffer_load_dwordx4 v[124:127], v0, s[12:15], s40 offen offset:3072 sc1\n"
"buffer_load_dwordx4 v[128:131], v0, s[12:15], s41 offen offset:0 sc1\n"
"buffer_load_dwordx4 v[132:135], v0, s[12:15], s41 offen offset:1024 sc1\n"
"buffer_load_dwordx4 v[136:139], v0, s[12:15], s41 offen offset:2048 sc1\n"
"buffer_load_dwordx4 v[140:143], v0, s[12:15], s41 offen offset:3072 sc1\n"
"s_waitcnt vmcnt(7)\n"
"v_mfma_f32_16x16x32_bf16 v[144:147], v[112:115], a[16:19], 0\n"
"v_mfma_f32_16x16x32_bf16 v[148:151], v[112:115], a[20:23], 0\n"
"s_waitcnt vmcnt(6)\n"
"v_mfma_f32_16x16x32_bf16 v[144:147], v[116:119], a[24:27], v[144:147]\n"
"v_mfma_f32_16x16x32_bf16 v[148:151], v[116:119], a[28:31], v[148:151]\n"
"s_waitcnt vmcnt(5)\n"
"v_mfma_f32_16x16x32_bf16 v[144:147], v[120:123], a[32:35], v[144:147]\n"
"v_mfma_f32_16x16x32_bf16 v[148:151], v[120:123], a[36:39], v[148:151]\n"
"s_waitcnt vmcnt(4)\n"
"v_mfma_f32_16x16x32_bf16 v[144:147], v[124:127], a[40:43], v[144:147]\n"
"v_mfma_f32_16x16x32_bf16 v[148:151], v[124:127], a[44:47], v[148:151]\n"
"s_add_u32 s42, s26, s75\n"
"s_add_u32 s42, s42, 524288\n"
"s_add_u32 s43, s42, 4096\n"
"s_add_u32 s24, s24, 1\n"
"v_mov_b32 v5, s24\n"
"s_nop 1\n"
"v_max_f32 v152, v144, v145\n"
"v_max_f32 v154, v148, v149\n"
"v_mov_b32 v248, v152\n"
"v_mov_b32 v249, v154\n"
"s_nop 0\n"
"v_permlane32_swap_b32 v152, v248\n"
"v_permlane32_swap_b32 v154, v249\n"
"v_max_f32 v152, v152, v248\n"
"v_max_f32 v154, v154, v249\n"
"v_mov_b32 v248, v152\n"
"v_mov_b32 v249, v154\n"
"s_nop 0\n"
"v_permlane16_swap_b32 v152, v248\n"
"v_permlane16_swap_b32 v154, v249\n"
"v_max_f32 v152, v152, v248\n"
"v_max_f32 v154, v154, v249\n"
"v_mul_f32 v152, s84, v152\n"
"v_mul_f32 v154, s84, v154\n"
"v_max_f32 v152, v152, v240\n"
"v_max_f32 v154, v154, v242\n"
"v_pk_fma_f32 v[144:145], v[144:145], s[84:85], v[152:153] op_sel_hi:[1,1,0] neg_lo:[0,0,1] neg_hi:[0,0,1]\n"
"v_pk_fma_f32 v[148:149], v[148:149], s[84:85], v[154:155] op_sel_hi:[1,1,0] neg_lo:[0,0,1] neg_hi:[0,0,1]\n"
"v_sub_f32 v240, v240, v152\n"
"v_sub_f32 v242, v242, v154\n"
"v_exp_f32 v144, v144\n"
"v_exp_f32 v145, v145\n"
"v_exp_f32 v148, v148\n"
"v_exp_f32 v149, v149\n"
"v_exp_f32 v240, v240\n"
"v_exp_f32 v242, v242\n"
"v_add_f32 v152, v144, v145\n"
"v_add_f32 v154, v148, v149\n"
"v_fmac_f32 v152, v240, v244\n"
"v_fmac_f32 v154, v242, v246\n"
"v_mov_b32 v248, v152\n"
"v_mov_b32 v249, v154\n"
"s_nop 0\n"
"v_permlane32_swap_b32 v152, v248\n"
"v_permlane32_swap_b32 v154, v249\n"
"v_add_f32 v152, v152, v248\n"
"v_add_f32 v154, v154, v249\n"
"v_mov_b32 v248, v152\n"
"v_mov_b32 v249, v154\n"
"s_nop 0\n"
"v_permlane16_swap_b32 v152, v248\n"
"v_permlane16_swap_b32 v154, v249\n"
"v_add_f32 v152, v152, v248\n"
"v_add_f32 v154, v154, v249\n"
"v_rcp_f32 v152, v152\n"
"v_rcp_f32 v154, v154\n"
"v_pk_mul_f32 v[144:145], v[144:145], v[152:153] op_sel_hi:[1,0]\n"
"v_pk_mul_f32 v[148:149], v[148:149], v[154:155] op_sel_hi:[1,0]\n"
"v_mul_f32 v240, v240, v152\n"
"v_mul_f32 v242, v242, v154\n"
"v_cvt_pk_bf16_f32 v234, v144, v145\n"
"v_cvt_pk_bf16_f32 v10, v148, v149\n"
"v_pk_mul_f32 v[160:161], v[160:161], v[240:241] op_sel_hi:[1,0]\n"
"v_pk_mul_f32 v[162:163], v[162:163], v[240:241] op_sel_hi:[1,0]\n"
"v_pk_mul_f32 v[164:165], v[164:165], v[240:241] op_sel_hi:[1,0]\n"
"v_pk_mul_f32 v[166:167], v[166:167], v[240:241] op_sel_hi:[1,0]\n"
"v_pk_mul_f32 v[192:193], v[192:193], v[242:243] op_sel_hi:[1,0]\n"
"v_pk_mul_f32 v[194:195], v[194:195], v[242:243] op_sel_hi:[1,0]\n"
"v_pk_mul_f32 v[196:197], v[196:197], v[242:243] op_sel_hi:[1,0]\n"
"v_pk_mul_f32 v[198:199], v[198:199], v[242:243] op_sel_hi:[1,0]\n"
"s_waitcnt vmcnt(3)\n"
"v_mfma_f32_16x16x32_bf16 v[160:163], v[128:131], v[234:237], v[160:163]\n"
"v_mfma_f32_16x16x32_bf16 v[164:167], v[128:131], v[232:235], v[164:167]\n"
"v_pk_mul_f32 v[168:169], v[168:169], v[240:241] op_sel_hi:[1,0]\n"
"v_pk_mul_f32 v[170:171], v[170:171], v[240:241] op_sel_hi:[1,0]\n"
"v_pk_mul_f32 v[172:173], v[172:173], v[240:241] op_sel_hi:[1,0]\n"
"v_pk_mul_f32 v[174:175], v[174:175], v[240:241] op_sel_hi:[1,0]\n"
"v_mfma_f32_16x16x32_bf16 v[192:195], v[128:131], v[10:13], v[192:195]\n"
"v_mfma_f32_16x16x32_bf16 v[196:199], v[128:131], v[8:11], v[196:199]\n"
"v_pk_mul_f32 v[200:201], v[200:201], v[242:243] op_sel_hi:[1,0]\n"
"v_pk_mul_f32 v[202:203], v[202:203], v[242:243] op_sel_hi:[1,0]\n"
"v_pk_mul_f32 v[204:205], v[204:205], v[242:243] op_sel_hi:[1,0]\n"
"v_pk_mul_f32 v[206:207], v[206:207], v[242:243] op_sel_hi:[1,0]\n"
"s_waitcnt vmcnt(2)\n"
"v_mfma_f32_16x16x32_bf16 v[168:171], v[132:135], v[234:237], v[168:171]\n"
"v_mfma_f32_16x16x32_bf16 v[172:175], v[132:135], v[232:235], v[172:175]\n"
"v_cvt_pk_bf16_f32 v160, v160, v161\n"
"v_cvt_pk_bf16_f32 v161, v162, v163\n"
"v_cvt_pk_bf16_f32 v162, v164, v165\n"
"v_cvt_pk_bf16_f32 v163, v166, v167\n"
"buffer_store_dwordx4 v[160:163], v0, s[12:15], s42 offen offset:0 sc1\n"
"v_pk_mul_f32 v[176:177], v[176:177], v[240:241] op_sel_hi:[1,0]\n"
"v_pk_mul_f32 v[178:179], v[178:179], v[240:241] op_sel_hi:[1,0]\n"
"v_pk_mul_f32 v[180:181], v[180:181], v[240:241] op_sel_hi:[1,0]\n"
"v_pk_mul_f32 v[182:183], v[182:183], v[240:241] op_sel_hi:[1,0]\n"
"v_mfma_f32_16x16x32_bf16 v[200:203], v[132:135], v[10:13], v[200:203]\n"
"v_mfma_f32_16x16x32_bf16 v[204:207], v[132:135], v[8:11], v[204:207]\n"
"v_cvt_pk_bf16_f32 v192, v192, v193\n"
"v_cvt_pk_bf16_f32 v193, v194, v195\n"
"v_cvt_pk_bf16_f32 v194, v196, v197\n"
"v_cvt_pk_bf16_f32 v195, v198, v199\n"
"buffer_store_dwordx4 v[192:195], v0, s[12:15], s42 offen offset:1024 sc1\n"
"v_pk_mul_f32 v[208:209], v[208:209], v[242:243] op_sel_hi:[1,0]\n"
"v_pk_mul_f32 v[210:211], v[210:211], v[242:243] op_sel_hi:[1,0]\n"
"v_pk_mul_f32 v[212:213], v[212:213], v[242:243] op_sel_hi:[1,0]\n"
"v_pk_mul_f32 v[214:215], v[214:215], v[242:243] op_sel_hi:[1,0]\n"
"s_waitcnt vmcnt(3)\n"
"v_mfma_f32_16x16x32_bf16 v[176:179], v[136:139], v[234:237], v[176:179]\n"
"v_mfma_f32_16x16x32_bf16 v[180:183], v[136:139], v[232:235], v[180:183]\n"
"v_cvt_pk_bf16_f32 v168, v168, v169\n"
"v_cvt_pk_bf16_f32 v169, v170, v171\n"
"v_cvt_pk_bf16_f32 v170, v172, v173\n"
"v_cvt_pk_bf16_f32 v171, v174, v175\n"
"buffer_store_dwordx4 v[168:171], v0, s[12:15], s42 offen offset:2048 sc1\n"
"v_pk_mul_f32 v[184:185], v[184:185], v[240:241] op_sel_hi:[1,0]\n"
"v_pk_mul_f32 v[186:187], v[186:187], v[240:241] op_sel_hi:[1,0]\n"
"v_pk_mul_f32 v[188:189], v[188:189], v[240:241] op_sel_hi:[1,0]\n"
"v_pk_mul_f32 v[190:191], v[190:191], v[240:241] op_sel_hi:[1,0]\n"
"v_mfma_f32_16x16x32_bf16 v[208:211], v[136:139], v[10:13], v[208:211]\n"
"v_mfma_f32_16x16x32_bf16 v[212:215], v[136:139], v[8:11], v[212:215]\n"
"v_cvt_pk_bf16_f32 v200, v200, v201\n"
"v_cvt_pk_bf16_f32 v201, v202, v203\n"
"v_cvt_pk_bf16_f32 v202, v204, v205\n"
"v_cvt_pk_bf16_f32 v203, v206, v207\n"
"buffer_store_dwordx4 v[200:203], v0, s[12:15], s42 offen offset:3072 sc1\n"
"v_pk_mul_f32 v[216:217], v[216:217], v[242:243] op_sel_hi:[1,0]\n"
"v_pk_mul_f32 v[218:219], v[218:219], v[242:243] op_sel_hi:[1,0]\n"
"v_pk_mul_f32 v[220:221], v[220:221], v[242:243] op_sel_hi:[1,0]\n"
"v_pk_mul_f32 v[222:223], v[222:223], v[242:243] op_sel_hi:[1,0]\n"
"s_waitcnt vmcnt(4)\n"
"v_mfma_f32_16x16x32_bf16 v[184:187], v[140:143], v[234:237], v[184:187]\n"
"v_mfma_f32_16x16x32_bf16 v[188:191], v[140:143], v[232:235], v[188:191]\n"
"v_cvt_pk_bf16_f32 v176, v176, v177\n"
"v_cvt_pk_bf16_f32 v177, v178, v179\n"
"v_cvt_pk_bf16_f32 v178, v180, v181\n"
"v_cvt_pk_bf16_f32 v179, v182, v183\n"
"buffer_store_dwordx4 v[176:179], v0, s[12:15], s43 offen offset:0 sc1\n"
"v_mfma_f32_16x16x32_bf16 v[216:219], v[140:143], v[10:13], v[216:219]\n"
"v_mfma_f32_16x16x32_bf16 v[220:223], v[140:143], v[8:11], v[220:223]\n"
"v_cvt_pk_bf16_f32 v208, v208, v209\n"
"v_cvt_pk_bf16_f32 v209, v210, v211\n"
"v_cvt_pk_bf16_f32 v210, v212, v213\n"
"v_cvt_pk_bf16_f32 v211, v214, v215\n"
"buffer_store_dwordx4 v[208:211], v0, s[12:15], s43 offen offset:1024 sc1\n"
"v_cvt_pk_bf16_f32 v184, v184, v185\n"
"v_cvt_pk_bf16_f32 v185, v186, v187\n"
"v_cvt_pk_bf16_f32 v186, v188, v189\n"
"v_cvt_pk_bf16_f32 v187, v190, v191\n"
"buffer_store_dwordx4 v[184:187], v0, s[12:15], s43 offen offset:2048 sc1\n"
"v_cvt_pk_bf16_f32 v216, v216, v217\n"
"v_cvt_pk_bf16_f32 v217, v218, v219\n"
"v_cvt_pk_bf16_f32 v218, v220, v221\n"
"v_cvt_pk_bf16_f32 v219, v222, v223\n"
"buffer_store_dwordx4 v[216:219], v0, s[12:15], s43 offen offset:3072 sc1\n"
"s_waitcnt vmcnt(0)\n"
"buffer_store_dword v5, off, s[12:15], s27 sc1\n"
"s_cmp_lt_u32 s24, 64\n"
"s_cbranch_scc1 A_loop0_%=\n"
"s_waitcnt vmcnt(0)\n"
"s_endpgm\n"
"ROLE5_%=:\n"
"s_load_dwordx2 s[28:29], %0, 0\n"
"s_load_dwordx2 s[64:65], %0, 64\n"
"s_load_dwordx8 s[52:59], %0, 16\n"
"s_load_dwordx4 s[60:63], %0, 48\n"
"s_load_dwordx2 s[12:13], %0, 72\n"
"s_mov_b32 s21, %1\n"
"s_mov_b32 s20, %2\n"
"s_mov_b32 s22, 0x40000000\n"
"s_mov_b32 s23, 0\n"
"v_mbcnt_lo_u32_b32 v10, -1, 0\n"
"v_mbcnt_hi_u32_b32 v10, -1, v10\n"
"v_lshlrev_b32 v0, 4, v10\n"
"v_and_b32 v11, 15, v10\n"
"v_lshrrev_b32 v12, 4, v10\n"
"s_waitcnt lgkmcnt(0)\n"
"s_and_b32 s13, s13, 0xffff\n"
"s_mov_b32 s14, 149946368\n"
"s_mov_b32 s15, 0x20000\n"
"s_cmp_ge_u32 s21, 16\n"
"s_cselect_b32 s68, 4, s20\n"
"s_lshl2_add_u32 s40, s21, s20\n"
"s_sub_u32 s40, s40, 64\n"
"s_cmp_ge_u32 s21, 16\n"
"s_cselect_b32 s69, s40, s21\n"
"s_cmp_eq_u32 s68, 3\n"
"s_cselect_b32 s28, s64, s28\n"
"s_cselect_b32 s29, s65, s29\n"
"s_and_b32 s29, s29, 0xffff\n"
"s_mov_b32 s30, 2621440\n"
"s_mov_b32 s31, 0x20000\n"
"v_lshrrev_b32 v27, 5, v10\n"
"v_and_b32 v28, 1, v12\n"
"v_mov_b32 v29, s22\n"
"v_cmp_eq_u32 s[76:77], 0, v28\n"
"s_mul_i32 s40, s69, 86\n"
"s_lshr_b32 s40, s40, 8\n"
"s_mul_i32 s40, s40, 8704\n"
"s_mul_i32 s41, s69, 2560\n"
"s_add_u32 s40, s40, s41\n"
"v_lshlrev_b32 v30, 5, v11\n"
"v_lshl_add_u32 v30, v27, 4, v30\n"
"v_add_u32 v30, s40, v30\n"
"v_mul_u32_u24 v31, 98304, v28\n"
"v_add_u32 v16, v30, v31\n"
"v_cndmask_b32 v17, v29, v30, s[76:77]\n"
"v_lshl_add_u32 v21, v28, 9, v30\n"
"v_mov_b32 v22, v17\n"
"s_lshl_b32 s41, s69, 1\n"
"v_add_u32 v31, s41, v27\n"
"v_mul_u32_u24 v30, 1280, v31\n"
"v_lshl_add_u32 v30, v11, 4, v30\n"
"v_cndmask_b32 v18, v29, v30, s[76:77]\n"
"v_lshl_add_u32 v23, v28, 8, v30\n"
"v_mov_b32 v24, v18\n"
"v_lshlrev_b32 v30, 9, v11\n"
"v_lshl_add_u32 v30, v31, 4, v30\n"
"v_cndmask_b32 v20, v29, v30, s[76:77]\n"
"v_lshl_add_u32 v19, v28, 14, v30\n"
"v_mov_b32 v25, 64\n"
"v_mov_b32 v26, 0x3727c5ac\n"
"s_mov_b32 s84, 0x3acccccd\n"
"s_lshr_b32 s25, s68, 1\n"
"s_and_b32 s25, s25, 1\n"
"s_mul_i32 s40, s25, 1792\n"
"s_add_u32 s40, s40, 49152\n"
"s_lshl_b32 s41, s25, 4\n"
"s_add_u32 s41, s41, 32768\n"
"s_bitcmp1_b32 s68, 0\n"
"s_cselect_b32 s40, s40, s41\n"
"s_cselect_b32 s42, 14, 32\n"
"v_lshlrev_b32 v30, 7, v10\n"
"v_add_u32 v30, s40, v30\n"
"v_cmp_gt_u32 vcc, s42, v10\n"
"s_nop 1\n"
"v_cndmask_b32 v1, v29, v30, vcc\n"
"s_lshl_b32 s40, s25, 4\n"
"s_add_u32 s40, s40, 288\n"
"s_cmp_eq_u32 s68, 1\n"
"s_cselect_b32 s40, 416, s40\n"
"s_cmp_eq_u32 s68, 4\n"
"s_cselect_b32 s40, 48, s40\n"
"s_add_u32 s40, s40, s69\n"
"s_lshl_b32 s27, s40, 7\n"
"v_lshrrev_b32 v30, 3, v11\n"
"v_cmp_eq_u32 vcc, v30, v27\n"
"v_and_b32 v30, 1, v11\n"
"v_lshlrev_b32 v30, 4, v30\n"
"v_mov_b32 v31, 0x3f80\n"
"v_lshlrev_b32 v30, v30, v31\n"
"v_cndmask_b32 v30, 0, v30, vcc\n"
"v_bfe_u32 v31, v11, 1, 2\n"
"v_cmp_eq_u32 vcc, 0, v31\n"
"s_nop 1\n"
"v_cndmask_b32 v8, 0, v30, vcc\n"
"v_cmp_eq_u32 vcc, 1, v31\n"
"s_nop 1\n"
"v_cndmask_b32 v9, 0, v30, vcc\n"
"v_cmp_eq_u32 vcc, 2, v31\n"
"s_nop 1\n"
"v_cndmask_b32 v10, 0, v30, vcc\n"
"v_cmp_eq_u32 vcc, 3, v31\n"
"s_nop 1\n"
"v_cndmask_b32 v11, 0, v30, vcc\n"
"s_mov_b32 s24, 0\n"
"s_cmp_eq_u32 s68, 4\n"
"s_cbranch_scc1 R4_loop_%=\n"
"R_pol_%=:\n"
"buffer_load_dword v13, off, s[12:15], 0 sc1\n"
"s_waitcnt vmcnt(0)\n"
"v_readfirstlane_b32 s40, v13\n"
"s_add_u32 s23, s23, 1\n"
"s_cmp_ge_u32 s23, 400000\n"
"s_cselect_b32 s40, 16, s40\n"
"s_cmp_eq_u32 s40, 0\n"
"s_cbranch_scc1 R_pol_%=\n"
"s_bitcmp1_b32 s40, 5\n"
"s_cselect_b32 s74, 8192, 0\n"
"s_xor_b32 s75, s74, 8192\n"
"s_bitcmp1_b32 s68, 0\n"
"s_cselect_b32 s40, s74, s74\n"
"s_cselect_b32 s41, s74, s74\n"
"v_add_u32 v1, s40, v1\n"
"s_add_u32 s27, s27, s41\n"
"s_cmp_eq_u32 s68, 1\n"
"s_cbranch_scc1 R1_loop_%=\n"
"s_cmp_eq_u32 s68, 2\n"
"s_cbranch_scc1 R2_loop_%=\n"
"s_cmp_eq_u32 s68, 3\n"
"s_cbranch_scc1 R3_loop_%=\n"
"R0_loop_%=:\n"
"s_lshl_b32 s26, s24, 1\n"
"s_add_u32 s44, s26, 15\n"
"s_and_b32 s44, s44, 31\n"
"s_lshl_b32 s44, s44, 22\n"
"s_and_b32 s26, s26, 31\n"
"s_lshl_b32 s26, s26, 22\n"
"s_add_u32 s45, s26, s75\n"
"s_add_u32 s45, s45, 11534336\n"
"s_add_u32 s46, s26, s74\n"
"s_add_u32 s46, s46, 13959168\n"
"s_add_u32 s47, s26, s75\n"
"s_add_u32 s47, s47, 12419072\n"
"s_mul_i32 s48, s24, 40960\n"
"buffer_load_dwordx4 v[112:115], v18, s[28:31], s48 offen offset:0\n"
"buffer_load_dwordx4 v[116:119], v18, s[28:31], s48 offen offset:256\n"
"buffer_load_dwordx4 v[120:123], v18, s[28:31], s48 offen offset:512\n"
"buffer_load_dwordx4 v[124:127], v18, s[28:31], s48 offen offset:768\n"
"buffer_load_dwordx4 v[128:131], v18, s[28:31], s48 offen offset:1024\n"
"R0_w_%=:\n"
"buffer_load_dwordx4 v[4:7], v1, s[12:15], 0 offen sc1\n"
"s_waitcnt vmcnt(0)\n"
"v_min3_u32 v4, v4, v5, v6\n"
"v_min_u32 v4, v4, v7\n"
"v_cmp_ge_u32 vcc, s24, v4\n"
"s_nop 0\n"
"s_and_b32 s40, vcc_lo, 0xffffffff\n"
"s_cbranch_scc0 R0_wg_%=\n"
"s_sleep 4\n"
"s_add_u32 s23, s23, 1\n"
"s_cmp_lt_u32 s23, 400000\n"
"s_cbranch_scc1 R0_w_%=\n"
"R0_wg_%=:\n"
"s_add_u32 s40, s45, 0\n"
"buffer_load_dwordx4 v[32:35], v16, s[12:15], s40 offen offset:0 sc1\n"
"buffer_load_dwordx4 v[36:39], v16, s[12:15], s40 offen offset:512 sc1\n"
"buffer_load_dwordx4 v[40:43], v16, s[12:15], s40 offen offset:1024 sc1\n"
"buffer_load_dwordx4 v[44:47], v16, s[12:15], s40 offen offset:1536 sc1\n"
"buffer_load_dwordx4 v[48:51], v16, s[12:15], s40 offen offset:2048 sc1\n"
"s_add_u32 s40, s45, 196608\n"
"buffer_load_dwordx4 v[52:55], v16, s[12:15], s40 offen offset:0 sc1\n"
"buffer_load_dwordx4 v[56:59], v16, s[12:15], s40 offen offset:512 sc1\n"
"buffer_load_dwordx4 v[60:63], v16, s[12:15], s40 offen offset:1024 sc1\n"
"buffer_load_dwordx4 v[64:67], v16, s[12:15], s40 offen offset:1536 sc1\n"
"buffer_load_dwordx4 v[68:71], v16, s[12:15], s40 offen offset:2048 sc1\n"
"s_add_u32 s40, s45, 393216\n"
"buffer_load_dwordx4 v[72:75], v16, s[12:15], s40 offen offset:0 sc1\n"
"buffer_load_dwordx4 v[76:79], v16, s[12:15], s40 offen offset:512 sc1\n"
"buffer_load_dwordx4 v[80:83], v16, s[12:15], s40 offen offset:1024 sc1\n"
"buffer_load_dwordx4 v[84:87], v16, s[12:15], s40 offen offset:1536 sc1\n"
"buffer_load_dwordx4 v[88:91], v16, s[12:15], s40 offen offset:2048 sc1\n"
"s_add_u32 s40, s45, 589824\n"
"buffer_load_dwordx4 v[92:95], v16, s[12:15], s40 offen offset:0 sc1\n"
"buffer_load_dwordx4 v[96:99], v16, s[12:15], s40 offen offset:512 sc1\n"
"buffer_load_dwordx4 v[100:103], v16, s[12:15], s40 offen offset:1024 sc1\n"
"buffer_load_dwordx4 v[104:107], v16, s[12:15], s40 offen offset:1536 sc1\n"
"buffer_load_dwordx4 v[108:111], v16, s[12:15], s40 offen offset:2048 sc1\n"
"v_mfma_f32_16x16x32_bf16 v[200:203], v[8:11], v[112:115], 0\n"
"v_mfma_f32_16x16x32_bf16 v[204:207], v[8:11], v[116:119], 0\n"
"v_mfma_f32_16x16x32_bf16 v[208:211], v[8:11], v[120:123], 0\n"
"v_mfma_f32_16x16x32_bf16 v[212:215], v[8:11], v[124:127], 0\n"
"v_mfma_f32_16x16x32_bf16 v[216:219], v[8:11], v[128:131], 0\n"
"s_waitcnt vmcnt(19)\n"
"v_mfma_f32_16x16x32_bf16 v[200:203], v[8:11], v[32:35], v[200:203]\n"
"s_waitcnt vmcnt(18)\n"
"v_mfma_f32_16x16x32_bf16 v[204:207], v[8:11], v[36:39], v[204:207]\n"
"s_waitcnt vmcnt(17)\n"
"v_mfma_f32_16x16x32_bf16 v[208:211], v[8:11], v[40:43], v[208:211]\n"
"s_waitcnt vmcnt(16)\n"
"v_mfma_f32_16x16x32_bf16 v[212:215], v[8:11], v[44:47], v[212:215]\n"
"s_waitcnt vmcnt(15)\n"
"v_mfma_f32_16x16x32_bf16 v[216:219], v[8:11], v[48:51], v[216:219]\n"
"s_waitcnt vmcnt(14)\n"
"v_mfma_f32_16x16x32_bf16 v[200:203], v[8:11], v[52:55], v[200:203]\n"
"s_waitcnt vmcnt(13)\n"
"v_mfma_f32_16x16x32_bf16 v[204:207], v[8:11], v[56:59], v[204:207]\n"
"s_waitcnt vmcnt(12)\n"
"v_mfma_f32_16x16x32_bf16 v[208:211], v[8:11], v[60:63], v[208:211]\n"
"s_waitcnt vmcnt(11)\n"
"v_mfma_f32_16x16x32_bf16 v[212:215], v[8:11], v[64:67], v[212:215]\n"
"s_waitcnt vmcnt(10)\n"
"v_mfma_f32_16x16x32_bf16 v[216:219], v[8:11], v[68:71], v[216:219]\n"
"s_waitcnt vmcnt(9)\n"
"v_mfma_f32_16x16x32_bf16 v[200:203], v[8:11], v[72:75], v[200:203]\n"
"s_waitcnt vmcnt(8)\n"
"v_mfma_f32_16x16x32_bf16 v[204:207], v[8:11], v[76:79], v[204:207]\n"
"s_waitcnt vmcnt(7)\n"
"v_mfma_f32_16x16x32_bf16 v[208:211], v[8:11], v[80:83], v[208:211]\n"
"s_waitcnt vmcnt(6)\n"
"v_mfma_f32_16x16x32_bf16 v[212:215], v[8:11], v[84:87], v[212:215]\n"
"s_waitcnt vmcnt(5)\n"
"v_mfma_f32_16x16x32_bf16 v[216:219], v[8:11], v[88:91], v[216:219]\n"
"s_waitcnt vmcnt(4)\n"
"v_mfma_f32_16x16x32_bf16 v[200:203], v[8:11], v[92:95], v[200:203]\n"
"s_waitcnt vmcnt(3)\n"
"v_mfma_f32_16x16x32_bf16 v[204:207], v[8:11], v[96:99], v[204:207]\n"
"s_waitcnt vmcnt(2)\n"
"v_mfma_f32_16x16x32_bf16 v[208:211], v[8:11], v[100:103], v[208:211]\n"
"s_waitcnt vmcnt(1)\n"
"v_mfma_f32_16x16x32_bf16 v[212:215], v[8:11], v[104:107], v[212:215]\n"
"s_waitcnt vmcnt(0)\n"
"v_mfma_f32_16x16x32_bf16 v[216:219], v[8:11], v[108:111], v[216:219]\n"
"s_add_u32 s24, s24, 1\n"
"v_mov_b32 v5, s24\n"
"v_cvt_pk_bf16_f32 v220, v200, v201\n"
"v_cvt_pk_bf16_f32 v221, v202, v203\n"
"v_cvt_pk_bf16_f32 v222, v204, v205\n"
"v_cvt_pk_bf16_f32 v223, v206, v207\n"
"v_cvt_pk_bf16_f32 v224, v208, v209\n"
"v_cvt_pk_bf16_f32 v225, v210, v211\n"
"v_cvt_pk_bf16_f32 v226, v212, v213\n"
"v_cvt_pk_bf16_f32 v227, v214, v215\n"
"v_cvt_pk_bf16_f32 v228, v216, v217\n"
"v_cvt_pk_bf16_f32 v229, v218, v219\n"
"v_lshlrev_b32 v200, 16, v220\n"
"v_and_b32 v201, 0xffff0000, v220\n"
"v_lshlrev_b32 v202, 16, v221\n"
"v_and_b32 v203, 0xffff0000, v221\n"
"v_lshlrev_b32 v204, 16, v222\n"
"v_and_b32 v205, 0xffff0000, v222\n"
"v_lshlrev_b32 v206, 16, v223\n"
"v_and_b32 v207, 0xffff0000, v223\n"
"v_lshlrev_b32 v208, 16, v224\n"
"v_and_b32 v209, 0xffff0000, v224\n"
"v_lshlrev_b32 v210, 16, v225\n"
"v_and_b32 v211, 0xffff0000, v225\n"
"v_lshlrev_b32 v212, 16, v226\n"
"v_and_b32 v213, 0xffff0000, v226\n"
"v_lshlrev_b32 v214, 16, v227\n"
"v_and_b32 v215, 0xffff0000, v227\n"
"v_lshlrev_b32 v216, 16, v228\n"
"v_and_b32 v217, 0xffff0000, v228\n"
"v_lshlrev_b32 v218, 16, v229\n"
"v_and_b32 v219, 0xffff0000, v229\n"
"v_pk_add_f32 v[244:245], v[200:201], v[202:203]\n"
"v_pk_add_f32 v[246:247], v[204:205], v[206:207]\n"
"v_pk_add_f32 v[244:245], v[244:245], v[208:209]\n"
"v_pk_add_f32 v[246:247], v[246:247], v[210:211]\n"
"v_pk_add_f32 v[244:245], v[244:245], v[212:213]\n"
"v_pk_add_f32 v[246:247], v[246:247], v[214:215]\n"
"v_pk_add_f32 v[244:245], v[244:245], v[216:217]\n"
"v_pk_add_f32 v[246:247], v[246:247], v[218:219]\n"
"v_pk_add_f32 v[244:245], v[244:245], v[246:247]\n"
"v_add_f32 v244, v244, v245\n"
"v_mov_b64 v[230:231], v[228:229]\n"
"v_permlane16_swap_b32 v220, v222\n"
"v_permlane16_swap_b32 v221, v223\n"
"buffer_store_dwordx4 v[220:223], v21, s[12:15], s46 offen offset:0 sc1\n"
"v_permlane16_swap_b32 v224, v226\n"
"v_permlane16_swap_b32 v225, v227\n"
"buffer_store_dwordx4 v[224:227], v21, s[12:15], s46 offen offset:1024 sc1\n"
"v_permlane16_swap_b32 v228, v230\n"
"v_permlane16_swap_b32 v229, v231\n"
"buffer_store_dwordx4 v[228:231], v22, s[12:15], s46 offen offset:2048 sc1\n"
"v_add_f32_dpp v244, v244, v244 quad_perm:[1,0,3,2] row_mask:0xf bank_mask:0xf\n"
"s_nop 1\n"
"v_add_f32_dpp v244, v244, v244 quad_perm:[2,3,0,1] row_mask:0xf bank_mask:0xf\n"
"s_nop 1\n"
"v_add_f32_dpp v244, v244, v244 row_half_mirror row_mask:0xf bank_mask:0xf\n"
"s_nop 1\n"
"v_add_f32_dpp v244, v244, v244 row_mirror row_mask:0xf bank_mask:0xf\n"
"v_mov_b32 v245, v244\n"
"s_nop 1\n"
"v_permlane16_swap_b32 v244, v245\n"
"v_add_f32 v244, v244, v245\n"
"v_mul_f32 v244, 0x3acccccd, v244\n"
"v_pk_add_f32 v[200:201], v[200:201], v[244:245] op_sel_hi:[1,0] neg_lo:[0,1] neg_hi:[0,1]\n"
"v_pk_add_f32 v[202:203], v[202:203], v[244:245] op_sel_hi:[1,0] neg_lo:[0,1] neg_hi:[0,1]\n"
"v_pk_add_f32 v[204:205], v[204:205], v[244:245] op_sel_hi:[1,0] neg_lo:[0,1] neg_hi:[0,1]\n"
"v_pk_add_f32 v[206:207], v[206:207], v[244:245] op_sel_hi:[1,0] neg_lo:[0,1] neg_hi:[0,1]\n"
"v_pk_add_f32 v[208:209], v[208:209], v[244:245] op_sel_hi:[1,0] neg_lo:[0,1] neg_hi:[0,1]\n"
"v_pk_add_f32 v[210:211], v[210:211], v[244:245] op_sel_hi:[1,0] neg_lo:[0,1] neg_hi:[0,1]\n"
"v_pk_add_f32 v[212:213], v[212:213], v[244:245] op_sel_hi:[1,0] neg_lo:[0,1] neg_hi:[0,1]\n"
"v_pk_add_f32 v[214:215], v[214:215], v[244:245] op_sel_hi:[1,0] neg_lo:[0,1] neg_hi:[0,1]\n"
"v_pk_add_f32 v[216:217], v[216:217], v[244:245] op_sel_hi:[1,0] neg_lo:[0,1] neg_hi:[0,1]\n"
"v_pk_add_f32 v[218:219], v[218:219], v[244:245] op_sel_hi:[1,0] neg_lo:[0,1] neg_hi:[0,1]\n"
"v_pk_mul_f32 v[244:245], v[200:201], v[200:201]\n"
"v_pk_mul_f32 v[246:247], v[202:203], v[202:203]\n"
"v_pk_fma_f32 v[244:245], v[204:205], v[204:205], v[244:245]\n"
"v_pk_fma_f32 v[246:247], v[206:207], v[206:207], v[246:247]\n"
"v_pk_fma_f32 v[244:245], v[208:209], v[208:209], v[244:245]\n"
"v_pk_fma_f32 v[246:247], v[210:211], v[210:211], v[246:247]\n"
"v_pk_fma_f32 v[244:245], v[212:213], v[212:213], v[244:245]\n"
"v_pk_fma_f32 v[246:247], v[214:215], v[214:215], v[246:247]\n"
"v_pk_fma_f32 v[244:245], v[216:217], v[216:217], v[244:245]\n"
"v_pk_fma_f32 v[246:247], v[218:219], v[218:219], v[246:247]\n"
"v_pk_add_f32 v[244:245], v[244:245], v[246:247]\n"
"v_add_f32 v244, v244, v245\n"
"s_nop 1\n"
"v_add_f32_dpp v244, v244, v244 quad_perm:[1,0,3,2] row_mask:0xf bank_mask:0xf\n"
"s_nop 1\n"
"v_add_f32_dpp v244, v244, v244 quad_perm:[2,3,0,1] row_mask:0xf bank_mask:0xf\n"
"s_nop 1\n"
"v_add_f32_dpp v244, v244, v244 row_half_mirror row_mask:0xf bank_mask:0xf\n"
"s_nop 1\n"
"v_add_f32_dpp v244, v244, v244 row_mirror row_mask:0xf bank_mask:0xf\n"
"v_mov_b32 v245, v244\n"
"s_nop 1\n"
"v_permlane16_swap_b32 v244, v245\n"
"v_add_f32 v244, v244, v245\n"
"v_fma_f32 v244, v244, s84, v26\n"
"v_rsq_f32 v244, v244\n"
"s_nop 0\n"
"v_pk_mul_f32 v[200:201], v[200:201], v[244:245] op_sel_hi:[1,0]\n"
"v_pk_mul_f32 v[202:203], v[202:203], v[244:245] op_sel_hi:[1,0]\n"
"v_pk_mul_f32 v[204:205], v[204:205], v[244:245] op_sel_hi:[1,0]\n"
"v_pk_mul_f32 v[206:207], v[206:207], v[244:245] op_sel_hi:[1,0]\n"
"v_pk_mul_f32 v[208:209], v[208:209], v[244:245] op_sel_hi:[1,0]\n"
"v_pk_mul_f32 v[210:211], v[210:211], v[244:245] op_sel_hi:[1,0]\n"
"v_pk_mul_f32 v[212:213], v[212:213], v[244:245] op_sel_hi:[1,0]\n"
"v_pk_mul_f32 v[214:215], v[214:215], v[244:245] op_sel_hi:[1,0]\n"
"v_pk_mul_f32 v[216:217], v[216:217], v[244:245] op_sel_hi:[1,0]\n"
"v_pk_mul_f32 v[218:219], v[218:219], v[244:245] op_sel_hi:[1,0]\n"
"v_cvt_pk_bf16_f32 v232, v200, v201\n"
"v_cvt_pk_bf16_f32 v233, v202, v203\n"
"v_cvt_pk_bf16_f32 v234, v204, v205\n"
"v_cvt_pk_bf16_f32 v235, v206, v207\n"
"v_cvt_pk_bf16_f32 v236, v208, v209\n"
"v_cvt_pk_bf16_f32 v237, v210, v211\n"
"v_cvt_pk_bf16_f32 v238, v212, v213\n"
"v_cvt_pk_bf16_f32 v239, v214, v215\n"
"v_cvt_pk_bf16_f32 v240, v216, v217\n"
"v_cvt_pk_bf16_f32 v241, v218, v219\n"
"v_mov_b64 v[242:243], v[240:241]\n"
"v_permlane16_swap_b32 v232, v234\n"
"v_permlane16_swap_b32 v233, v235\n"
"buffer_store_dwordx4 v[232:235], v19, s[12:15], s47 offen offset:0 sc1\n"
"v_permlane16_swap_b32 v236, v238\n"
"v_permlane16_swap_b32 v237, v239\n"
"s_add_u32 s40, s47, 32768\n"
"buffer_store_dwordx4 v[236:239], v19, s[12:15], s40 offen sc1\n"
"v_permlane16_swap_b32 v240, v242\n"
"v_permlane16_swap_b32 v241, v243\n"
"s_add_u32 s40, s47, 65536\n"
"buffer_store_dwordx4 v[240:243], v20, s[12:15], s40 offen sc1\n"
"s_waitcnt vmcnt(0)\n"
"buffer_store_dword v5, off, s[12:15], s27 sc1\n"
"s_cmp_lt_u32 s24, 64\n"
"s_cbranch_scc1 R0_loop_%=\n"
"s_waitcnt vmcnt(0) lgkmcnt(0)\n"
"s_endpgm\n"
"R1_loop_%=:\n"
"s_lshl_b32 s26, s24, 1\n"
"s_add_u32 s44, s26, 15\n"
"s_and_b32 s44, s44, 31\n"
"s_lshl_b32 s44, s44, 22\n"
"s_and_b32 s26, s26, 31\n"
"s_lshl_b32 s26, s26, 22\n"
"s_add_u32 s45, s26, s74\n"
"s_add_u32 s45, s45, 12582912\n"
"s_add_u32 s46, s44, s75\n"
"s_add_u32 s46, s46, 12320768\n"
"s_add_u32 s47, s44, s75\n"
"s_add_u32 s47, s47, 12500992\n"
"s_mul_i32 s48, s24, 40960\n"
"R1_bp_%=:\n"
"ds_read_b32 v4, v25\n"
"s_waitcnt lgkmcnt(0)\n"
"v_readfirstlane_b32 s40, v4\n"
"s_nop 0\n"
"s_add_u32 s40, s40, 12\n"
"s_cmp_gt_u32 s40, s24\n"
"s_cbranch_scc1 R1_bpg_%=\n"
"s_sleep 4\n"
"s_add_u32 s23, s23, 1\n"
"s_cmp_lt_u32 s23, 400000\n"
"s_cbranch_scc1 R1_bp_%=\n"
"R1_bpg_%=:\n"
"R1_w_%=:\n"
"buffer_load_dwordx4 v[4:7], v1, s[12:15], 0 offen sc1\n"
"s_waitcnt vmcnt(0)\n"
"v_min3_u32 v4, v4, v5, v6\n"
"v_min_u32 v4, v4, v7\n"
"v_cmp_ge_u32 vcc, s24, v4\n"
"s_nop 0\n"
"s_and_b32 s40, vcc_lo, 0x3fff\n"
"s_cbranch_scc0 R1_wg_%=\n"
"s_sleep 4\n"
"s_add_u32 s23, s23, 1\n"
"s_cmp_lt_u32 s23, 400000\n"
"s_cbranch_scc1 R1_w_%=\n"
"R1_wg_%=:\n"
"s_add_u32 s40, s45, 0\n"
"buffer_load_dwordx4 v[32:35], v16, s[12:15], s40 offen offset:0 sc1\n"
"buffer_load_dwordx4 v[36:39], v16, s[12:15], s40 offen offset:512 sc1\n"
"buffer_load_dwordx4 v[40:43], v16, s[12:15], s40 offen offset:1024 sc1\n"
"buffer_load_dwordx4 v[44:47], v16, s[12:15], s40 offen offset:1536 sc1\n"
"buffer_load_dwordx4 v[48:51], v16, s[12:15], s40 offen offset:2048 sc1\n"
"s_add_u32 s40, s45, 196608\n"
"buffer_load_dwordx4 v[52:55], v16, s[12:15], s40 offen offset:0 sc1\n"
"buffer_load_dwordx4 v[56:59], v16, s[12:15], s40 offen offset:512 sc1\n"
"buffer_load_dwordx4 v[60:63], v16, s[12:15], s40 offen offset:1024 sc1\n"
"buffer_load_dwordx4 v[64:67], v16, s[12:15], s40 offen offset:1536 sc1\n"
"buffer_load_dwordx4 v[68:71], v16, s[12:15], s40 offen offset:2048 sc1\n"
"s_add_u32 s40, s45, 393216\n"
"buffer_load_dwordx4 v[72:75], v16, s[12:15], s40 offen offset:0 sc1\n"
"buffer_load_dwordx4 v[76:79], v16, s[12:15], s40 offen offset:512 sc1\n"
"buffer_load_dwordx4 v[80:83], v16, s[12:15], s40 offen offset:1024 sc1\n"
"buffer_load_dwordx4 v[84:87], v16, s[12:15], s40 offen offset:1536 sc1\n"
"buffer_load_dwordx4 v[88:91], v16, s[12:15], s40 offen offset:2048 sc1\n"
"s_add_u32 s40, s45, 589824\n"
"buffer_load_dwordx4 v[92:95], v16, s[12:15], s40 offen offset:0 sc1\n"
"buffer_load_dwordx4 v[96:99], v16, s[12:15], s40 offen offset:512 sc1\n"
"buffer_load_dwordx4 v[100:103], v16, s[12:15], s40 offen offset:1024 sc1\n"
"buffer_load_dwordx4 v[104:107], v16, s[12:15], s40 offen offset:1536 sc1\n"
"buffer_load_dwordx4 v[108:111], v16, s[12:15], s40 offen offset:2048 sc1\n"
"s_add_u32 s40, s45, 786432\n"
"buffer_load_dwordx4 v[112:115], v16, s[12:15], s40 offen offset:0 sc1\n"
"buffer_load_dwordx4 v[116:119], v16, s[12:15], s40 offen offset:512 sc1\n"
"buffer_load_dwordx4 v[120:123], v16, s[12:15], s40 offen offset:1024 sc1\n"
"buffer_load_dwordx4 v[124:127], v16, s[12:15], s40 offen offset:1536 sc1\n"
"buffer_load_dwordx4 v[128:131], v16, s[12:15], s40 offen offset:2048 sc1\n"
"s_add_u32 s40, s45, 983040\n"
"buffer_load_dwordx4 v[132:135], v16, s[12:15], s40 offen offset:0 sc1\n"
"buffer_load_dwordx4 v[136:139], v16, s[12:15], s40 offen offset:512 sc1\n"
"buffer_load_dwordx4 v[140:143], v16, s[12:15], s40 offen offset:1024 sc1\n"
"buffer_load_dwordx4 v[144:147], v16, s[12:15], s40 offen offset:1536 sc1\n"
"buffer_load_dwordx4 v[148:151], v16, s[12:15], s40 offen offset:2048 sc1\n"
"s_add_u32 s40, s45, 1179648\n"
"buffer_load_dwordx4 v[152:155], v16, s[12:15], s40 offen offset:0 sc1\n"
"buffer_load_dwordx4 v[156:159], v16, s[12:15], s40 offen offset:512 sc1\n"
"buffer_load_dwordx4 v[160:163], v16, s[12:15], s40 offen offset:1024 sc1\n"
"buffer_load_dwordx4 v[164:167], v16, s[12:15], s40 offen offset:1536 sc1\n"
"buffer_load_dwordx4 v[168:171], v16, s[12:15], s40 offen offset:2048 sc1\n"
"s_add_u32 s40, s45, 1376256\n"
"buffer_load_dwordx4 v[172:175], v17, s[12:15], s40 offen offset:0 sc1\n"
"buffer_load_dwordx4 v[176:179], v17, s[12:15], s40 offen offset:512 sc1\n"
"buffer_load_dwordx4 v[180:183], v17, s[12:15], s40 offen offset:1024 sc1\n"
"buffer_load_dwordx4 v[184:187], v17, s[12:15], s40 offen offset:1536 sc1\n"
"buffer_load_dwordx4 v[188:191], v17, s[12:15], s40 offen offset:2048 sc1\n"
"s_waitcnt vmcnt(39)\n"
"v_mfma_f32_16x16x32_bf16 v[200:203], v[8:11], v[32:35], 0\n"
"s_waitcnt vmcnt(38)\n"
"v_mfma_f32_16x16x32_bf16 v[204:207], v[8:11], v[36:39], 0\n"
"s_waitcnt vmcnt(37)\n"
"v_mfma_f32_16x16x32_bf16 v[208:211], v[8:11], v[40:43], 0\n"
"s_waitcnt vmcnt(36)\n"
"v_mfma_f32_16x16x32_bf16 v[212:215], v[8:11], v[44:47], 0\n"
"s_waitcnt vmcnt(35)\n"
"v_mfma_f32_16x16x32_bf16 v[216:219], v[8:11], v[48:51], 0\n"
"s_waitcnt vmcnt(34)\n"
"v_mfma_f32_16x16x32_bf16 v[200:203], v[8:11], v[52:55], v[200:203]\n"
"s_waitcnt vmcnt(33)\n"
"v_mfma_f32_16x16x32_bf16 v[204:207], v[8:11], v[56:59], v[204:207]\n"
"s_waitcnt vmcnt(32)\n"
"v_mfma_f32_16x16x32_bf16 v[208:211], v[8:11], v[60:63], v[208:211]\n"
"s_waitcnt vmcnt(31)\n"
"v_mfma_f32_16x16x32_bf16 v[212:215], v[8:11], v[64:67], v[212:215]\n"
"s_waitcnt vmcnt(30)\n"
"v_mfma_f32_16x16x32_bf16 v[216:219], v[8:11], v[68:71], v[216:219]\n"
"s_waitcnt vmcnt(29)\n"
"v_mfma_f32_16x16x32_bf16 v[200:203], v[8:11], v[72:75], v[200:203]\n"
"s_waitcnt vmcnt(28)\n"
"v_mfma_f32_16x16x32_bf16 v[204:207], v[8:11], v[76:79], v[204:207]\n"
"s_waitcnt vmcnt(27)\n"
"v_mfma_f32_16x16x32_bf16 v[208:211], v[8:11], v[80:83], v[208:211]\n"
"s_waitcnt vmcnt(26)\n"
"v_mfma_f32_16x16x32_bf16 v[212:215], v[8:11], v[84:87], v[212:215]\n"
"s_waitcnt vmcnt(25)\n"
"v_mfma_f32_16x16x32_bf16 v[216:219], v[8:11], v[88:91], v[216:219]\n"
"s_waitcnt vmcnt(24)\n"
"v_mfma_f32_16x16x32_bf16 v[200:203], v[8:11], v[92:95], v[200:203]\n"
"s_waitcnt vmcnt(23)\n"
"v_mfma_f32_16x16x32_bf16 v[204:207], v[8:11], v[96:99], v[204:207]\n"
"s_waitcnt vmcnt(22)\n"
"v_mfma_f32_16x16x32_bf16 v[208:211], v[8:11], v[100:103], v[208:211]\n"
"s_waitcnt vmcnt(21)\n"
"v_mfma_f32_16x16x32_bf16 v[212:215], v[8:11], v[104:107], v[212:215]\n"
"s_waitcnt vmcnt(20)\n"
"v_mfma_f32_16x16x32_bf16 v[216:219], v[8:11], v[108:111], v[216:219]\n"
"s_waitcnt vmcnt(19)\n"
"v_mfma_f32_16x16x32_bf16 v[200:203], v[8:11], v[112:115], v[200:203]\n"
"s_waitcnt vmcnt(18)\n"
"v_mfma_f32_16x16x32_bf16 v[204:207], v[8:11], v[116:119], v[204:207]\n"
"s_waitcnt vmcnt(17)\n"
"v_mfma_f32_16x16x32_bf16 v[208:211], v[8:11], v[120:123], v[208:211]\n"
"s_waitcnt vmcnt(16)\n"
"v_mfma_f32_16x16x32_bf16 v[212:215], v[8:11], v[124:127], v[212:215]\n"
"s_waitcnt vmcnt(15)\n"
"v_mfma_f32_16x16x32_bf16 v[216:219], v[8:11], v[128:131], v[216:219]\n"
"s_waitcnt vmcnt(14)\n"
"v_mfma_f32_16x16x32_bf16 v[200:203], v[8:11], v[132:135], v[200:203]\n"
"s_waitcnt vmcnt(13)\n"
"v_mfma_f32_16x16x32_bf16 v[204:207], v[8:11], v[136:139], v[204:207]\n"
"s_waitcnt vmcnt(12)\n"
"v_mfma_f32_16x16x32_bf16 v[208:211], v[8:11], v[140:143], v[208:211]\n"
"s_waitcnt vmcnt(11)\n"
"v_mfma_f32_16x16x32_bf16 v[212:215], v[8:11], v[144:147], v[212:215]\n"
"s_waitcnt vmcnt(10)\n"
"v_mfma_f32_16x16x32_bf16 v[216:219], v[8:11], v[148:151], v[216:219]\n"
"s_waitcnt vmcnt(9)\n"
"v_mfma_f32_16x16x32_bf16 v[200:203], v[8:11], v[152:155], v[200:203]\n"
"s_waitcnt vmcnt(8)\n"
"v_mfma_f32_16x16x32_bf16 v[204:207], v[8:11], v[156:159], v[204:207]\n"
"s_waitcnt vmcnt(7)\n"
"v_mfma_f32_16x16x32_bf16 v[208:211], v[8:11], v[160:163], v[208:211]\n"
"s_waitcnt vmcnt(6)\n"
"v_mfma_f32_16x16x32_bf16 v[212:215], v[8:11], v[164:167], v[212:215]\n"
"s_waitcnt vmcnt(5)\n"
"v_mfma_f32_16x16x32_bf16 v[216:219], v[8:11], v[168:171], v[216:219]\n"
"s_waitcnt vmcnt(4)\n"
"v_mfma_f32_16x16x32_bf16 v[200:203], v[8:11], v[172:175], v[200:203]\n"
"s_waitcnt vmcnt(3)\n"
"v_mfma_f32_16x16x32_bf16 v[204:207], v[8:11], v[176:179], v[204:207]\n"
"s_waitcnt vmcnt(2)\n"
"v_mfma_f32_16x16x32_bf16 v[208:211], v[8:11], v[180:183], v[208:211]\n"
"s_waitcnt vmcnt(1)\n"
"v_mfma_f32_16x16x32_bf16 v[212:215], v[8:11], v[184:187], v[212:215]\n"
"s_waitcnt vmcnt(0)\n"
"v_mfma_f32_16x16x32_bf16 v[216:219], v[8:11], v[188:191], v[216:219]\n"
"s_add_u32 s24, s24, 1\n"
"v_mov_b32 v5, s24\n"
"v_cvt_pk_bf16_f32 v220, v200, v201\n"
"v_cvt_pk_bf16_f32 v221, v202, v203\n"
"v_cvt_pk_bf16_f32 v222, v204, v205\n"
"v_cvt_pk_bf16_f32 v223, v206, v207\n"
"v_cvt_pk_bf16_f32 v224, v208, v209\n"
"v_cvt_pk_bf16_f32 v225, v210, v211\n"
"v_cvt_pk_bf16_f32 v226, v212, v213\n"
"v_cvt_pk_bf16_f32 v227, v214, v215\n"
"v_cvt_pk_bf16_f32 v228, v216, v217\n"
"v_cvt_pk_bf16_f32 v229, v218, v219\n"
"v_lshlrev_b32 v200, 16, v220\n"
"v_and_b32 v201, 0xffff0000, v220\n"
"v_lshlrev_b32 v202, 16, v221\n"
"v_and_b32 v203, 0xffff0000, v221\n"
"v_lshlrev_b32 v204, 16, v222\n"
"v_and_b32 v205, 0xffff0000, v222\n"
"v_lshlrev_b32 v206, 16, v223\n"
"v_and_b32 v207, 0xffff0000, v223\n"
"v_lshlrev_b32 v208, 16, v224\n"
"v_and_b32 v209, 0xffff0000, v224\n"
"v_lshlrev_b32 v210, 16, v225\n"
"v_and_b32 v211, 0xffff0000, v225\n"
"v_lshlrev_b32 v212, 16, v226\n"
"v_and_b32 v213, 0xffff0000, v226\n"
"v_lshlrev_b32 v214, 16, v227\n"
"v_and_b32 v215, 0xffff0000, v227\n"
"v_lshlrev_b32 v216, 16, v228\n"
"v_and_b32 v217, 0xffff0000, v228\n"
"v_lshlrev_b32 v218, 16, v229\n"
"v_and_b32 v219, 0xffff0000, v229\n"
"v_pk_add_f32 v[244:245], v[200:201], v[202:203]\n"
"v_pk_add_f32 v[246:247], v[204:205], v[206:207]\n"
"v_pk_add_f32 v[244:245], v[244:245], v[208:209]\n"
"v_pk_add_f32 v[246:247], v[246:247], v[210:211]\n"
"v_pk_add_f32 v[244:245], v[244:245], v[212:213]\n"
"v_pk_add_f32 v[246:247], v[246:247], v[214:215]\n"
"v_pk_add_f32 v[244:245], v[244:245], v[216:217]\n"
"v_pk_add_f32 v[246:247], v[246:247], v[218:219]\n"
"v_pk_add_f32 v[244:245], v[244:245], v[246:247]\n"
"v_add_f32 v244, v244, v245\n"
"v_mov_b64 v[230:231], v[228:229]\n"
"v_permlane16_swap_b32 v220, v222\n"
"v_permlane16_swap_b32 v221, v223\n"
"buffer_store_dwordx4 v[220:223], v21, s[12:15], s46 offen offset:0 sc1\n"
"v_permlane16_swap_b32 v224, v226\n"
"v_permlane16_swap_b32 v225, v227\n"
"buffer_store_dwordx4 v[224:227], v21, s[12:15], s46 offen offset:1024 sc1\n"
"v_permlane16_swap_b32 v228, v230\n"
"v_permlane16_swap_b32 v229, v231\n"
"buffer_store_dwordx4 v[228:231], v22, s[12:15], s46 offen offset:2048 sc1\n"
"v_add_f32_dpp v244, v244, v244 quad_perm:[1,0,3,2] row_mask:0xf bank_mask:0xf\n"
"s_nop 1\n"
"v_add_f32_dpp v244, v244, v244 quad_perm:[2,3,0,1] row_mask:0xf bank_mask:0xf\n"
"s_nop 1\n"
"v_add_f32_dpp v244, v244, v244 row_half_mirror row_mask:0xf bank_mask:0xf\n"
"s_nop 1\n"
"v_add_f32_dpp v244, v244, v244 row_mirror row_mask:0xf bank_mask:0xf\n"
"v_mov_b32 v245, v244\n"
"s_nop 1\n"
"v_permlane16_swap_b32 v244, v245\n"
"v_add_f32 v244, v244, v245\n"
"v_mul_f32 v244, 0x3acccccd, v244\n"
"v_pk_add_f32 v[200:201], v[200:201], v[244:245] op_sel_hi:[1,0] neg_lo:[0,1] neg_hi:[0,1]\n"
"v_pk_add_f32 v[202:203], v[202:203], v[244:245] op_sel_hi:[1,0] neg_lo:[0,1] neg_hi:[0,1]\n"
"v_pk_add_f32 v[204:205], v[204:205], v[244:245] op_sel_hi:[1,0] neg_lo:[0,1] neg_hi:[0,1]\n"
"v_pk_add_f32 v[206:207], v[206:207], v[244:245] op_sel_hi:[1,0] neg_lo:[0,1] neg_hi:[0,1]\n"
"v_pk_add_f32 v[208:209], v[208:209], v[244:245] op_sel_hi:[1,0] neg_lo:[0,1] neg_hi:[0,1]\n"
"v_pk_add_f32 v[210:211], v[210:211], v[244:245] op_sel_hi:[1,0] neg_lo:[0,1] neg_hi:[0,1]\n"
"v_pk_add_f32 v[212:213], v[212:213], v[244:245] op_sel_hi:[1,0] neg_lo:[0,1] neg_hi:[0,1]\n"
"v_pk_add_f32 v[214:215], v[214:215], v[244:245] op_sel_hi:[1,0] neg_lo:[0,1] neg_hi:[0,1]\n"
"v_pk_add_f32 v[216:217], v[216:217], v[244:245] op_sel_hi:[1,0] neg_lo:[0,1] neg_hi:[0,1]\n"
"v_pk_add_f32 v[218:219], v[218:219], v[244:245] op_sel_hi:[1,0] neg_lo:[0,1] neg_hi:[0,1]\n"
"v_pk_mul_f32 v[244:245], v[200:201], v[200:201]\n"
"v_pk_mul_f32 v[246:247], v[202:203], v[202:203]\n"
"v_pk_fma_f32 v[244:245], v[204:205], v[204:205], v[244:245]\n"
"v_pk_fma_f32 v[246:247], v[206:207], v[206:207], v[246:247]\n"
"v_pk_fma_f32 v[244:245], v[208:209], v[208:209], v[244:245]\n"
"v_pk_fma_f32 v[246:247], v[210:211], v[210:211], v[246:247]\n"
"v_pk_fma_f32 v[244:245], v[212:213], v[212:213], v[244:245]\n"
"v_pk_fma_f32 v[246:247], v[214:215], v[214:215], v[246:247]\n"
"v_pk_fma_f32 v[244:245], v[216:217], v[216:217], v[244:245]\n"
"v_pk_fma_f32 v[246:247], v[218:219], v[218:219], v[246:247]\n"
"v_pk_add_f32 v[244:245], v[244:245], v[246:247]\n"
"v_add_f32 v244, v244, v245\n"
"s_nop 1\n"
"v_add_f32_dpp v244, v244, v244 quad_perm:[1,0,3,2] row_mask:0xf bank_mask:0xf\n"
"s_nop 1\n"
"v_add_f32_dpp v244, v244, v244 quad_perm:[2,3,0,1] row_mask:0xf bank_mask:0xf\n"
"s_nop 1\n"
"v_add_f32_dpp v244, v244, v244 row_half_mirror row_mask:0xf bank_mask:0xf\n"
"s_nop 1\n"
"v_add_f32_dpp v244, v244, v244 row_mirror row_mask:0xf bank_mask:0xf\n"
"v_mov_b32 v245, v244\n"
"s_nop 1\n"
"v_permlane16_swap_b32 v244, v245\n"
"v_add_f32 v244, v244, v245\n"
"v_fma_f32 v244, v244, s84, v26\n"
"v_rsq_f32 v244, v244\n"
"s_nop 0\n"
"v_pk_mul_f32 v[200:201], v[200:201], v[244:245] op_sel_hi:[1,0]\n"
"v_pk_mul_f32 v[202:203], v[202:203], v[244:245] op_sel_hi:[1,0]\n"
"v_pk_mul_f32 v[204:205], v[204:205], v[244:245] op_sel_hi:[1,0]\n"
"v_pk_mul_f32 v[206:207], v[206:207], v[244:245] op_sel_hi:[1,0]\n"
"v_pk_mul_f32 v[208:209], v[208:209], v[244:245] op_sel_hi:[1,0]\n"
"v_pk_mul_f32 v[210:211], v[210:211], v[244:245] op_sel_hi:[1,0]\n"
"v_pk_mul_f32 v[212:213], v[212:213], v[244:245] op_sel_hi:[1,0]\n"
"v_pk_mul_f32 v[214:215], v[214:215], v[244:245] op_sel_hi:[1,0]\n"
"v_pk_mul_f32 v[216:217], v[216:217], v[244:245] op_sel_hi:[1,0]\n"
"v_pk_mul_f32 v[218:219], v[218:219], v[244:245] op_sel_hi:[1,0]\n"
"v_cvt_pk_bf16_f32 v232, v200, v201\n"
"v_cvt_pk_bf16_f32 v233, v202, v203\n"
"v_cvt_pk_bf16_f32 v234, v204, v205\n"
"v_cvt_pk_bf16_f32 v235, v206, v207\n"
"v_cvt_pk_bf16_f32 v236, v208, v209\n"
"v_cvt_pk_bf16_f32 v237, v210, v211\n"
"v_cvt_pk_bf16_f32 v238, v212, v213\n"
"v_cvt_pk_bf16_f32 v239, v214, v215\n"
"v_cvt_pk_bf16_f32 v240, v216, v217\n"
"v_cvt_pk_bf16_f32 v241, v218, v219\n"
"v_mov_b64 v[242:243], v[240:241]\n"
"v_permlane16_swap_b32 v232, v234\n"
"v_permlane16_swap_b32 v233, v235\n"
"buffer_store_dwordx4 v[232:235], v19, s[12:15], s47 offen offset:0 sc1\n"
"v_permlane16_swap_b32 v236, v238\n"
"v_permlane16_swap_b32 v237, v239\n"
"s_add_u32 s40, s47, 32768\n"
"buffer_store_dwordx4 v[236:239], v19, s[12:15], s40 offen sc1\n"
"v_permlane16_swap_b32 v240, v242\n"
"v_permlane16_swap_b32 v241, v243\n"
"s_add_u32 s40, s47, 65536\n"
"buffer_store_dwordx4 v[240:243], v20, s[12:15], s40 offen sc1\n"
"s_waitcnt vmcnt(0)\n"
"buffer_store_dword v5, off, s[12:15], s27 sc1\n"
"s_cmp_lt_u32 s24, 64\n"
"s_cbranch_scc1 R1_loop_%=\n"
"s_waitcnt vmcnt(0) lgkmcnt(0)\n"
"s_endpgm\n"
"R2_loop_%=:\n"
"s_lshl_b32 s26, s24, 1\n"
"s_add_u32 s44, s26, 15\n"
"s_and_b32 s44, s44, 31\n"
"s_lshl_b32 s44, s44, 22\n"
"s_mov_b32 s26, s44\n"
"s_add_u32 s45, s26, s75\n"
"s_add_u32 s45, s45, 11534336\n"
"s_add_u32 s46, s26, s74\n"
"s_add_u32 s46, s46, 13959168\n"
"s_add_u32 s47, s26, s75\n"
"s_add_u32 s47, s47, 12419072\n"
"s_mul_i32 s48, s24, 40960\n"
"R2_w_%=:\n"
"buffer_load_dwordx4 v[4:7], v1, s[12:15], 0 offen sc1\n"
"s_waitcnt vmcnt(0)\n"
"v_min3_u32 v4, v4, v5, v6\n"
"v_min_u32 v4, v4, v7\n"
"v_cmp_ge_u32 vcc, s24, v4\n"
"s_nop 0\n"
"s_and_b32 s40, vcc_lo, 0xffffffff\n"
"s_cbranch_scc0 R2_wg_%=\n"
"s_sleep 4\n"
"s_add_u32 s23, s23, 1\n"
"s_cmp_lt_u32 s23, 400000\n"
"s_cbranch_scc1 R2_w_%=\n"
"R2_wg_%=:\n"
"s_add_u32 s40, s45, 0\n"
"buffer_load_dwordx4 v[32:35], v16, s[12:15], s40 offen offset:0 sc1\n"
"buffer_load_dwordx4 v[36:39], v16, s[12:15], s40 offen offset:512 sc1\n"
"buffer_load_dwordx4 v[40:43], v16, s[12:15], s40 offen offset:1024 sc1\n"
"buffer_load_dwordx4 v[44:47], v16, s[12:15], s40 offen offset:1536 sc1\n"
"buffer_load_dwordx4 v[48:51], v16, s[12:15], s40 offen offset:2048 sc1\n"
"s_add_u32 s40, s45, 196608\n"
"buffer_load_dwordx4 v[52:55], v16, s[12:15], s40 offen offset:0 sc1\n"
"buffer_load_dwordx4 v[56:59], v16, s[12:15], s40 offen offset:512 sc1\n"
"buffer_load_dwordx4 v[60:63], v16, s[12:15], s40 offen offset:1024 sc1\n"
"buffer_load_dwordx4 v[64:67], v16, s[12:15], s40 offen offset:1536 sc1\n"
"buffer_load_dwordx4 v[68:71], v16, s[12:15], s40 offen offset:2048 sc1\n"
"s_add_u32 s40, s45, 393216\n"
"buffer_load_dwordx4 v[72:75], v16, s[12:15], s40 offen offset:0 sc1\n"
"buffer_load_dwordx4 v[76:79], v16, s[12:15], s40 offen offset:512 sc1\n"
"buffer_load_dwordx4 v[80:83], v16, s[12:15], s40 offen offset:1024 sc1\n"
"buffer_load_dwordx4 v[84:87], v16, s[12:15], s40 offen offset:1536 sc1\n"
"buffer_load_dwordx4 v[88:91], v16, s[12:15], s40 offen offset:2048 sc1\n"
"s_add_u32 s40, s45, 589824\n"
"buffer_load_dwordx4 v[92:95], v16, s[12:15], s40 offen offset:0 sc1\n"
"buffer_load_dwordx4 v[96:99], v16, s[12:15], s40 offen offset:512 sc1\n"
"buffer_load_dwordx4 v[100:103], v16, s[12:15], s40 offen offset:1024 sc1\n"
"buffer_load_dwordx4 v[104:107], v16, s[12:15], s40 offen offset:1536 sc1\n"
"buffer_load_dwordx4 v[108:111], v16, s[12:15], s40 offen offset:2048 sc1\n"
"s_add_u32 s40, s45, 786432\n"
"buffer_load_dwordx4 v[112:115], v17, s[12:15], s40 offen offset:0 sc1\n"
"buffer_load_dwordx4 v[116:119], v17, s[12:15], s40 offen offset:512 sc1\n"
"buffer_load_dwordx4 v[120:123], v17, s[12:15], s40 offen offset:1024 sc1\n"
"buffer_load_dwordx4 v[124:127], v17, s[12:15], s40 offen offset:1536 sc1\n"
"buffer_load_dwordx4 v[128:131], v17, s[12:15], s40 offen offset:2048 sc1\n"
"s_waitcnt vmcnt(24)\n"
"v_mfma_f32_16x16x32_bf16 v[200:203], v[8:11], v[32:35], 0\n"
"s_waitcnt vmcnt(23)\n"
"v_mfma_f32_16x16x32_bf16 v[204:207], v[8:11], v[36:39], 0\n"
"s_waitcnt vmcnt(22)\n"
"v_mfma_f32_16x16x32_bf16 v[208:211], v[8:11], v[40:43], 0\n"
"s_waitcnt vmcnt(21)\n"
"v_mfma_f32_16x16x32_bf16 v[212:215], v[8:11], v[44:47], 0\n"
"s_waitcnt vmcnt(20)\n"
"v_mfma_f32_16x16x32_bf16 v[216:219], v[8:11], v[48:51], 0\n"
"s_waitcnt vmcnt(19)\n"
"v_mfma_f32_16x16x32_bf16 v[200:203], v[8:11], v[52:55], v[200:203]\n"
"s_waitcnt vmcnt(18)\n"
"v_mfma_f32_16x16x32_bf16 v[204:207], v[8:11], v[56:59], v[204:207]\n"
"s_waitcnt vmcnt(17)\n"
"v_mfma_f32_16x16x32_bf16 v[208:211], v[8:11], v[60:63], v[208:211]\n"
"s_waitcnt vmcnt(16)\n"
"v_mfma_f32_16x16x32_bf16 v[212:215], v[8:11], v[64:67], v[212:215]\n"
"s_waitcnt vmcnt(15)\n"
"v_mfma_f32_16x16x32_bf16 v[216:219], v[8:11], v[68:71], v[216:219]\n"
"s_waitcnt vmcnt(14)\n"
"v_mfma_f32_16x16x32_bf16 v[200:203], v[8:11], v[72:75], v[200:203]\n"
"s_waitcnt vmcnt(13)\n"
"v_mfma_f32_16x16x32_bf16 v[204:207], v[8:11], v[76:79], v[204:207]\n"
"s_waitcnt vmcnt(12)\n"
"v_mfma_f32_16x16x32_bf16 v[208:211], v[8:11], v[80:83], v[208:211]\n"
"s_waitcnt vmcnt(11)\n"
"v_mfma_f32_16x16x32_bf16 v[212:215], v[8:11], v[84:87], v[212:215]\n"
"s_waitcnt vmcnt(10)\n"
"v_mfma_f32_16x16x32_bf16 v[216:219], v[8:11], v[88:91], v[216:219]\n"
"s_waitcnt vmcnt(9)\n"
"v_mfma_f32_16x16x32_bf16 v[200:203], v[8:11], v[92:95], v[200:203]\n"
"s_waitcnt vmcnt(8)\n"
"v_mfma_f32_16x16x32_bf16 v[204:207], v[8:11], v[96:99], v[204:207]\n"
"s_waitcnt vmcnt(7)\n"
"v_mfma_f32_16x16x32_bf16 v[208:211], v[8:11], v[100:103], v[208:211]\n"
"s_waitcnt vmcnt(6)\n"
"v_mfma_f32_16x16x32_bf16 v[212:215], v[8:11], v[104:107], v[212:215]\n"
"s_waitcnt vmcnt(5)\n"
"v_mfma_f32_16x16x32_bf16 v[216:219], v[8:11], v[108:111], v[216:219]\n"
"s_waitcnt vmcnt(4)\n"
"v_mfma_f32_16x16x32_bf16 v[200:203], v[8:11], v[112:115], v[200:203]\n"
"s_waitcnt vmcnt(3)\n"
"v_mfma_f32_16x16x32_bf16 v[204:207], v[8:11], v[116:119], v[204:207]\n"
"s_waitcnt vmcnt(2)\n"
"v_mfma_f32_16x16x32_bf16 v[208:211], v[8:11], v[120:123], v[208:211]\n"
"s_waitcnt vmcnt(1)\n"
"v_mfma_f32_16x16x32_bf16 v[212:215], v[8:11], v[124:127], v[212:215]\n"
"s_waitcnt vmcnt(0)\n"
"v_mfma_f32_16x16x32_bf16 v[216:219], v[8:11], v[128:131], v[216:219]\n"
"s_add_u32 s24, s24, 1\n"
"v_mov_b32 v5, s24\n"
"v_cvt_pk_bf16_f32 v220, v200, v201\n"
"v_cvt_pk_bf16_f32 v221, v202, v203\n"
"v_cvt_pk_bf16_f32 v222, v204, v205\n"
"v_cvt_pk_bf16_f32 v223, v206, v207\n"
"v_cvt_pk_bf16_f32 v224, v208, v209\n"
"v_cvt_pk_bf16_f32 v225, v210, v211\n"
"v_cvt_pk_bf16_f32 v226, v212, v213\n"
"v_cvt_pk_bf16_f32 v227, v214, v215\n"
"v_cvt_pk_bf16_f32 v228, v216, v217\n"
"v_cvt_pk_bf16_f32 v229, v218, v219\n"
"v_lshlrev_b32 v200, 16, v220\n"
"v_and_b32 v201, 0xffff0000, v220\n"
"v_lshlrev_b32 v202, 16, v221\n"
"v_and_b32 v203, 0xffff0000, v221\n"
"v_lshlrev_b32 v204, 16, v222\n"
"v_and_b32 v205, 0xffff0000, v222\n"
"v_lshlrev_b32 v206, 16, v223\n"
"v_and_b32 v207, 0xffff0000, v223\n"
"v_lshlrev_b32 v208, 16, v224\n"
"v_and_b32 v209, 0xffff0000, v224\n"
"v_lshlrev_b32 v210, 16, v225\n"
"v_and_b32 v211, 0xffff0000, v225\n"
"v_lshlrev_b32 v212, 16, v226\n"
"v_and_b32 v213, 0xffff0000, v226\n"
"v_lshlrev_b32 v214, 16, v227\n"
"v_and_b32 v215, 0xffff0000, v227\n"
"v_lshlrev_b32 v216, 16, v228\n"
"v_and_b32 v217, 0xffff0000, v228\n"
"v_lshlrev_b32 v218, 16, v229\n"
"v_and_b32 v219, 0xffff0000, v229\n"
"v_pk_add_f32 v[244:245], v[200:201], v[202:203]\n"
"v_pk_add_f32 v[246:247], v[204:205], v[206:207]\n"
"v_pk_add_f32 v[244:245], v[244:245], v[208:209]\n"
"v_pk_add_f32 v[246:247], v[246:247], v[210:211]\n"
"v_pk_add_f32 v[244:245], v[244:245], v[212:213]\n"
"v_pk_add_f32 v[246:247], v[246:247], v[214:215]\n"
"v_pk_add_f32 v[244:245], v[244:245], v[216:217]\n"
"v_pk_add_f32 v[246:247], v[246:247], v[218:219]\n"
"v_pk_add_f32 v[244:245], v[244:245], v[246:247]\n"
"v_add_f32 v244, v244, v245\n"
"v_mov_b64 v[230:231], v[228:229]\n"
"v_permlane16_swap_b32 v220, v222\n"
"v_permlane16_swap_b32 v221, v223\n"
"buffer_store_dwordx4 v[220:223], v21, s[12:15], s46 offen offset:0 sc1\n"
"v_permlane16_swap_b32 v224, v226\n"
"v_permlane16_swap_b32 v225, v227\n"
"buffer_store_dwordx4 v[224:227], v21, s[12:15], s46 offen offset:1024 sc1\n"
"v_permlane16_swap_b32 v228, v230\n"
"v_permlane16_swap_b32 v229, v231\n"
"buffer_store_dwordx4 v[228:231], v22, s[12:15], s46 offen offset:2048 sc1\n"
"v_add_f32_dpp v244, v244, v244 quad_perm:[1,0,3,2] row_mask:0xf bank_mask:0xf\n"
"s_nop 1\n"
"v_add_f32_dpp v244, v244, v244 quad_perm:[2,3,0,1] row_mask:0xf bank_mask:0xf\n"
"s_nop 1\n"
"v_add_f32_dpp v244, v244, v244 row_half_mirror row_mask:0xf bank_mask:0xf\n"
"s_nop 1\n"
"v_add_f32_dpp v244, v244, v244 row_mirror row_mask:0xf bank_mask:0xf\n"
"v_mov_b32 v245, v244\n"
"s_nop 1\n"
"v_permlane16_swap_b32 v244, v245\n"
"v_add_f32 v244, v244, v245\n"
"v_mul_f32 v244, 0x3acccccd, v244\n"
"v_pk_add_f32 v[200:201], v[200:201], v[244:245] op_sel_hi:[1,0] neg_lo:[0,1] neg_hi:[0,1]\n"
"v_pk_add_f32 v[202:203], v[202:203], v[244:245] op_sel_hi:[1,0] neg_lo:[0,1] neg_hi:[0,1]\n"
"v_pk_add_f32 v[204:205], v[204:205], v[244:245] op_sel_hi:[1,0] neg_lo:[0,1] neg_hi:[0,1]\n"
"v_pk_add_f32 v[206:207], v[206:207], v[244:245] op_sel_hi:[1,0] neg_lo:[0,1] neg_hi:[0,1]\n"
"v_pk_add_f32 v[208:209], v[208:209], v[244:245] op_sel_hi:[1,0] neg_lo:[0,1] neg_hi:[0,1]\n"
"v_pk_add_f32 v[210:211], v[210:211], v[244:245] op_sel_hi:[1,0] neg_lo:[0,1] neg_hi:[0,1]\n"
"v_pk_add_f32 v[212:213], v[212:213], v[244:245] op_sel_hi:[1,0] neg_lo:[0,1] neg_hi:[0,1]\n"
"v_pk_add_f32 v[214:215], v[214:215], v[244:245] op_sel_hi:[1,0] neg_lo:[0,1] neg_hi:[0,1]\n"
"v_pk_add_f32 v[216:217], v[216:217], v[244:245] op_sel_hi:[1,0] neg_lo:[0,1] neg_hi:[0,1]\n"
"v_pk_add_f32 v[218:219], v[218:219], v[244:245] op_sel_hi:[1,0] neg_lo:[0,1] neg_hi:[0,1]\n"
"v_pk_mul_f32 v[244:245], v[200:201], v[200:201]\n"
"v_pk_mul_f32 v[246:247], v[202:203], v[202:203]\n"
"v_pk_fma_f32 v[244:245], v[204:205], v[204:205], v[244:245]\n"
"v_pk_fma_f32 v[246:247], v[206:207], v[206:207], v[246:247]\n"
"v_pk_fma_f32 v[244:245], v[208:209], v[208:209], v[244:245]\n"
"v_pk_fma_f32 v[246:247], v[210:211], v[210:211], v[246:247]\n"
"v_pk_fma_f32 v[244:245], v[212:213], v[212:213], v[244:245]\n"
"v_pk_fma_f32 v[246:247], v[214:215], v[214:215], v[246:247]\n"
"v_pk_fma_f32 v[244:245], v[216:217], v[216:217], v[244:245]\n"
"v_pk_fma_f32 v[246:247], v[218:219], v[218:219], v[246:247]\n"
"v_pk_add_f32 v[244:245], v[244:245], v[246:247]\n"
"v_add_f32 v244, v244, v245\n"
"s_nop 1\n"
"v_add_f32_dpp v244, v244, v244 quad_perm:[1,0,3,2] row_mask:0xf bank_mask:0xf\n"
"s_nop 1\n"
"v_add_f32_dpp v244, v244, v244 quad_perm:[2,3,0,1] row_mask:0xf bank_mask:0xf\n"
"s_nop 1\n"
"v_add_f32_dpp v244, v244, v244 row_half_mirror row_mask:0xf bank_mask:0xf\n"
"s_nop 1\n"
"v_add_f32_dpp v244, v244, v244 row_mirror row_mask:0xf bank_mask:0xf\n"
"v_mov_b32 v245, v244\n"
"s_nop 1\n"
"v_permlane16_swap_b32 v244, v245\n"
"v_add_f32 v244, v244, v245\n"
"v_fma_f32 v244, v244, s84, v26\n"
"v_rsq_f32 v244, v244\n"
"s_nop 0\n"
"v_pk_mul_f32 v[200:201], v[200:201], v[244:245] op_sel_hi:[1,0]\n"
"v_pk_mul_f32 v[202:203], v[202:203], v[244:245] op_sel_hi:[1,0]\n"
"v_pk_mul_f32 v[204:205], v[204:205], v[244:245] op_sel_hi:[1,0]\n"
"v_pk_mul_f32 v[206:207], v[206:207], v[244:245] op_sel_hi:[1,0]\n"
"v_pk_mul_f32 v[208:209], v[208:209], v[244:245] op_sel_hi:[1,0]\n"
"v_pk_mul_f32 v[210:211], v[210:211], v[244:245] op_sel_hi:[1,0]\n"
"v_pk_mul_f32 v[212:213], v[212:213], v[244:245] op_sel_hi:[1,0]\n"
"v_pk_mul_f32 v[214:215], v[214:215], v[244:245] op_sel_hi:[1,0]\n"
"v_pk_mul_f32 v[216:217], v[216:217], v[244:245] op_sel_hi:[1,0]\n"
"v_pk_mul_f32 v[218:219], v[218:219], v[244:245] op_sel_hi:[1,0]\n"
"v_cvt_pk_bf16_f32 v232, v200, v201\n"
"v_cvt_pk_bf16_f32 v233, v202, v203\n"
"v_cvt_pk_bf16_f32 v234, v204, v205\n"
"v_cvt_pk_bf16_f32 v235, v206, v207\n"
"v_cvt_pk_bf16_f32 v236, v208, v209\n"
"v_cvt_pk_bf16_f32 v237, v210, v211\n"
"v_cvt_pk_bf16_f32 v238, v212, v213\n"
"v_cvt_pk_bf16_f32 v239, v214, v215\n"
"v_cvt_pk_bf16_f32 v240, v216, v217\n"
"v_cvt_pk_bf16_f32 v241, v218, v219\n"
"v_mov_b64 v[242:243], v[240:241]\n"
"v_permlane16_swap_b32 v232, v234\n"
"v_permlane16_swap_b32 v233, v235\n"
"buffer_store_dwordx4 v[232:235], v19, s[12:15], s47 offen offset:0 sc1\n"
"v_permlane16_swap_b32 v236, v238\n"
"v_permlane16_swap_b32 v237, v239\n"
"s_add_u32 s40, s47, 32768\n"
"buffer_store_dwordx4 v[236:239], v19, s[12:15], s40 offen sc1\n"
"v_permlane16_swap_b32 v240, v242\n"
"v_permlane16_swap_b32 v241, v243\n"
"s_add_u32 s40, s47, 65536\n"
"buffer_store_dwordx4 v[240:243], v20, s[12:15], s40 offen sc1\n"
"s_waitcnt vmcnt(0)\n"
"buffer_store_dword v5, off, s[12:15], s27 sc1\n"
"s_cmp_lt_u32 s24, 64\n"
"s_cbranch_scc1 R2_loop_%=\n"
"s_waitcnt vmcnt(0) lgkmcnt(0)\n"
"s_endpgm\n"
"R3_loop_%=:\n"
"s_lshl_b32 s26, s24, 1\n"
"s_add_u32 s44, s26, 15\n"
"s_and_b32 s44, s44, 31\n"
"s_lshl_b32 s44, s44, 22\n"
"s_mov_b32 s26, s44\n"
"s_add_u32 s45, s26, s74\n"
"s_add_u32 s45, s45, 12582912\n"
"s_add_u32 s46, s44, s75\n"
"s_add_u32 s46, s46, 12320768\n"
"s_add_u32 s47, s44, s75\n"
"s_add_u32 s47, s47, 12500992\n"
"s_mul_i32 s48, s24, 40960\n"
"R3_w_%=:\n"
"buffer_load_dwordx4 v[4:7], v1, s[12:15], 0 offen sc1\n"
"s_waitcnt vmcnt(0)\n"
"v_min3_u32 v4, v4, v5, v6\n"
"v_min_u32 v4, v4, v7\n"
"v_cmp_ge_u32 vcc, s24, v4\n"
"s_nop 0\n"
"s_and_b32 s40, vcc_lo, 0x3fff\n"
"s_cbranch_scc0 R3_wg_%=\n"
"s_sleep 4\n"
"s_add_u32 s23, s23, 1\n"
"s_cmp_lt_u32 s23, 400000\n"
"s_cbranch_scc1 R3_w_%=\n"
"R3_wg_%=:\n"
"s_add_u32 s40, s45, 0\n"
"buffer_load_dwordx4 v[32:35], v16, s[12:15], s40 offen offset:0 sc1\n"
"buffer_load_dwordx4 v[36:39], v16, s[12:15], s40 offen offset:512 sc1\n"
"buffer_load_dwordx4 v[40:43], v16, s[12:15], s40 offen offset:1024 sc1\n"
"buffer_load_dwordx4 v[44:47], v16, s[12:15], s40 offen offset:1536 sc1\n"
"buffer_load_dwordx4 v[48:51], v16, s[12:15], s40 offen offset:2048 sc1\n"
"s_add_u32 s40, s45, 196608\n"
"buffer_load_dwordx4 v[52:55], v16, s[12:15], s40 offen offset:0 sc1\n"
"buffer_load_dwordx4 v[56:59], v16, s[12:15], s40 offen offset:512 sc1\n"
"buffer_load_dwordx4 v[60:63], v16, s[12:15], s40 offen offset:1024 sc1\n"
"buffer_load_dwordx4 v[64:67], v16, s[12:15], s40 offen offset:1536 sc1\n"
"buffer_load_dwordx4 v[68:71], v16, s[12:15], s40 offen offset:2048 sc1\n"
"s_add_u32 s40, s45, 393216\n"
"buffer_load_dwordx4 v[72:75], v16, s[12:15], s40 offen offset:0 sc1\n"
"buffer_load_dwordx4 v[76:79], v16, s[12:15], s40 offen offset:512 sc1\n"
"buffer_load_dwordx4 v[80:83], v16, s[12:15], s40 offen offset:1024 sc1\n"
"buffer_load_dwordx4 v[84:87], v16, s[12:15], s40 offen offset:1536 sc1\n"
"buffer_load_dwordx4 v[88:91], v16, s[12:15], s40 offen offset:2048 sc1\n"
"s_add_u32 s40, s45, 589824\n"
"buffer_load_dwordx4 v[92:95], v16, s[12:15], s40 offen offset:0 sc1\n"
"buffer_load_dwordx4 v[96:99], v16, s[12:15], s40 offen offset:512 sc1\n"
"buffer_load_dwordx4 v[100:103], v16, s[12:15], s40 offen offset:1024 sc1\n"
"buffer_load_dwordx4 v[104:107], v16, s[12:15], s40 offen offset:1536 sc1\n"
"buffer_load_dwordx4 v[108:111], v16, s[12:15], s40 offen offset:2048 sc1\n"
"s_add_u32 s40, s45, 786432\n"
"buffer_load_dwordx4 v[112:115], v16, s[12:15], s40 offen offset:0 sc1\n"
"buffer_load_dwordx4 v[116:119], v16, s[12:15], s40 offen offset:512 sc1\n"
"buffer_load_dwordx4 v[120:123], v16, s[12:15], s40 offen offset:1024 sc1\n"
"buffer_load_dwordx4 v[124:127], v16, s[12:15], s40 offen offset:1536 sc1\n"
"buffer_load_dwordx4 v[128:131], v16, s[12:15], s40 offen offset:2048 sc1\n"
"s_add_u32 s40, s45, 983040\n"
"buffer_load_dwordx4 v[132:135], v16, s[12:15], s40 offen offset:0 sc1\n"
"buffer_load_dwordx4 v[136:139], v16, s[12:15], s40 offen offset:512 sc1\n"
"buffer_load_dwordx4 v[140:143], v16, s[12:15], s40 offen offset:1024 sc1\n"
"buffer_load_dwordx4 v[144:147], v16, s[12:15], s40 offen offset:1536 sc1\n"
"buffer_load_dwordx4 v[148:151], v16, s[12:15], s40 offen offset:2048 sc1\n"
"s_add_u32 s40, s45, 1179648\n"
"buffer_load_dwordx4 v[152:155], v16, s[12:15], s40 offen offset:0 sc1\n"
"buffer_load_dwordx4 v[156:159], v16, s[12:15], s40 offen offset:512 sc1\n"
"buffer_load_dwordx4 v[160:163], v16, s[12:15], s40 offen offset:1024 sc1\n"
"buffer_load_dwordx4 v[164:167], v16, s[12:15], s40 offen offset:1536 sc1\n"
"buffer_load_dwordx4 v[168:171], v16, s[12:15], s40 offen offset:2048 sc1\n"
"s_add_u32 s40, s45, 1376256\n"
"buffer_load_dwordx4 v[172:175], v17, s[12:15], s40 offen offset:0 sc1\n"
"buffer_load_dwordx4 v[176:179], v17, s[12:15], s40 offen offset:512 sc1\n"
"buffer_load_dwordx4 v[180:183], v17, s[12:15], s40 offen offset:1024 sc1\n"
"buffer_load_dwordx4 v[184:187], v17, s[12:15], s40 offen offset:1536 sc1\n"
"buffer_load_dwordx4 v[188:191], v17, s[12:15], s40 offen offset:2048 sc1\n"
"s_waitcnt vmcnt(39)\n"
"v_mfma_f32_16x16x32_bf16 v[200:203], v[8:11], v[32:35], 0\n"
"s_waitcnt vmcnt(38)\n"
"v_mfma_f32_16x16x32_bf16 v[204:207], v[8:11], v[36:39], 0\n"
"s_waitcnt vmcnt(37)\n"
"v_mfma_f32_16x16x32_bf16 v[208:211], v[8:11], v[40:43], 0\n"
"s_waitcnt vmcnt(36)\n"
"v_mfma_f32_16x16x32_bf16 v[212:215], v[8:11], v[44:47], 0\n"
"s_waitcnt vmcnt(35)\n"
"v_mfma_f32_16x16x32_bf16 v[216:219], v[8:11], v[48:51], 0\n"
"s_waitcnt vmcnt(34)\n"
"v_mfma_f32_16x16x32_bf16 v[200:203], v[8:11], v[52:55], v[200:203]\n"
"s_waitcnt vmcnt(33)\n"
"v_mfma_f32_16x16x32_bf16 v[204:207], v[8:11], v[56:59], v[204:207]\n"
"s_waitcnt vmcnt(32)\n"
"v_mfma_f32_16x16x32_bf16 v[208:211], v[8:11], v[60:63], v[208:211]\n"
"s_waitcnt vmcnt(31)\n"
"v_mfma_f32_16x16x32_bf16 v[212:215], v[8:11], v[64:67], v[212:215]\n"
"s_waitcnt vmcnt(30)\n"
"v_mfma_f32_16x16x32_bf16 v[216:219], v[8:11], v[68:71], v[216:219]\n"
"s_waitcnt vmcnt(29)\n"
"v_mfma_f32_16x16x32_bf16 v[200:203], v[8:11], v[72:75], v[200:203]\n"
"s_waitcnt vmcnt(28)\n"
"v_mfma_f32_16x16x32_bf16 v[204:207], v[8:11], v[76:79], v[204:207]\n"
"s_waitcnt vmcnt(27)\n"
"v_mfma_f32_16x16x32_bf16 v[208:211], v[8:11], v[80:83], v[208:211]\n"
"s_waitcnt vmcnt(26)\n"
"v_mfma_f32_16x16x32_bf16 v[212:215], v[8:11], v[84:87], v[212:215]\n"
"s_waitcnt vmcnt(25)\n"
"v_mfma_f32_16x16x32_bf16 v[216:219], v[8:11], v[88:91], v[216:219]\n"
"s_waitcnt vmcnt(24)\n"
"v_mfma_f32_16x16x32_bf16 v[200:203], v[8:11], v[92:95], v[200:203]\n"
"s_waitcnt vmcnt(23)\n"
"v_mfma_f32_16x16x32_bf16 v[204:207], v[8:11], v[96:99], v[204:207]\n"
"s_waitcnt vmcnt(22)\n"
"v_mfma_f32_16x16x32_bf16 v[208:211], v[8:11], v[100:103], v[208:211]\n"
"s_waitcnt vmcnt(21)\n"
"v_mfma_f32_16x16x32_bf16 v[212:215], v[8:11], v[104:107], v[212:215]\n"
"s_waitcnt vmcnt(20)\n"
"v_mfma_f32_16x16x32_bf16 v[216:219], v[8:11], v[108:111], v[216:219]\n"
"s_waitcnt vmcnt(19)\n"
"v_mfma_f32_16x16x32_bf16 v[200:203], v[8:11], v[112:115], v[200:203]\n"
"s_waitcnt vmcnt(18)\n"
"v_mfma_f32_16x16x32_bf16 v[204:207], v[8:11], v[116:119], v[204:207]\n"
"s_waitcnt vmcnt(17)\n"
"v_mfma_f32_16x16x32_bf16 v[208:211], v[8:11], v[120:123], v[208:211]\n"
"s_waitcnt vmcnt(16)\n"
"v_mfma_f32_16x16x32_bf16 v[212:215], v[8:11], v[124:127], v[212:215]\n"
"s_waitcnt vmcnt(15)\n"
"v_mfma_f32_16x16x32_bf16 v[216:219], v[8:11], v[128:131], v[216:219]\n"
"s_waitcnt vmcnt(14)\n"
"v_mfma_f32_16x16x32_bf16 v[200:203], v[8:11], v[132:135], v[200:203]\n"
"s_waitcnt vmcnt(13)\n"
"v_mfma_f32_16x16x32_bf16 v[204:207], v[8:11], v[136:139], v[204:207]\n"
"s_waitcnt vmcnt(12)\n"
"v_mfma_f32_16x16x32_bf16 v[208:211], v[8:11], v[140:143], v[208:211]\n"
"s_waitcnt vmcnt(11)\n"
"v_mfma_f32_16x16x32_bf16 v[212:215], v[8:11], v[144:147], v[212:215]\n"
"s_waitcnt vmcnt(10)\n"
"v_mfma_f32_16x16x32_bf16 v[216:219], v[8:11], v[148:151], v[216:219]\n"
"s_waitcnt vmcnt(9)\n"
"v_mfma_f32_16x16x32_bf16 v[200:203], v[8:11], v[152:155], v[200:203]\n"
"s_waitcnt vmcnt(8)\n"
"v_mfma_f32_16x16x32_bf16 v[204:207], v[8:11], v[156:159], v[204:207]\n"
"s_waitcnt vmcnt(7)\n"
"v_mfma_f32_16x16x32_bf16 v[208:211], v[8:11], v[160:163], v[208:211]\n"
"s_waitcnt vmcnt(6)\n"
"v_mfma_f32_16x16x32_bf16 v[212:215], v[8:11], v[164:167], v[212:215]\n"
"s_waitcnt vmcnt(5)\n"
"v_mfma_f32_16x16x32_bf16 v[216:219], v[8:11], v[168:171], v[216:219]\n"
"s_waitcnt vmcnt(4)\n"
"v_mfma_f32_16x16x32_bf16 v[200:203], v[8:11], v[172:175], v[200:203]\n"
"s_waitcnt vmcnt(3)\n"
"v_mfma_f32_16x16x32_bf16 v[204:207], v[8:11], v[176:179], v[204:207]\n"
"s_waitcnt vmcnt(2)\n"
"v_mfma_f32_16x16x32_bf16 v[208:211], v[8:11], v[180:183], v[208:211]\n"
"s_waitcnt vmcnt(1)\n"
"v_mfma_f32_16x16x32_bf16 v[212:215], v[8:11], v[184:187], v[212:215]\n"
"s_waitcnt vmcnt(0)\n"
"v_mfma_f32_16x16x32_bf16 v[216:219], v[8:11], v[188:191], v[216:219]\n"
"s_add_u32 s24, s24, 1\n"
"v_mov_b32 v5, s24\n"
"v_cvt_pk_bf16_f32 v220, v200, v201\n"
"v_cvt_pk_bf16_f32 v221, v202, v203\n"
"v_cvt_pk_bf16_f32 v222, v204, v205\n"
"v_cvt_pk_bf16_f32 v223, v206, v207\n"
"v_cvt_pk_bf16_f32 v224, v208, v209\n"
"v_cvt_pk_bf16_f32 v225, v210, v211\n"
"v_cvt_pk_bf16_f32 v226, v212, v213\n"
"v_cvt_pk_bf16_f32 v227, v214, v215\n"
"v_cvt_pk_bf16_f32 v228, v216, v217\n"
"v_cvt_pk_bf16_f32 v229, v218, v219\n"
"v_mov_b64 v[230:231], v[228:229]\n"
"v_permlane16_swap_b32 v220, v222\n"
"v_permlane16_swap_b32 v221, v223\n"
"buffer_store_dwordx4 v[220:223], v23, s[28:31], s48 offen offset:0\n"
"v_permlane16_swap_b32 v224, v226\n"
"v_permlane16_swap_b32 v225, v227\n"
"buffer_store_dwordx4 v[224:227], v23, s[28:31], s48 offen offset:512\n"
"v_permlane16_swap_b32 v228, v230\n"
"v_permlane16_swap_b32 v229, v231\n"
"buffer_store_dwordx4 v[228:231], v24, s[28:31], s48 offen offset:1024\n"
"ds_write_b32 v25, v5\n"
"s_cmp_lt_u32 s24, 64\n"
"s_cbranch_scc1 R3_loop_%=\n"
"s_waitcnt vmcnt(0) lgkmcnt(0)\n"
"s_endpgm\n"
"R4_loop_%=:\n"
"s_mul_i32 s47, s24, 81920\n"
"s_add_u32 s47, s47, 144703488\n"
"s_mul_i32 s48, s24, 40960\n"
"buffer_load_dwordx4 v[32:35], v18, s[28:31], s48 offen offset:0\n"
"buffer_load_dwordx4 v[36:39], v18, s[28:31], s48 offen offset:256\n"
"buffer_load_dwordx4 v[40:43], v18, s[28:31], s48 offen offset:512\n"
"buffer_load_dwordx4 v[44:47], v18, s[28:31], s48 offen offset:768\n"
"buffer_load_dwordx4 v[48:51], v18, s[28:31], s48 offen offset:1024\n"
"s_waitcnt vmcnt(4)\n"
"v_mfma_f32_16x16x32_bf16 v[200:203], v[8:11], v[32:35], 0\n"
"s_waitcnt vmcnt(3)\n"
"v_mfma_f32_16x16x32_bf16 v[204:207], v[8:11], v[36:39], 0\n"
"s_waitcnt vmcnt(2)\n"
"v_mfma_f32_16x16x32_bf16 v[208:211], v[8:11], v[40:43], 0\n"
"s_waitcnt vmcnt(1)\n"
"v_mfma_f32_16x16x32_bf16 v[212:215], v[8:11], v[44:47], 0\n"
"s_waitcnt vmcnt(0)\n"
"v_mfma_f32_16x16x32_bf16 v[216:219], v[8:11], v[48:51], 0\n"
"s_add_u32 s24, s24, 1\n"
"v_mov_b32 v5, s24\n"
"v_cvt_pk_bf16_f32 v220, v200, v201\n"
"v_cvt_pk_bf16_f32 v221, v202, v203\n"
"v_cvt_pk_bf16_f32 v222, v204, v205\n"
"v_cvt_pk_bf16_f32 v223, v206, v207\n"
"v_cvt_pk_bf16_f32 v224, v208, v209\n"
"v_cvt_pk_bf16_f32 v225, v210, v211\n"
"v_cvt_pk_bf16_f32 v226, v212, v213\n"
"v_cvt_pk_bf16_f32 v227, v214, v215\n"
"v_cvt_pk_bf16_f32 v228, v216, v217\n"
"v_cvt_pk_bf16_f32 v229, v218, v219\n"
"v_lshlrev_b32 v200, 16, v220\n"
"v_and_b32 v201, 0xffff0000, v220\n"
"v_lshlrev_b32 v202, 16, v221\n"
"v_and_b32 v203, 0xffff0000, v221\n"
"v_lshlrev_b32 v204, 16, v222\n"
"v_and_b32 v205, 0xffff0000, v222\n"
"v_lshlrev_b32 v206, 16, v223\n"
"v_and_b32 v207, 0xffff0000, v223\n"
"v_lshlrev_b32 v208, 16, v224\n"
"v_and_b32 v209, 0xffff0000, v224\n"
"v_lshlrev_b32 v210, 16, v225\n"
"v_and_b32 v211, 0xffff0000, v225\n"
"v_lshlrev_b32 v212, 16, v226\n"
"v_and_b32 v213, 0xffff0000, v226\n"
"v_lshlrev_b32 v214, 16, v227\n"
"v_and_b32 v215, 0xffff0000, v227\n"
"v_lshlrev_b32 v216, 16, v228\n"
"v_and_b32 v217, 0xffff0000, v228\n"
"v_lshlrev_b32 v218, 16, v229\n"
"v_and_b32 v219, 0xffff0000, v229\n"
"v_pk_add_f32 v[244:245], v[200:201], v[202:203]\n"
"v_pk_add_f32 v[246:247], v[204:205], v[206:207]\n"
"v_pk_add_f32 v[244:245], v[244:245], v[208:209]\n"
"v_pk_add_f32 v[246:247], v[246:247], v[210:211]\n"
"v_pk_add_f32 v[244:245], v[244:245], v[212:213]\n"
"v_pk_add_f32 v[246:247], v[246:247], v[214:215]\n"
"v_pk_add_f32 v[244:245], v[244:245], v[216:217]\n"
"v_pk_add_f32 v[246:247], v[246:247], v[218:219]\n"
"v_pk_add_f32 v[244:245], v[244:245], v[246:247]\n"
"v_add_f32 v244, v244, v245\n"
"s_nop 1\n"
"v_add_f32_dpp v244, v244, v244 quad_perm:[1,0,3,2] row_mask:0xf bank_mask:0xf\n"
"s_nop 1\n"
"v_add_f32_dpp v244, v244, v244 quad_perm:[2,3,0,1] row_mask:0xf bank_mask:0xf\n"
"s_nop 1\n"
"v_add_f32_dpp v244, v244, v244 row_half_mirror row_mask:0xf bank_mask:0xf\n"
"s_nop 1\n"
"v_add_f32_dpp v244, v244, v244 row_mirror row_mask:0xf bank_mask:0xf\n"
"v_mov_b32 v245, v244\n"
"s_nop 1\n"
"v_permlane16_swap_b32 v244, v245\n"
"v_add_f32 v244, v244, v245\n"
"v_mul_f32 v244, 0x3acccccd, v244\n"
"v_pk_add_f32 v[200:201], v[200:201], v[244:245] op_sel_hi:[1,0] neg_lo:[0,1] neg_hi:[0,1]\n"
"v_pk_add_f32 v[202:203], v[202:203], v[244:245] op_sel_hi:[1,0] neg_lo:[0,1] neg_hi:[0,1]\n"
"v_pk_add_f32 v[204:205], v[204:205], v[244:245] op_sel_hi:[1,0] neg_lo:[0,1] neg_hi:[0,1]\n"
"v_pk_add_f32 v[206:207], v[206:207], v[244:245] op_sel_hi:[1,0] neg_lo:[0,1] neg_hi:[0,1]\n"
"v_pk_add_f32 v[208:209], v[208:209], v[244:245] op_sel_hi:[1,0] neg_lo:[0,1] neg_hi:[0,1]\n"
"v_pk_add_f32 v[210:211], v[210:211], v[244:245] op_sel_hi:[1,0] neg_lo:[0,1] neg_hi:[0,1]\n"
"v_pk_add_f32 v[212:213], v[212:213], v[244:245] op_sel_hi:[1,0] neg_lo:[0,1] neg_hi:[0,1]\n"
"v_pk_add_f32 v[214:215], v[214:215], v[244:245] op_sel_hi:[1,0] neg_lo:[0,1] neg_hi:[0,1]\n"
"v_pk_add_f32 v[216:217], v[216:217], v[244:245] op_sel_hi:[1,0] neg_lo:[0,1] neg_hi:[0,1]\n"
"v_pk_add_f32 v[218:219], v[218:219], v[244:245] op_sel_hi:[1,0] neg_lo:[0,1] neg_hi:[0,1]\n"
"v_pk_mul_f32 v[244:245], v[200:201], v[200:201]\n"
"v_pk_mul_f32 v[246:247], v[202:203], v[202:203]\n"
"v_pk_fma_f32 v[244:245], v[204:205], v[204:205], v[244:245]\n"
"v_pk_fma_f32 v[246:247], v[206:207], v[206:207], v[246:247]\n"
"v_pk_fma_f32 v[244:245], v[208:209], v[208:209], v[244:245]\n"
"v_pk_fma_f32 v[246:247], v[210:211], v[210:211], v[246:247]\n"
"v_pk_fma_f32 v[244:245], v[212:213], v[212:213], v[244:245]\n"
"v_pk_fma_f32 v[246:247], v[214:215], v[214:215], v[246:247]\n"
"v_pk_fma_f32 v[244:245], v[216:217], v[216:217], v[244:245]\n"
"v_pk_fma_f32 v[246:247], v[218:219], v[218:219], v[246:247]\n"
"v_pk_add_f32 v[244:245], v[244:245], v[246:247]\n"
"v_add_f32 v244, v244, v245\n"
"s_nop 1\n"
"v_add_f32_dpp v244, v244, v244 quad_perm:[1,0,3,2] row_mask:0xf bank_mask:0xf\n"
"s_nop 1\n"
"v_add_f32_dpp v244, v244, v244 quad_perm:[2,3,0,1] row_mask:0xf bank_mask:0xf\n"
"s_nop 1\n"
"v_add_f32_dpp v244, v244, v244 row_half_mirror row_mask:0xf bank_mask:0xf\n"
"s_nop 1\n"
"v_add_f32_dpp v244, v244, v244 row_mirror row_mask:0xf bank_mask:0xf\n"
"v_mov_b32 v245, v244\n"
"s_nop 1\n"
"v_permlane16_swap_b32 v244, v245\n"
"v_add_f32 v244, v244, v245\n"
"v_fma_f32 v244, v244, s84, v26\n"
"v_rsq_f32 v244, v244\n"
"s_nop 0\n"
"v_pk_mul_f32 v[200:201], v[200:201], v[244:245] op_sel_hi:[1,0]\n"
"v_pk_mul_f32 v[202:203], v[202:203], v[244:245] op_sel_hi:[1,0]\n"
"v_pk_mul_f32 v[204:205], v[204:205], v[244:245] op_sel_hi:[1,0]\n"
"v_pk_mul_f32 v[206:207], v[206:207], v[244:245] op_sel_hi:[1,0]\n"
"v_pk_mul_f32 v[208:209], v[208:209], v[244:245] op_sel_hi:[1,0]\n"
"v_pk_mul_f32 v[210:211], v[210:211], v[244:245] op_sel_hi:[1,0]\n"
"v_pk_mul_f32 v[212:213], v[212:213], v[244:245] op_sel_hi:[1,0]\n"
"v_pk_mul_f32 v[214:215], v[214:215], v[244:245] op_sel_hi:[1,0]\n"
"v_pk_mul_f32 v[216:217], v[216:217], v[244:245] op_sel_hi:[1,0]\n"
"v_pk_mul_f32 v[218:219], v[218:219], v[244:245] op_sel_hi:[1,0]\n"
"v_cvt_pk_bf16_f32 v232, v200, v201\n"
"v_cvt_pk_bf16_f32 v233, v202, v203\n"
"v_cvt_pk_bf16_f32 v234, v204, v205\n"
"v_cvt_pk_bf16_f32 v235, v206, v207\n"
"v_cvt_pk_bf16_f32 v236, v208, v209\n"
"v_cvt_pk_bf16_f32 v237, v210, v211\n"
"v_cvt_pk_bf16_f32 v238, v212, v213\n"
"v_cvt_pk_bf16_f32 v239, v214, v215\n"
"v_cvt_pk_bf16_f32 v240, v216, v217\n"
"v_cvt_pk_bf16_f32 v241, v218, v219\n"
"v_mov_b64 v[242:243], v[240:241]\n"
"v_permlane16_swap_b32 v232, v234\n"
"v_permlane16_swap_b32 v233, v235\n"
"buffer_store_dwordx4 v[232:235], v19, s[12:15], s47 offen offset:0 sc1\n"
"v_permlane16_swap_b32 v236, v238\n"
"v_permlane16_swap_b32 v237, v239\n"
"s_add_u32 s40, s47, 32768\n"
"buffer_store_dwordx4 v[236:239], v19, s[12:15], s40 offen sc1\n"
"v_permlane16_swap_b32 v240, v242\n"
"v_permlane16_swap_b32 v241, v243\n"
"s_add_u32 s40, s47, 65536\n"
"buffer_store_dwordx4 v[240:243], v20, s[12:15], s40 offen sc1\n"
"s_waitcnt vmcnt(0)\n"
"buffer_store_dword v5, off, s[12:15], s27 sc1\n"
"s_cmp_lt_u32 s24, 64\n"
"s_cbranch_scc1 R4_loop_%=\n"
"s_waitcnt vmcnt(0) lgkmcnt(0)\n"
"s_endpgm\n"
"s_endpgm\n"

        :: "s"(__builtin_amdgcn_kernarg_segment_ptr()), "s"(id), "s"(w), "s"(role), "s"((unsigned)(size_t)(__attribute__((address_space(3))) char*)smem)
        : "v0", "v1", "v2", "v3", "v4", "v5", "v6", "v7", "v8", "v9", "v10", "v11", "v12", "v13", "v14", "v15", "v16", "v17", "v18", "v19", "v20", "v21", "v22", "v23", "v24", "v25", "v26", "v27", "v28", "v29", "v30", "v31", "v32", "v33", "v34", "v35", "v36", "v37", "v38", "v39", "v40", "v41", "v42", "v43", "v44", "v45", "v46", "v47", "v48", "v49", "v50", "v51", "v52", "v53", "v54", "v55", "v56", "v57", "v58", "v59", "v60", "v61", "v62", "v63", "v64", "v65", "v66", "v67", "v68", "v69", "v70", "v71", "v72", "v73", "v74", "v75", "v76", "v77", "v78", "v79", "v80", "v81", "v82", "v83", "v84", "v85", "v86", "v87", "v88", "v89", "v90", "v91", "v92", "v93", "v94", "v95", "v96", "v97", "v98", "v99", "v100", "v101", "v102", "v103", "v104", "v105", "v106", "v107", "v108", "v109", "v110", "v111", "v112", "v113", "v114", "v115", "v116", "v117", "v118", "v119", "v120", "v121", "v122", "v123", "v124", "v125", "v126", "v127", "v128", "v129", "v130", "v131", "v132", "v133", "v134", "v135", "v136", "v137", "v138", "v139", "v140", "v141", "v142", "v143", "v144", "v145", "v146", "v147", "v148", "v149", "v150", "v151", "v152", "v153", "v154", "v155", "v156", "v157", "v158", "v159", "v160", "v161", "v162", "v163", "v164", "v165", "v166", "v167", "v168", "v169", "v170", "v171", "v172", "v173", "v174", "v175", "v176", "v177", "v178", "v179", "v180", "v181", "v182", "v183", "v184", "v185", "v186", "v187", "v188", "v189", "v190", "v191", "v192", "v193", "v194", "v195", "v196", "v197", "v198", "v199", "v200", "v201", "v202", "v203", "v204", "v205", "v206", "v207", "v208", "v209", "v210", "v211", "v212", "v213", "v214", "v215", "v216", "v217", "v218", "v219", "v220", "v221", "v222", "v223", "v224", "v225", "v226", "v227", "v228", "v229", "v230", "v231", "v232", "v233", "v234", "v235", "v236", "v237", "v238", "v239", "v240", "v241", "v242", "v243", "v244", "v245", "v246", "v247", "v248", "v249", "v250", "v251", "v252", "v253", "v254", "v255", "a0", "a1", "a2", "a3", "a4", "a5", "a6", "a7", "a8", "a9", "a10", "a11", "a12", "a13", "a14", "a15", "a16", "a17", "a18", "a19", "a20", "a21", "a22", "a23", "a24", "a25", "a26", "a27", "a28", "a29", "a30", "a31", "a32", "a33", "a34", "a35", "a36", "a37", "a38", "a39", "a40", "a41", "a42", "a43", "a44", "a45", "a46", "a47", "a48", "a49", "a50", "a51", "a52", "a53", "a54", "a55", "a56", "a57", "a58", "a59", "a60", "a61", "a62", "a63", "a64", "a65", "a66", "a67", "a68", "a69", "a70", "a71", "a72", "a73", "a74", "a75", "a76", "a77", "a78", "a79", "a80", "a81", "a82", "a83", "a84", "a85", "a86", "a87", "a88", "a89", "a90", "a91", "a92", "a93", "a94", "a95", "a96", "a97", "a98", "a99", "a100", "a101", "a102", "a103", "a104", "a105", "a106", "a107", "a108", "a109", "a110", "a111", "a112", "a113", "a114", "a115", "a116", "a117", "a118", "a119", "a120", "a121", "a122", "a123", "a124", "a125", "a126", "a127", "a128", "a129", "a130", "a131", "a132", "a133", "a134", "a135", "a136", "a137", "a138", "a139", "a140", "a141", "a142", "a143", "a144", "a145", "a146", "a147", "a148", "a149", "a150", "a151", "a152", "a153", "a154", "a155", "a156", "a157", "a158", "a159", "a160", "a161", "a162", "a163", "a164", "a165", "a166", "a167", "a168", "a169", "a170", "a171", "a172", "a173", "a174", "a175", "a176", "a177", "a178", "a179", "a180", "a181", "a182", "a183", "a184", "a185", "a186", "a187", "a188", "a189", "a190", "a191", "a192", "a193", "a194", "a195", "a196", "a197", "a198", "a199", "a200", "a201", "a202", "a203", "a204", "a205", "a206", "a207", "a208", "a209", "a210", "a211", "a212", "a213", "a214", "a215", "a216", "a217", "a218", "a219", "a220", "a221", "a222", "a223", "a224", "a225", "a226", "a227", "a228", "a229", "a230", "a231", "a232", "a233", "a234", "a235", "a236", "a237", "a238", "a239", "a240", "a241", "a242", "a243", "a244", "a245", "a246", "a247", "a248", "a249", "a250", "a251", "a252", "a253", "a254", "a255", "s12", "s13", "s14", "s15", "s16", "s17", "s18", "s19", "s20", "s21", "s22", "s23", "s24", "s25", "s26", "s27", "s28", "s29", "s30", "s31", "s33", "s34", "s35", "s36", "s37", "s38", "s39", "s40", "s41", "s42", "s43", "s44", "s45", "s46", "s47", "s48", "s49", "s50", "s51", "s52", "s53", "s54", "s55", "s56", "s57", "s58", "s59", "s60", "s61", "s62", "s63", "s64", "s65", "s66", "s67", "s68", "s69", "s70", "s71", "s72", "s73", "s74", "s75", "s76", "s77", "s78", "s79", "s80", "s81", "s82", "s83", "s84", "s85", "s86", "s87", "s88", "s89", "s90", "s91", "s92", "s93", "s94", "s95", "s96", "s97", "s98", "s99", "vcc", "scc", "memory");
}

extern "C" void kernel_launch(void* const* d_in, const int* in_sizes, int n_in, void* d_out, int out_size, void* d_ws, size_t ws_size, hipStream_t stream) {
    Params p;
    p.x = d_in[0]; p.mem0 = d_in[1]; p.Wq = d_in[2]; p.Wk = d_in[3]; p.Wv = d_in[4]; p.Wo = d_in[5]; p.w1 = d_in[8]; p.w2 = d_in[9]; p.out = d_out;
    p.ws = (void*)(((uintptr_t)d_ws + 0x1FFFFF) & ~(uintptr_t)0x1FFFFF);
    (void)hipMemsetAsync(p.ws, 0, FLAG_BYTES, stream);
    hipLaunchKernelGGL(mega, dim3(256), dim3(256), 0, stream, p);
}
```
